```python
import torch

def _softmax(x, axis=-1):
    return torch.softmax(x, dim=axis)
def _gelu(x):
    return torch.nn.functional.gelu(x, approximate="tanh")
def _silu(x):
    return torch.nn.functional.silu(x)
def _sigmoid(x):
    return torch.sigmoid(x)
def _layernorm(x, axis=-1, eps=1e-5):
    m = x.mean(dim=axis, keepdim=True)
    v = x.var(dim=axis, keepdim=True, unbiased=False)
    return (x - m) / torch.sqrt(v + eps)
def _scatter_add(tgt, idx, src, axis=0):
    return tgt.clone().index_add_(axis, idx, src)
def _segment_sum(src, seg, n):
    out = torch.zeros((n,) + src.shape[1:], dtype=src.dtype)
    return out.index_add_(0, seg, src)
def _pad(x, pw):
    flat = tuple(v for pair in reversed(pw) for v in pair)
    return torch.nn.functional.pad(x, flat)

kernel_name = 'interleave_layers__spectral_gfnet__glu_gated_reglu__prompt'

def setup_inputs():

    _w = (lambda a: (lambda t: t.to(torch.bfloat16) if t.is_floating_point() else t)(torch.as_tensor(a)))
    def _A_setup():
        B, S, D = 1, 2048, 512
        Df = D // 2 + 1
        return {
            "x": _w((lambda *s: torch.randn(*s, dtype=torch.bfloat16) * 0.05)(B, S, D)),
            "w_r": _w((lambda *s: torch.randn(*s, dtype=torch.bfloat16) * 0.4)(S, Df) * 0.02),
            "w_i": _w((lambda *s: torch.randn(*s, dtype=torch.bfloat16) * 0.4)(S, Df) * 0.02),
        }
    _A_in = _A_setup()

    def _B_setup():
        B, S, D, F = 1, 2048, 512, 1792
        return {
            "x": _w((lambda *s: torch.randn(*s, dtype=torch.bfloat16) * 0.05)(B, S, D)),
            "W_gate": _w((lambda *s: torch.randn(*s, dtype=torch.bfloat16) * 0.2)(D, F) * (2.0 / D) ** 0.5),
            "W_up": _w((lambda *s: torch.randn(*s, dtype=torch.bfloat16) * 0.2)(D, F) * (2.0 / D) ** 0.5),
            "W_down": _w((lambda *s: torch.randn(*s, dtype=torch.bfloat16) * 0.2)(F, D) * (1.0 / F) ** 0.5),
        }
    _B_in = _B_setup()

    return {
        "A_x": _A_in["x"],
        "A_w_r": _A_in["w_r"],
        "A_w_i": _A_in["w_i"],
        "B_x": _B_in["x"],
        "B_W_gate": _B_in["W_gate"],
        "B_W_up": _B_in["W_up"],
        "B_W_down": _B_in["W_down"],
    }

def reference(A_x, A_w_r, A_w_i, B_x, B_W_gate, B_W_up, B_W_down):
    def _A_ref(x, w_r, w_i):
        B, S, D = x.shape
        X = (lambda x, axes=(-2,-1): torch.fft.rfft2(x.float(), dim=axes))(x, axes=(-2, -1))
        W = w_r + 1j * w_i
        X = X * W
        y = (lambda x, s, axes=(-2,-1): torch.fft.irfft2(x, s=s, dim=axes).to(torch.bfloat16))(X, (S, D), axes=(-2, -1))
        return y

    def _B_ref(x, W_gate, W_up, W_down):
        gate = torch.einsum("bsd,df->bsf", x, W_gate)
        up = torch.einsum("bsd,df->bsf", x, W_up)
        gate = gate * (lambda x, dt: x.to(dt))(gate > 0, gate.dtype)
        h = gate * up
        return torch.einsum("bsf,fd->bsd", h, W_down)

    x = A_x
    x = x + _A_ref(_layernorm(x, axis=-1), A_w_r, A_w_i)
    x = x + _B_ref(_layernorm(x, axis=-1), B_W_gate, B_W_up, B_W_down)
    x = x + _A_ref(_layernorm(x, axis=-1), A_w_r, A_w_i)
    x = x + _B_ref(_layernorm(x, axis=-1), B_W_gate, B_W_up, B_W_down)
    return x
```

```cpp
#include <hip/hip_runtime.h>
#include <hip/hip_bf16.h>

#define LAS __attribute__((address_space(3)))
#define DI __device__ __forceinline__
typedef unsigned short u16;
typedef unsigned v4u __attribute__((ext_vector_type(4)));
typedef unsigned v2u __attribute__((ext_vector_type(2)));
typedef float f32x2 __attribute__((ext_vector_type(2)));
typedef float f32x4 __attribute__((ext_vector_type(4)));
typedef float f32x16 __attribute__((ext_vector_type(16)));
typedef short s16x4 __attribute__((ext_vector_type(4)));
typedef __bf16 bf16x2 __attribute__((ext_vector_type(2)));
typedef __bf16 bf16x8 __attribute__((ext_vector_type(8)));
typedef __amdgpu_buffer_rsrc_t rsrc_t;

constexpr int MB = 1 << 20;
constexpr int OFF_Z0 = 0, OFF_Z1 = 2 * MB, OFF_Z2 = 4 * MB, OFF_Z3 = 6 * MB;
constexpr int OFF_WT = 8 * MB;
constexpr int OFF_W1 = 10 * MB;
constexpr int OFF_W2 = 14 * MB;
constexpr int OFF_W3 = 16 * MB;
constexpr int OFF_H1 = 18 * MB;
constexpr int OFF_H2 = 26 * MB;
constexpr int OFF_X3 = 34 * MB;
constexpr int OFF_R0 = 36 * MB;
constexpr int OFF_ST = 36 * MB + 65536;
constexpr int WS_BYTES = 37 * MB;
constexpr int SC1 = 16;
constexpr unsigned SENT = 0xFFFFFFFFu;
constexpr unsigned SPIN_MAX = 1u << 16;

struct Params {
  const u16* x; const u16* wr; const u16* wi; const u16* wg; const u16* wu; const u16* wd;
  u16* out; unsigned char* ws;
};

DI unsigned pk(float a, float b) { f32x2 v = {a, b}; return __builtin_bit_cast(unsigned, __builtin_convertvector(v, bf16x2)); }
DI float asf(unsigned u) { return __builtin_bit_cast(float, u); }
DI unsigned asu(float f) { return __builtin_bit_cast(unsigned, f); }
DI bf16x2 asb2(unsigned u) { return __builtin_bit_cast(bf16x2, u); }
template <int V> struct IC { static constexpr int v = V; };
template <int I, int N, class Fn> DI void sfor(Fn&& fn) { if constexpr (I < N) { fn(IC<I>{}); sfor<I + 1, N>(fn); } }
DI int opaque(int x) { asm volatile("" : "+s"(x)); return x; }
DI float blo(unsigned u) { return __builtin_bit_cast(float, u << 16); }
DI float bhi(unsigned u) { return __builtin_bit_cast(float, u & 0xFFFF0000u); }
DI float rbf(float a) { return blo(pk(a, a)); }
DI unsigned nosent(unsigned u) { return u < 0xFFFFFFFEu ? u : 0xFFFFFFFEu; }
DI v4u nosent4(v4u v) { v.x = nosent(v.x); v.y = nosent(v.y); v.z = nosent(v.z); v.w = nosent(v.w); return v; }
DI unsigned umax(unsigned a, unsigned b) { return a > b ? a : b; }
DI unsigned mx4(v4u v) { return umax(umax(v.x, v.y), umax(v.z, v.w)); }
DI bool any_sent(unsigned m) { return __builtin_amdgcn_ballot_w64(m == SENT) != 0ull; }
DI f32x16 mfma32(v4u a, v4u b, f32x16 c) {
  return __builtin_amdgcn_mfma_f32_32x32x16_bf16(__builtin_bit_cast(bf16x8, a), __builtin_bit_cast(bf16x8, b), c, 0, 0, 0);
}
DI f32x4 mfma16(v4u a, v4u b, f32x4 c) {
  return __builtin_amdgcn_mfma_f32_16x16x32_bf16(__builtin_bit_cast(bf16x8, a), __builtin_bit_cast(bf16x8, b), c, 0, 0, 0);
}
template <bool FIRST, bool WA> DI void mfma_w(f32x16& acc, const v4u& wt, const v4u& x) {
  if constexpr (FIRST && WA) asm volatile("v_mfma_f32_32x32x16_bf16 %0, %1, %2, 0" : "=&v"(acc) : "a"(wt), "v"(x));
  if constexpr (FIRST && !WA) asm volatile("v_mfma_f32_32x32x16_bf16 %0, %2, %1, 0" : "=&v"(acc) : "a"(wt), "v"(x));
  if constexpr (!FIRST && WA) asm volatile("v_mfma_f32_32x32x16_bf16 %0, %1, %2, %0" : "+v"(acc) : "a"(wt), "v"(x));
  if constexpr (!FIRST && !WA) asm volatile("v_mfma_f32_32x32x16_bf16 %0, %2, %1, %0" : "+v"(acc) : "a"(wt), "v"(x));
}
template <int I, bool REUSE> DI void wload(v4u& dst, const v4u& srd, int lane16, int base) {
  const int so = base + (I >> 2) * 4096;
  if constexpr (REUSE) asm volatile("buffer_load_dwordx4 %0, %1, %2, %3 offen offset:%4" : "+a"(dst) : "v"(lane16), "s"(srd), "s"(so), "i"((I & 3) * 1024));
  else asm volatile("buffer_load_dwordx4 %0, %1, %2, %3 offen offset:%4" : "=a"(dst) : "v"(lane16), "s"(srd), "s"(so), "i"((I & 3) * 1024));
}
DI void mfma_done(f32x16& a, f32x16& b, f32x16& c, f32x16& d) { asm volatile("s_nop 12" : "+v"(a), "+v"(b), "+v"(c), "+v"(d)); }
DI v2u trread(LAS unsigned char* p) { return __builtin_bit_cast(v2u, __builtin_amdgcn_ds_read_tr16_b64_v4i16((LAS s16x4*)p)); }
DI v4u tr2(LAS unsigned char* p, int d) { v2u a = trread(p), b = trread(p + d); return v4u{a.x, a.y, b.x, b.y}; }
DI v4u bload(rsrc_t rs, int voff, int soff) { return __builtin_amdgcn_raw_buffer_load_b128(rs, voff, soff, 0); }
DI v4u bload_sc1(rsrc_t rs, int voff, int soff) { return __builtin_amdgcn_raw_buffer_load_b128(rs, voff, soff, SC1); }
DI void bstore(v4u v, rsrc_t rs, int voff, int soff) { __builtin_amdgcn_raw_buffer_store_b128(v, rs, voff + soff, 0, 0); }
DI void bstore_sc1(v4u v, rsrc_t rs, int voff, int soff) { __builtin_amdgcn_raw_buffer_store_b128(v, rs, voff + soff, 0, SC1); }
template <int I> DI v4u fload(rsrc_t rs, int lane16, int base) { return bload(rs, lane16 + (I & 3) * 1024, base + (I >> 2) * 4096); }
template <int I> DI v4u fload_sc1(rsrc_t rs, int lane16, int base) { return bload_sc1(rs, lane16 + (I & 3) * 1024, base + (I >> 2) * 4096); }
#define SB() __builtin_amdgcn_sched_barrier(0)
#define MEMFENCE() asm volatile("" ::: "memory")
#define BAR() asm volatile("s_waitcnt lgkmcnt(0)\n\ts_barrier" ::: "memory")
DI f32x16 zero16() { f32x16 z; for (int i = 0; i < 16; ++i) z[i] = 0.f; return z; }
DI v4u pack8(const f32x16& a, int s, float sc) {
  return v4u{pk(a[8 * s] * sc, a[8 * s + 1] * sc), pk(a[8 * s + 2] * sc, a[8 * s + 3] * sc),
             pk(a[8 * s + 4] * sc, a[8 * s + 5] * sc), pk(a[8 * s + 6] * sc, a[8 * s + 7] * sc)};
}
DI void widen(const f32x16& a, float sc, v4u& o0, v4u& o1) {
  unsigned d[8];
#pragma unroll
  for (int i = 0; i < 8; ++i) d[i] = pk(a[2 * i] * sc, a[2 * i + 1] * sc);
  auto r0 = __builtin_amdgcn_permlane32_swap(d[0], d[2], false, false);
  auto r1 = __builtin_amdgcn_permlane32_swap(d[1], d[3], false, false);
  auto r2 = __builtin_amdgcn_permlane32_swap(d[4], d[6], false, false);
  auto r3 = __builtin_amdgcn_permlane32_swap(d[5], d[7], false, false);
  o0 = v4u{r0[0], r1[0], r0[1], r1[1]};
  o1 = v4u{r2[0], r3[0], r2[1], r3[1]};
}
template <int CTRL> DI float dppf(float v) {
  return __builtin_bit_cast(float, __builtin_amdgcn_update_dpp(0, __builtin_bit_cast(int, v), CTRL, 0xF, 0xF, true));
}
DI float sum32(float v) {
  v += dppf<0xB1>(v); v += dppf<0x4E>(v); v += dppf<0x141>(v); v += dppf<0x140>(v);
  unsigned u = __builtin_bit_cast(unsigned, v);
  auto r = __builtin_amdgcn_permlane16_swap(u, u, false, false);
  return __builtin_bit_cast(float, (unsigned)r[0]) + __builtin_bit_cast(float, (unsigned)r[1]);
}
DI float rstd_of(float var) {
  const float t = rbf(rbf(var) + 1e-5f);
  return __builtin_amdgcn_rcpf(rbf(__builtin_amdgcn_sqrtf(t)));
}
DI int token_of(int p) {
  const int l = (p >> 3) & 63, e = p & 7;
  return 32 * (16 * (p >> 9) + 4 * (l >> 4) + (e & 3)) + (l & 15) + 16 * (e >> 2);
}
DI void glds16(const void* sbase, unsigned voff, unsigned lds) {
  unsigned keep;
  asm volatile("s_mov_b32 %0, m0\n\ts_mov_b32 m0, %3\n\ts_nop 0\n\tglobal_load_lds_dwordx4 %1, %2\n\ts_mov_b32 m0, %0"
               : "=&s"(keep) : "v"(voff), "s"(sbase), "s"(lds) : "memory");
}
DI v4u cosfrag(float a0, float step, int off) {
  float v[8];
#pragma unroll
  for (int j = 0; j < 8; ++j) v[j] = __builtin_amdgcn_cosf(__builtin_fmaf(step, (float)(off + j), a0));
  return v4u{pk(v[0], v[1]), pk(v[2], v[3]), pk(v[4], v[5]), pk(v[6], v[7])};
}

constexpr int P_XL = 0, P_GL = 33280, P_UL = P_GL + 32768 + 32, P_DL = P_UL + 32768 + 32, P_LDS = P_DL + 33280;

__global__ void __launch_bounds__(256, 1) k_prep(Params p) {
  extern __shared__ __attribute__((aligned(16))) unsigned char smem[];
  LAS unsigned char* L = (LAS unsigned char*)smem;
  const unsigned Lb = (unsigned)(size_t)L;
  const int tid = threadIdx.x, lane = tid & 63, w = __builtin_amdgcn_readfirstlane(tid >> 6);
  const int b = blockIdx.x, xcd = b & 7, q = (b >> 3) & 3, tgi = b >> 5;
  const int t1 = xcd * 8 + tgi, tg = tgi * 8 + xcd;
  const bool heavy = tgi < 7;
  const int h = lane >> 5, r = lane & 31, ft = 4 * q + w;
  const rsrc_t rs = __builtin_amdgcn_make_buffer_rsrc((void*)p.ws, 0, WS_BYTES, 0x00020000);

  const int wc = 64 * q + lane, kk0 = 32 * tg + 8 * w;
  u16 fr[8], fi[8];
  {
    const int src = kk0 * 257 + (wc == 0 ? 256 : wc);
#pragma unroll
    for (int i = 0; i < 8; ++i) { fr[i] = p.wr[src + i * 257]; fi[i] = p.wi[src + i * 257]; }
  }
  SB();
#pragma unroll
  for (int i = 0; i < 8; ++i)
    glds16(p.x + (size_t)token_of(32 * t1 + 8 * w + i) * 512, lane * 16, Lb + P_XL + (8 * w + i) * 1040);
  if (heavy) {
    const unsigned vo = (lane >> 2) * 3584 + (lane & 3) * 16;
#pragma unroll
    for (int i = 0; i < 8; ++i) glds16(p.wg + (size_t)(8 * w + i) * 16 * 1792 + 32 * tg, vo, Lb + P_GL + (8 * w + i) * 1024);
#pragma unroll
    for (int i = 0; i < 8; ++i) glds16(p.wu + (size_t)(8 * w + i) * 16 * 1792 + 32 * tg, vo, Lb + P_UL + (8 * w + i) * 1024);
#pragma unroll
    for (int i = 0; i < 8; ++i) glds16(p.wd + (size_t)(32 * tg + 8 * w + i) * 512, lane * 16, Lb + P_DL + (8 * w + i) * 1040);
  }
  {
    const v4u sv = {SENT, SENT, SENT, SENT};
#pragma unroll
    for (int i = 0; i < 4; ++i) bstore_sc1(sv, rs, tid * 16, OFF_Z1 + b * 16384 + i * 4096);
#pragma unroll
    for (int i = 0; i < 7; ++i) bstore_sc1(sv, rs, tid * 16, OFF_H1 + b * 28672 + i * 4096);
    if (tid < 16) bstore_sc1(sv, rs, tid * 16, OFF_ST + b * 256);
  }
  SB();
  const int feat = 32 * ft + r;
  const float step = (feat == 0 ? 0.f : feat == 1 ? 256.f : (float)(feat >> 1)) * (1.f / 512.f);
  const float ph = (feat > 1 && (feat & 1)) ? 0.25f : 0.f;
  const float a0 = ph + step * (float)(8 * h);
  const unsigned sgn = (feat > 1 && (feat & 2)) ? 0x80008000u : 0u;
  v4u F[32];
#pragma unroll
  for (int ks = 0; ks < 16; ++ks) { F[ks] = cosfrag(a0, step, 16 * ks); F[ks + 16] = F[ks] ^ sgn; }
  v4u I[2];
#pragma unroll
  for (int s = 0; s < 2; ++s) {
    const int e = r - 16 * s - 8 * h;
    const unsigned one = (e & 1) ? 0x3F800000u : 0x00003F80u;
    I[s] = v4u{(e >> 1) == 0 ? one : 0u, (e >> 1) == 1 ? one : 0u, (e >> 1) == 2 ? one : 0u, (e >> 1) == 3 ? one : 0u};
  }
  const float lsc = feat < 2 ? 0.04419417382415922f : 0.0625f;
  SB();
  if (heavy) asm volatile("s_waitcnt vmcnt(35)" ::: "memory"); else asm volatile("s_waitcnt vmcnt(11)" ::: "memory");
  __builtin_amdgcn_s_barrier();
  MEMFENCE();
  f32x16 aZ = zero16();
  {
    LAS unsigned char* xa = L + P_XL + r * 1040 + h * 16;
#pragma unroll
    for (int ks = 0; ks < 32; ++ks) aZ = mfma32(*(LAS v4u*)(xa + ks * 32), F[ks], aZ);
  }
  {
    const int row = 8 * q + 2 * w + h;
    const v4u u0 = *(LAS v4u*)(L + P_XL + row * 1040 + r * 32), u1 = *(LAS v4u*)(L + P_XL + row * 1040 + r * 32 + 16);
    float v[16];
#pragma unroll
    for (int i = 0; i < 4; ++i) { v[2 * i] = blo(u0[i]); v[2 * i + 1] = bhi(u0[i]); v[8 + 2 * i] = blo(u1[i]); v[9 + 2 * i] = bhi(u1[i]); }
    float s = 0.f;
#pragma unroll
    for (int i = 0; i < 16; ++i) s += v[i];
    const float m = sum32(s) * (1.f / 512.f);
    float ss = 0.f;
#pragma unroll
    for (int i = 0; i < 16; ++i) { const float d = v[i] - m; ss = __builtin_fmaf(d, d, ss); }
    const float rstd = rstd_of(sum32(ss) * (1.f / 512.f));
    if (r == 0) ((float*)(p.ws + OFF_R0))[32 * t1 + row] = rstd;
  }
  if (heavy) {
    asm volatile("s_waitcnt vmcnt(20)" ::: "memory");
    __builtin_amdgcn_s_barrier();
    MEMFENCE();
    f32x16 aG0 = zero16(), aG1 = zero16();
    {
      LAS unsigned char* ba = L + (((lane >> 4) & 1) ? P_UL : P_GL) + (8 * h + ((lane & 15) >> 2)) * 64 + (lane & 3) * 8;
#pragma unroll
      for (int ks = 0; ks < 32; ++ks) {
        aG0 = mfma32(F[ks], tr2(ba + ks * 1024, 256), aG0);
        aG1 = mfma32(F[ks], tr2(ba + ks * 1024 + 32, 256), aG1);
      }
    }
    asm volatile("s_waitcnt vmcnt(12)" ::: "memory");
    __builtin_amdgcn_s_barrier();
    MEMFENCE();
    f32x16 aD = zero16(), aT = zero16();
    {
      LAS unsigned char* da = L + P_DL + r * 1040 + h * 16;
#pragma unroll
      for (int ks = 0; ks < 32; ++ks) aD = mfma32(*(LAS v4u*)(da + ks * 32), F[ks], aD);
      aT = mfma32(*(LAS v4u*)(da + ft * 64), I[0], aT);
      aT = mfma32(*(LAS v4u*)(da + ft * 64 + 32), I[1], aT);
    }
    if (ft == 0 && h == 0) { aG0[0] = 0.f; aG1[0] = 0.f; aG0[1] *= 0.70710678118654752f; aG1[1] *= 0.70710678118654752f; }
    const int g0 = 2 * tg, g1 = 2 * tg + 1;
    const int o0 = OFF_W1 + ((g0 / 7) * 8 + g0 % 7) * 32768 + 2 * ft * 1024, o1 = OFF_W1 + ((g1 / 7) * 8 + g1 % 7) * 32768 + 2 * ft * 1024;
#pragma unroll
    for (int s = 0; s < 2; ++s) {
      bstore(pack8(aG0, s, 0.0625f), rs, lane * 16, o0 + s * 1024);
      bstore(pack8(aG1, s, 0.0625f), rs, lane * 16, o1 + s * 1024);
      bstore(pack8(aD, s, lsc), rs, lane * 16, OFF_W2 + (ft * 112 + 2 * tg + s) * 1024);
      bstore(pack8(aT, s, 1.f), rs, lane * 16, OFF_W3 + (ft * 112 + 2 * tg + s) * 1024);
    }
  } else {
#pragma unroll
    for (int i = 0; i < 2; ++i) {
      const int dt = 2 * xcd + i;
      const float b0 = __builtin_amdgcn_fractf(__builtin_fmaf(step, (float)(32 * dt), a0));
      f32x16 aI = mfma32(cosfrag(b0, step, 0), I[0], zero16());
      aI = mfma32(cosfrag(b0, step, 16), I[1], aI);
      if (ft == 0 && h == 0) { aI[0] *= 0.70710678118654752f; aI[1] *= 0.70710678118654752f; }
#pragma unroll
      for (int s = 0; s < 2; ++s) bstore(pack8(aI, s, 0.0625f), rs, lane * 16, OFF_W1 + (dt * 8 + 7) * 32768 + (2 * ft + s) * 1024);
    }
  }
  {
    v4u o0, o1;
    widen(aZ, lsc, o0, o1);
    bstore(o0, rs, feat * 4096 + h * 16, OFF_Z0 + t1 * 64);
    bstore(o1, rs, feat * 4096 + h * 16, OFF_Z0 + t1 * 64 + 32);
  }
  {
    v4u o0, o1;
#pragma unroll
    for (int i = 0; i < 4; ++i) { o0[i] = fr[i] | ((unsigned)fi[i] << 16); o1[i] = fr[4 + i] | ((unsigned)fi[4 + i] << 16); }
    bstore(o0, rs, wc * 8192, OFF_WT + kk0 * 4);
    bstore(o1, rs, wc * 8192, OFF_WT + kk0 * 4 + 16);
  }
}

constexpr int M_Z = 0;
constexpr int M_S1 = 131072;
constexpr int M_S2 = M_S1 + 2048;
constexpr int M_C1 = M_S2 + 1024;
constexpr int M_C2 = M_C1 + 2 * 4608;
constexpr int M_LDS = M_C2 + 2 * 4608;
#define DELAY_A 6
#define DELAY_C 6

struct ColConst { v4u C1[2], S1[2], N1[2], C2[2], S2[2], N2[2]; float tc[8], ts[8]; };

DI void col_consts(ColConst& k, int w, int lane) {
  const int g = lane >> 4, r = lane & 15;
  const float k1 = (float)(16 * w + r);
  const unsigned neg = 0x80008000u;
#pragma unroll
  for (int cs = 0; cs < 2; ++cs) {
    const float st = k1 * (1.f / 64.f), a0 = st * (float)(8 * g);
    k.C1[cs] = cosfrag(a0, st, 32 * cs);
    k.S1[cs] = cosfrag(a0 - 0.25f, st, 32 * cs);
    k.N1[cs] = k.S1[cs] ^ neg;
  }
#pragma unroll
  for (int t = 0; t < 2; ++t) {
    const float st = (float)(16 * t + r) * (1.f / 32.f), a0 = st * (float)(4 * g);
    float c[8], s[8];
#pragma unroll
    for (int j = 0; j < 8; ++j) {
      const float a = __builtin_fmaf(st, (float)(16 * (j >> 2) + (j & 3)), a0);
      c[j] = __builtin_amdgcn_cosf(a); s[j] = __builtin_amdgcn_sinf(a);
    }
    k.C2[t] = v4u{pk(c[0], c[1]), pk(c[2], c[3]), pk(c[4], c[5]), pk(c[6], c[7])};
    k.S2[t] = v4u{pk(s[0], s[1]), pk(s[2], s[3]), pk(s[4], s[5]), pk(s[6], s[7])};
    k.N2[t] = k.S2[t] ^ neg;
  }
#pragma unroll
  for (int e = 0; e < 8; ++e) {
    const float a = k1 * (1.f / 2048.f) * (float)(16 * (e >> 2) + 4 * g + (e & 3));
    k.tc[e] = __builtin_amdgcn_cosf(a); k.ts[e] = __builtin_amdgcn_sinf(a);
  }
}

DI void col_compute(LAS unsigned char* L, rsrc_t rs, const ColConst& k, const unsigned (&wf)[8], v4u zr, v4u zi, const float (&rstd)[8],
                    int c, int w, int lane, int out_off) {
  const int g = lane >> 4, r = lane & 15;
  const bool first = c == 0;
  float xr[8], xi[8];
#pragma unroll
  for (int i = 0; i < 4; ++i) { xr[2 * i] = blo(zr[i]); xr[2 * i + 1] = bhi(zr[i]); xi[2 * i] = blo(zi[i]); xi[2 * i + 1] = bhi(zi[i]); }
#pragma unroll
  for (int t = 0; t < 2; ++t) {
    float a[4], bq[4];
#pragma unroll
    for (int i = 0; i < 4; ++i) {
      a[i] = (first ? xi[4 * t + i] : xr[4 * t + i]) * rstd[4 * t + i];
      bq[i] = first ? 0.f : xi[4 * t + i] * rstd[4 * t + i];
    }
    LAS unsigned char* d = L + M_C1 + (r + 16 * t) * 144 + (16 * w + 4 * g) * 2;
    *(LAS v2u*)d = v2u{pk(a[0], a[1]), pk(a[2], a[3])};
    *(LAS v2u*)(d + 4608) = v2u{pk(bq[0], bq[1]), pk(bq[2], bq[3])};
  }
  BAR();
  const f32x4 z4 = {0.f, 0.f, 0.f, 0.f};
  f32x4 dr[2], di[2];
#pragma unroll
  for (int t = 0; t < 2; ++t) {
    dr[t] = z4; di[t] = z4;
#pragma unroll
    for (int cs = 0; cs < 2; ++cs) {
      LAS unsigned char* s = L + M_C1 + (r + 16 * t) * 144 + (32 * cs + 8 * g) * 2;
      const v4u ar = *(LAS v4u*)s, ai = *(LAS v4u*)(s + 4608);
      dr[t] = mfma16(ar, k.C1[cs], dr[t]); di[t] = mfma16(ai, k.C1[cs], di[t]);
      dr[t] = mfma16(ai, k.S1[cs], dr[t]); di[t] = mfma16(ar, k.N1[cs], di[t]);
    }
  }
  float ur[8], ui[8];
#pragma unroll
  for (int e = 0; e < 8; ++e) {
    const float a = dr[e >> 2][e & 3], bq = di[e >> 2][e & 3];
    ur[e] = a * k.tc[e] + bq * k.ts[e]; ui[e] = bq * k.tc[e] - a * k.ts[e];
  }
  v4u br = {pk(ur[0], ur[1]), pk(ur[2], ur[3]), pk(ur[4], ur[5]), pk(ur[6], ur[7])};
  v4u bi = {pk(ui[0], ui[1]), pk(ui[2], ui[3]), pk(ui[4], ui[5]), pk(ui[6], ui[7])};
#pragma unroll
  for (int t = 0; t < 2; ++t) {
    dr[t] = mfma16(k.S2[t], bi, mfma16(k.C2[t], br, z4));
    di[t] = mfma16(k.N2[t], br, mfma16(k.C2[t], bi, z4));
  }
#pragma unroll
  for (int e = 0; e < 8; ++e) {
    const float a = dr[e >> 2][e & 3], bq = di[e >> 2][e & 3];
    const float fr = blo(wf[e]) * (1.f / 2048.f), fi = bhi(wf[e]) * (1.f / 2048.f);
    ur[e] = a * fr - bq * fi; ui[e] = a * fi + bq * fr;
  }
  br = v4u{pk(ur[0], ur[1]), pk(ur[2], ur[3]), pk(ur[4], ur[5]), pk(ur[6], ur[7])};
  bi = v4u{pk(ui[0], ui[1]), pk(ui[2], ui[3]), pk(ui[4], ui[5]), pk(ui[6], ui[7])};
#pragma unroll
  for (int t = 0; t < 2; ++t) {
    dr[t] = mfma16(k.N2[t], bi, mfma16(k.C2[t], br, z4));
    di[t] = mfma16(k.S2[t], br, mfma16(k.C2[t], bi, z4));
  }
#pragma unroll
  for (int t = 0; t < 2; ++t) {
    float a[4], bq[4];
#pragma unroll
    for (int i = 0; i < 4; ++i) {
      const int e = 4 * t + i;
      a[i] = dr[t][i] * k.tc[e] - di[t][i] * k.ts[e]; bq[i] = di[t][i] * k.tc[e] + dr[t][i] * k.ts[e];
    }
    LAS unsigned char* d = L + M_C2 + (16 * w + r) * 72 + (16 * t + 4 * g) * 2;
    *(LAS v2u*)d = v2u{pk(a[0], a[1]), pk(a[2], a[3])};
    *(LAS v2u*)(d + 4608) = v2u{pk(bq[0], bq[1]), pk(bq[2], bq[3])};
  }
  BAR();
#pragma unroll
  for (int t = 0; t < 2; ++t) {
    dr[t] = z4; di[t] = z4;
#pragma unroll
    for (int cs = 0; cs < 2; ++cs) {
      LAS unsigned char* s = L + M_C2 + (32 * cs + 8 * g + (r >> 2)) * 72 + (16 * t + 4 * (r & 3)) * 2;
      const v4u fr = tr2(s, 4 * 72), fi = tr2(s + 4608, 4 * 72);
      dr[t] = mfma16(k.C1[cs], fr, dr[t]); di[t] = mfma16(k.C1[cs], fi, di[t]);
      dr[t] = mfma16(k.N1[cs], fi, dr[t]); di[t] = mfma16(k.S1[cs], fr, di[t]);
    }
  }
#pragma unroll
  for (int e = 0; e < 8; ++e) {
    const float yr = dr[e >> 2][e & 3], yi = di[e >> 2][e & 3];
    xr[e] += first ? 0.f : yr; xi[e] += first ? yr : yi;
  }
  const int vo = (w * 64 + lane) * 16;
  bstore_sc1(nosent4(v4u{pk(xr[0], xr[1]), pk(xr[2], xr[3]), pk(xr[4], xr[5]), pk(xr[6], xr[7])}), rs, vo, out_off + c * 8192);
  bstore_sc1(nosent4(v4u{pk(xi[0], xi[1]), pk(xi[2], xi[3]), pk(xi[4], xi[5]), pk(xi[6], xi[7])}), rs, vo, out_off + c * 8192 + 4096);
}

template <int LO, int HI> DI void load_w1(v4u (&W)[64], const v4u& srd, int lane, int base) {
  base = opaque(base);
  sfor<LO, HI>([&](auto I) { wload<I.v, false>(W[2 * I.v], srd, lane * 16, base); wload<32 + I.v, false>(W[2 * I.v + 1], srd, lane * 16, base); });
}
template <int U> DI void z_issue(v4u (&zs)[16], rsrc_t rs, int zvo, int zso) {
#pragma unroll
  for (int i = 0; i < 2; ++i) zs[(2 * U + i) & 15] = bload_sc1(rs, zvo + (U >> 3) * 128, opaque(zso) + ((U & 7) * 64 + 32 * i) * 4096);
}
template <int U, int END> DI void z_reissue(v4u (&zs)[16], rsrc_t rs, int zvo, int zso) {
  if constexpr (U < END) { z_issue<U>(zs, rs, zvo, zso); z_reissue<U + 1, END>(zs, rs, zvo, zso); }
}
template <int U, int N, int PEND> DI void z_stage(v4u (&zs)[16], rsrc_t rs, int zvo, int zso, LAS unsigned char* zw0, LAS unsigned char* zw1) {
  auto missing = [&]() {
    unsigned m = 0;
#pragma unroll
    for (int i = 0; i < 2 * N; ++i) m = umax(m, mx4(zs[(2 * U + i) & 15]));
    return any_sent(m);
  };
  if (__builtin_expect(missing(), 0)) {
    unsigned spin = 0;
    do { MEMFENCE(); z_reissue<U, PEND>(zs, rs, zvo, zso); } while (missing() && ++spin < SPIN_MAX);
  }
#pragma unroll
  for (int i = 0; i < 2 * N; ++i) {
    const int u = U + (i >> 1);
    *(LAS v4u*)(((u >> 3) ? zw1 : zw0) + ((u & 7) * 64 + 32 * (i & 1)) * 256) = zs[(2 * U + i) & 15];
  }
  if constexpr (U + 8 < 16) z_reissue<U + 8, (U + N + 8 < 16 ? U + N + 8 : 16)>(zs, rs, zvo, zso);
}

template <int LO, int HI> DI void h_issue(v4u (&hh)[28], rsrc_t rs, int lane, int base) {
  base = opaque(base);
  sfor<LO, HI>([&](auto I) { hh[I.v] = fload_sc1<I.v>(rs, lane * 16, base); });
}
template <bool DC> DI void sumsq(float& ss, v4u q, unsigned dcmask) {
  if constexpr (DC) q.x &= dcmask;
#pragma unroll
  for (int i = 0; i < 4; ++i) ss = __builtin_amdgcn_fdot2_f32_bf16(asb2(q[i]), asb2(q[i]), ss, false);
  asm volatile("" : "+v"(ss));
}
DI void h_piece(const f32x16& a, float sc, rsrc_t rs, int lane, int so) {
  unsigned d[4];
#pragma unroll
  for (int i = 0; i < 4; ++i) {
    const float h0 = __builtin_fmaxf(a[2 * i], 0.f) * sc * a[2 * i + 8], h1 = __builtin_fmaxf(a[2 * i + 1], 0.f) * sc * a[2 * i + 9];
    d[i] = nosent(pk(h0, h1));
  }
  bstore_sc1(v4u{d[0], d[1], d[2], d[3]}, rs, lane * 16, so);
}

DI void gemm1(LAS unsigned char* L, rsrc_t rs, const v4u& srd, v4u (&W)[64], v4u (&hA)[28], v4u (&res)[2], int layer, int rb, int fs, int w, int lane) {
  const int zoff = OFF_Z1 + layer * (OFF_Z3 - OFF_Z1), hoff = OFF_H1 + layer * (OFF_H2 - OFF_H1), voff = OFF_W2 + layer * (OFF_W3 - OFF_W2);
  const int h = lane >> 5, r = lane & 31, q = (lane & 15) >> 2;
  const int zvo = (8 * w + (lane >> 3)) * 4096 + (lane & 7) * 16, zso = zoff + rb * 256;
  const int zrow = 8 * w + (lane >> 3);
  LAS unsigned char* zw0 = L + M_Z + zrow * 256 + (((lane & 7) ^ ((zrow & 3) << 2)) * 16);
  LAS unsigned char* zw1 = L + M_Z + zrow * 256 + (((8 + (lane & 7)) ^ ((zrow & 3) << 2)) * 16);
  v4u zs[16];
  z_reissue<0, 8>(zs, rs, zvo, zso);
  asm volatile("s_waitcnt vmcnt(16)" ::: "memory");
  SB();
  LAS unsigned char* zr[4];
#pragma unroll
  for (int st = 0; st < 4; ++st)
    zr[st] = L + M_Z + (4 * h + q) * 256 + (4 * (st ^ q) + 2 * ((lane >> 4) & 1) + ((lane & 3) >> 1)) * 16 + 8 * (lane & 1);
  const unsigned dcmask = (w == 0 && h == 0) ? 0xFFFF0000u : 0xFFFFFFFFu;
  const int vbase = voff + ((fs & 7) * 2 * 112 + 28 * w) * 1024;
  const int hbase = hoff + ((4 * rb + (fs >> 3)) * 112 + 28 * w) * 1024;
  const int ks0 = fs * 7 + 2 * w;
  const bool two = w < 3;
  z_stage<0, 1, 8>(zs, rs, zvo, zso, zw0, zw1);
  BAR();
  f32x16 acc[2][2][2];
  float sc[2][2];
  sfor<0, 2>([&](auto HH) {
    constexpr int H = HH.v;
    float ss[2] = {0.f, 0.f};
    v4u bf[2][2], sq[2];
    LAS unsigned char* zb[2] = {zr[2 * H], zr[2 * H + 1]};
    LAS unsigned char* zq[2] = {zr[2 * H] + w * 32768, zr[2 * H + 1] + w * 32768};
#pragma unroll
    for (int s = 0; s < 2; ++s) bf[0][s] = tr2(zb[s], 2048);
    sfor<0, 32>([&](auto KS) {
      constexpr int ks = KS.v;
      constexpr bool edge = H == 0 && (ks == 3 || ks == 7 || ks == 15);
      if constexpr (ks == 15) { zb[0] += 65536; zb[1] += 65536; asm volatile("" : "+v"(zb[0]), "+v"(zb[1])); }
      if constexpr (ks + 1 < 32 && !edge) {
#pragma unroll
        for (int s = 0; s < 2; ++s) bf[(ks + 1) & 1][s] = tr2(zb[s] + ((ks + 1) & 15) * 4096, 2048);
      }
      if constexpr (ks >= 16) sq[ks & 1] = tr2(zq[ks & 1] + ((ks - 16) >> 1) * 4096, 2048);
#pragma unroll
      for (int s = 0; s < 2; ++s) {
        mfma_w<ks == 0, true>(acc[H][0][s], W[2 * ks], bf[ks & 1][s]);
        mfma_w<ks == 0, true>(acc[H][1][s], W[2 * ks + 1], bf[ks & 1][s]);
      }
      if constexpr (ks >= 17) sumsq<(ks < 19)>(ss[(ks - 1) & 1], sq[(ks - 1) & 1], dcmask);
      if constexpr (H == 0) {
        if constexpr (ks == 1) z_stage<1, 1, 9>(zs, rs, zvo, zso, zw0, zw1);
        if constexpr (ks == 5) z_stage<2, 2, 10>(zs, rs, zvo, zso, zw0, zw1);
        if constexpr (ks == 10) z_stage<4, 2, 12>(zs, rs, zvo, zso, zw0, zw1);
        if constexpr (ks == 13) z_stage<6, 2, 14>(zs, rs, zvo, zso, zw0, zw1);
        if constexpr (ks == 18) z_stage<8, 2, 16>(zs, rs, zvo, zso, zw0, zw1);
        if constexpr (ks == 21) z_stage<10, 2, 16>(zs, rs, zvo, zso, zw0, zw1);
        if constexpr (ks == 24) z_stage<12, 2, 16>(zs, rs, zvo, zso, zw0, zw1);
        if constexpr (ks == 27) z_stage<14, 2, 16>(zs, rs, zvo, zso, zw0, zw1);
        if constexpr (edge) {
          BAR();
#pragma unroll
          for (int s = 0; s < 2; ++s) bf[(ks + 1) & 1][s] = tr2(zb[s] + ((ks + 1) & 15) * 4096, 2048);
        }
      } else {
        if constexpr (ks == 1 || ks == 3) h_piece(acc[0][0][ks >> 1], sc[0][ks >> 1], rs, lane, hoff + ((4 * rb + (ks >> 1)) * 112 + ks0) * 1024);
        if constexpr (ks == 5 || ks == 7) {
          constexpr int s = (ks - 5) >> 1;
          if (two) h_piece(acc[0][1][s], sc[0][s], rs, lane, hoff + ((4 * rb + s) * 112 + ks0 + 1) * 1024);
          else if (layer == 1) {
            v4u o0, o1;
            widen(acc[0][1][s], 1.f, o0, o1);
            bstore_sc1(nosent4(o0), rs, r * 1024 + h * 16, OFF_X3 + (128 * rb + 32 * s) * 1024 + fs * 64);
            bstore_sc1(nosent4(o1), rs, r * 1024 + h * 16, OFF_X3 + (128 * rb + 32 * s) * 1024 + fs * 64 + 32);
          }
        }
        if constexpr (ks < 28) {
          wload<ks, true>(W[2 * ks], srd, lane * 16, opaque(vbase));
          wload<112 + ks, true>(W[2 * ks + 1], srd, lane * 16, opaque(vbase));
        }
        if constexpr (ks >= 18) h_issue<2 * (ks - 18), 2 * (ks - 18) + 2>(hA, rs, lane, hbase);
      }
      SB();
    });
    mfma_done(acc[H][0][0], acc[H][0][1], acc[H][1][0], acc[H][1][1]);
    sumsq<false>(ss[1], sq[1], dcmask);
    {
      auto x = __builtin_amdgcn_permlane32_swap(asu(ss[0]), asu(ss[1]), false, false);
      *(LAS float*)(L + M_S1 + H * 1024 + w * 256 + lane * 4) = asf(x[0]) + asf(x[1]);
    }
    if constexpr (H == 1) {
#pragma unroll
      for (int i = 0; i < 2; ++i) {
        const int n = (fs & 7) * 64 + 16 * w + (lane >> 3) + 8 * i;
        const int ch = ((lane & 4) ? 8 : 0) + 4 * (fs >> 3) + (lane & 3);
        res[i] = *(LAS v4u*)(L + M_Z + n * 256 + ((ch ^ ((n & 3) << 2)) * 16));
      }
    }
    BAR();
#pragma unroll
    for (int s = 0; s < 2; ++s) {
      LAS float* sp = (LAS float*)(L + M_S1 + H * 1024 + (32 * s + r) * 4);
      const float rstd = rstd_of(((sp[0] + sp[64]) + (sp[128] + sp[192])) * (1.f / 512.f));
      sc[H][s] = rstd * rstd;
    }
  });
#pragma unroll
  for (int s = 0; s < 2; ++s) {
    h_piece(acc[1][0][s], sc[1][s], rs, lane, hoff + ((4 * rb + 2 + s) * 112 + ks0) * 1024);
    if (two) h_piece(acc[1][1][s], sc[1][s], rs, lane, hoff + ((4 * rb + 2 + s) * 112 + ks0 + 1) * 1024);
    else if (layer == 1) {
      v4u o0, o1;
      widen(acc[1][1][s], 1.f, o0, o1);
      bstore_sc1(nosent4(o0), rs, r * 1024 + h * 16, OFF_X3 + (128 * rb + 64 + 32 * s) * 1024 + fs * 64);
      bstore_sc1(nosent4(o1), rs, r * 1024 + h * 16, OFF_X3 + (128 * rb + 64 + 32 * s) * 1024 + fs * 64 + 32);
    }
  }
  SB();
}

template <int LO> DI void h_check(v4u (&hh)[28], rsrc_t rs, int lane, int base) {
  auto missing = [&]() {
    unsigned m = 0;
#pragma unroll
    for (int i = LO; i < LO + 7; ++i) m = umax(m, mx4(hh[i]));
    return any_sent(m);
  };
  if (__builtin_expect(missing(), 0)) {
    unsigned spin = 0;
    do { MEMFENCE(); h_issue<LO, 28>(hh, rs, lane, base); } while (missing() && ++spin < SPIN_MAX);
  }
}

DI void gemm2(LAS unsigned char* L, rsrc_t rs, const Params& p, v4u (&W)[64], v4u (&hA)[28], v4u (&res)[2], int layer, int rb, int fs, int w, int lane) {
  const int h = lane >> 5, r = lane & 31, nb = fs & 7, pt = 4 * rb + (fs >> 3);
  const int hbase = OFF_H1 + layer * (OFF_H2 - OFF_H1) + (pt * 112 + 28 * w) * 1024;
  v4u hB[28], x3[2];
  f32x16 acc[2][2];
  asm volatile("s_waitcnt vmcnt(10)" ::: "memory");
  const int row = 16 * w + (lane >> 3);
  const int xvo = (((row & 32) ? 64 : 0) + (row & 31)) * 1024 + (lane & 7) * 16, xso = OFF_Z0 + layer * (OFF_X3 - OFF_Z0) + pt * 32 * 1024 + nb * 128;
  sfor<0, 2>([&](auto HH) {
    constexpr int H = HH.v;
    sfor<0, 28>([&](auto II) {
      constexpr int i = II.v;
      if constexpr (H == 0) {
        if constexpr (i % 7 == 0) h_check<i>(hA, rs, lane, hbase);
        if constexpr (i >= 14) h_issue<2 * (i - 14), 2 * (i - 14) + 2>(hB, rs, lane, hbase + 224 * 1024);
      } else {
        if constexpr (i % 7 == 0) h_check<i>(hB, rs, lane, hbase + 224 * 1024);
        if constexpr (i == 8) { x3[0] = bload_sc1(rs, xvo, xso); x3[1] = bload_sc1(rs, xvo, xso + 8192); }
      }
      const v4u hv = H == 0 ? hA[i] : hB[i];
      mfma_w<i == 0, false>(acc[H][0], W[2 * i], hv);
      mfma_w<i == 0, false>(acc[H][1], W[2 * i + 1], hv);
      SB();
    });
  });
  mfma_done(acc[0][0], acc[0][1], acc[1][0], acc[1][1]);
  if (layer == 0) {
#pragma unroll
    for (int H = 0; H < 2; ++H)
#pragma unroll
      for (int t = 0; t < 2; ++t)
#pragma unroll
        for (int g = 0; g < 4; ++g)
          *(LAS f32x4*)(L + M_Z + w * 17408 + (32 * t + r) * 272 + (32 * H + 8 * g + 4 * h) * 4) =
              f32x4{acc[H][t][4 * g], acc[H][t][4 * g + 1], acc[H][t][4 * g + 2], acc[H][t][4 * g + 3]};
    asm volatile("s_waitcnt vmcnt(0)" ::: "memory");
  } else {
#pragma unroll
    for (int H = 0; H < 2; ++H)
#pragma unroll
      for (int t = 0; t < 2; ++t)
#pragma unroll
        for (int i = 0; i < 16; ++i)
          *(LAS float*)(L + M_Z + w * 17408 + (32 * H + (i & 3) + 8 * (i >> 2) + 4 * h) * 272 + (32 * t + r) * 4) = acc[H][t][i];
    if (__builtin_expect(any_sent(umax(mx4(x3[0]), mx4(x3[1]))), 0)) {
      unsigned spin = 0;
      do { MEMFENCE(); x3[0] = bload_sc1(rs, xvo, xso); x3[1] = bload_sc1(rs, xvo, xso + 8192); }
      while (any_sent(umax(mx4(x3[0]), mx4(x3[1]))) && ++spin < SPIN_MAX);
    }
    res[0] = x3[0]; res[1] = x3[1];
  }
  BAR();
  v4u o[2];
#pragma unroll
  for (int i = 0; i < 2; ++i) {
    float v[8];
#pragma unroll
    for (int j = 0; j < 4; ++j) { v[2 * j] = blo(res[i][j]); v[2 * j + 1] = bhi(res[i][j]); }
    f32x4 s0 = {0.f, 0.f, 0.f, 0.f}, s1 = s0;
#pragma unroll
    for (int ww = 0; ww < 4; ++ww) {
      LAS unsigned char* s = L + M_Z + ww * 17408 + (row + 8 * i) * 272 + (lane & 7) * 32;
      s0 += *(LAS f32x4*)s; s1 += *(LAS f32x4*)(s + 16);
    }
#pragma unroll
    for (int j = 0; j < 4; ++j) { v[j] += s0[j]; v[4 + j] += s1[j]; }
    o[i] = v4u{pk(v[0], v[1]), pk(v[2], v[3]), pk(v[4], v[5]), pk(v[6], v[7])};
  }
  if (layer == 0) {
    float sq[8];
#pragma unroll
    for (int i = 0; i < 2; ++i) {
      bstore_sc1(nosent4(o[i]), rs, (row + 8 * i) * 4096 + ((lane & 4) ? 128 : 0) + (lane & 3) * 16, OFF_Z2 + nb * 64 * 4096 + pt * 64);
      const bool dc = nb == 0 && row + 8 * i == 0;
#pragma unroll
      for (int j = 0; j < 4; ++j) {
        const float a = dc ? 0.f : blo(o[i][j]), bq = dc ? 0.f : bhi(o[i][j]);
        sq[2 * j] = i == 0 ? a * a : __builtin_fmaf(a, a, sq[2 * j]);
        sq[2 * j + 1] = i == 0 ? bq * bq : __builtin_fmaf(bq, bq, sq[2 * j + 1]);
      }
    }
    float t4[4], t2[2];
#pragma unroll
    for (int j = 0; j < 4; ++j) {
      auto x = __builtin_amdgcn_permlane32_swap(asu(sq[j]), asu(sq[j + 4]), false, false);
      t4[j] = asf(x[0]) + asf(x[1]);
    }
#pragma unroll
    for (int j = 0; j < 2; ++j) {
      auto x = __builtin_amdgcn_permlane16_swap(asu(t4[j]), asu(t4[j + 2]), false, false);
      t2[j] = asf(x[0]) + asf(x[1]);
    }
    const bool up = lane & 8;
    const float keep = up ? t2[1] : t2[0], send = up ? t2[0] : t2[1];
    const float t1 = keep + dppf<0x128>(send);
    const int col = 8 * (lane & 7) + 4 * (lane >> 5) + 2 * ((lane >> 4) & 1) + ((lane >> 3) & 1);
    *(LAS float*)(L + M_S2 + w * 256 + col * 4) = t1;
    BAR();
    if (w == 0) {
      LAS float* sp = (LAS float*)(L + M_S2 + lane * 4);
      const float t = (sp[0] + sp[64]) + (sp[128] + sp[192]);
      __builtin_amdgcn_raw_buffer_store_b32(nosent(asu(t)), rs, (h * 64 + r) * 4, OFF_ST + nb * 8192 + pt * 128, SC1);
    }
  } else {
#pragma unroll
    for (int i = 0; i < 2; ++i) {
      const int rr = row + 8 * i;
      const int tok = token_of(32 * pt + ((rr & 32) ? 64 : 0) + (rr & 31));
      *(v4u*)(p.out + (size_t)tok * 512 + nb * 64 + (lane & 7) * 8) = o[i];
    }
  }
  SB();
}

__global__ void __launch_bounds__(256, 1) k_main(Params p) {
  extern __shared__ __attribute__((aligned(16))) unsigned char smem[];
  LAS unsigned char* L = (LAS unsigned char*)smem;
  const rsrc_t rs = __builtin_amdgcn_make_buffer_rsrc((void*)p.ws, 0, WS_BYTES, 0x00020000);
  const v4u srd = {(unsigned)(size_t)p.ws, (unsigned)((size_t)p.ws >> 32) & 0xFFFFu, (unsigned)WS_BYTES, 0x00020000u};
#pragma unroll 1
  for (int layer = 0; layer < 2; ++layer) {
    int tid = threadIdx.x, c = blockIdx.x;
    asm volatile("" : "+v"(tid), "+s"(c));
    const int lane = tid & 63, w = __builtin_amdgcn_readfirstlane(tid >> 6);
    const int rb = 2 * (c & 7) + (c >> 7), fs = (c >> 3) & 15;
    const int w1base = OFF_W1 + (fs * 8 + 2 * w) * 32768;
    v4u W[64], hA[28], res[2];
    unsigned wf[8];
    const int wfvo = (16 * w + (lane & 15) + 64 * 4 * (lane >> 4)) * 4;
    v4u zr, zi, r0, r1;
    asm volatile("buffer_load_dwordx4 %0, %4, %6, %7 offen\n\tbuffer_load_dwordx4 %1, %4, %6, %8 offen\n\t"
                 "buffer_load_dwordx4 %2, %5, %6, %9 offen\n\tbuffer_load_dwordx4 %3, %5, %6, %9 offen offset:16"
                 : "=&v"(zr), "=&v"(zi), "=&v"(r0), "=&v"(r1)
                 : "v"(tid * 16), "v"(tid * 32), "s"(srd), "s"(OFF_Z0 + c * 8192), "s"(OFF_Z0 + c * 8192 + 4096), "s"(OFF_R0));
    asm volatile("buffer_load_dword %0, %8, %9, %10 offen\n\tbuffer_load_dword %1, %8, %9, %10 offen offset:256\n\t"
                 "buffer_load_dword %2, %8, %9, %10 offen offset:512\n\tbuffer_load_dword %3, %8, %9, %10 offen offset:768\n\t"
                 "buffer_load_dword %4, %8, %9, %11 offen\n\tbuffer_load_dword %5, %8, %9, %11 offen offset:256\n\t"
                 "buffer_load_dword %6, %8, %9, %11 offen offset:512\n\tbuffer_load_dword %7, %8, %9, %11 offen offset:768"
                 : "=&v"(wf[0]), "=&v"(wf[1]), "=&v"(wf[2]), "=&v"(wf[3]), "=&v"(wf[4]), "=&v"(wf[5]), "=&v"(wf[6]), "=&v"(wf[7])
                 : "v"(wfvo), "s"(srd), "s"(OFF_WT + c * 8192), "s"(OFF_WT + c * 8192 + 4096));
    SB();
    load_w1<0, 16>(W, srd, lane, w1base);
    SB();
    ColConst k;
    col_consts(k, w, lane);
    SB();
    asm volatile("s_waitcnt vmcnt(32)"
                 : "+v"(zr), "+v"(zi), "+v"(r0), "+v"(r1), "+v"(wf[0]), "+v"(wf[1]), "+v"(wf[2]), "+v"(wf[3]), "+v"(wf[4]), "+v"(wf[5]), "+v"(wf[6]), "+v"(wf[7]));
    float rstd[8];
    if (layer == 0) {
#pragma unroll
      for (int i = 0; i < 4; ++i) { rstd[i] = asf(r0[i]); rstd[4 + i] = asf(r1[i]); }
    } else {
      __builtin_amdgcn_s_sleep(DELAY_C);
      v4u st[16];
      unsigned spin = 0, m;
      do {
        MEMFENCE();
        zr = bload_sc1(rs, tid * 16, OFF_Z2 + c * 8192); zi = bload_sc1(rs, tid * 16, OFF_Z2 + c * 8192 + 4096);
#pragma unroll
        for (int i = 0; i < 16; ++i) st[i] = bload_sc1(rs, tid * 32 + (i & 1) * 16, OFF_ST + (i >> 1) * 8192);
        m = umax(mx4(zr), mx4(zi));
#pragma unroll
        for (int i = 0; i < 16; ++i) m = umax(m, mx4(st[i]));
      } while (any_sent(m) && ++spin < SPIN_MAX);
#pragma unroll
      for (int e = 0; e < 8; ++e) {
        float s = 0.f;
#pragma unroll
        for (int n = 0; n < 8; ++n) s += asf(st[2 * n + (e >> 2)][e & 3]);
        rstd[e] = rstd_of(s * (1.f / 512.f));
      }
    }
    col_compute(L, rs, k, wf, zr, zi, rstd, c, w, lane, OFF_Z1 + layer * (OFF_Z3 - OFF_Z1));
    SB();
    load_w1<16, 32>(W, srd, lane, w1base);
    if (layer == 0) {
      const v4u sv = {SENT, SENT, SENT, SENT};
#pragma unroll
      for (int i = 0; i < 2; ++i) bstore_sc1(sv, rs, tid * 16, OFF_Z3 + c * 8192 + i * 4096);
#pragma unroll
      for (int i = 0; i < 7; ++i) bstore_sc1(sv, rs, lane * 16, OFF_H2 + ((4 * rb + w) * 112 + fs * 7 + i) * 1024);
#pragma unroll
      for (int i = 0; i < 2; ++i) bstore_sc1(sv, rs, (tid >> 2) * 1024 + (tid & 3) * 16, OFF_X3 + (128 * rb + 64 * i) * 1024 + fs * 64);
    }
    SB();
    __builtin_amdgcn_s_sleep(DELAY_A);
    gemm1(L, rs, srd, W, hA, res, layer, rb, fs, w, lane);
    gemm2(L, rs, p, W, hA, res, layer, rb, fs, w, lane);
  }
}

extern "C" void kernel_launch(void* const* d_in, const int* in_sizes, int n_in, void* d_out, int out_size, void* d_ws, size_t ws_size,
                              hipStream_t stream) {
  Params p;
  p.x = (const u16*)d_in[0]; p.wr = (const u16*)d_in[1]; p.wi = (const u16*)d_in[2];
  p.wg = (const u16*)d_in[4]; p.wu = (const u16*)d_in[5]; p.wd = (const u16*)d_in[6];
  p.out = (u16*)d_out; p.ws = (unsigned char*)d_ws;
  (void)hipFuncSetAttribute((const void*)k_prep, hipFuncAttributeMaxDynamicSharedMemorySize, P_LDS);
  (void)hipFuncSetAttribute((const void*)k_main, hipFuncAttributeMaxDynamicSharedMemorySize, M_LDS);
  k_prep<<<256, 256, P_LDS, stream>>>(p);
  k_main<<<256, 256, M_LDS, stream>>>(p);
}
```

```cpp
#include <hip/hip_runtime.h>
#include <hip/hip_bf16.h>

#define LAS __attribute__((address_space(3)))
#define DI __device__ __forceinline__
typedef unsigned short u16;
typedef unsigned v4u __attribute__((ext_vector_type(4)));
typedef unsigned v2u __attribute__((ext_vector_type(2)));
typedef float f32x2 __attribute__((ext_vector_type(2)));
typedef float f32x4 __attribute__((ext_vector_type(4)));
typedef float f32x16 __attribute__((ext_vector_type(16)));
typedef short s16x4 __attribute__((ext_vector_type(4)));
typedef __bf16 bf16x2 __attribute__((ext_vector_type(2)));
typedef __bf16 bf16x8 __attribute__((ext_vector_type(8)));
typedef __amdgpu_buffer_rsrc_t rsrc_t;

constexpr int MB = 1 << 20;
constexpr int ZS = 4224, XS = 1152, WS_ = 8320;
constexpr int OFF_Z0 = 0, OFF_Z1 = 9 * MB / 4, OFF_Z2 = 9 * MB / 2, OFF_Z3 = 27 * MB / 4;
constexpr int OFF_WT = 9 * MB;
constexpr int OFF_W1 = 12 * MB;
constexpr int OFF_W2 = 16 * MB;
constexpr int OFF_W3 = 18 * MB;
constexpr int OFF_H1 = 20 * MB;
constexpr int OFF_H2 = 28 * MB;
constexpr int OFF_X3 = 36 * MB;
constexpr int OFF_R0 = 39 * MB;
constexpr int OFF_ST = 39 * MB + 65536;
constexpr int WS_BYTES = 40 * MB;
constexpr int SC1 = 16;
constexpr unsigned SENT = 0xFFFFFFFFu;
constexpr unsigned SPIN_MAX = 1u << 16;

struct Params {
  const u16* x; const u16* wr; const u16* wi; const u16* wg; const u16* wu; const u16* wd;
  u16* out; unsigned char* ws;
};

DI unsigned pk(float a, float b) { f32x2 v = {a, b}; return __builtin_bit_cast(unsigned, __builtin_convertvector(v, bf16x2)); }
DI float asf(unsigned u) { return __builtin_bit_cast(float, u); }
DI unsigned asu(float f) { return __builtin_bit_cast(unsigned, f); }
DI bf16x2 asb2(unsigned u) { return __builtin_bit_cast(bf16x2, u); }
template <int V> struct IC { static constexpr int v = V; };
template <int I, int N, class Fn> DI void sfor(Fn&& fn) { if constexpr (I < N) { fn(IC<I>{}); sfor<I + 1, N>(fn); } }
DI int opaque(int x) { asm volatile("" : "+s"(x)); return x; }
DI float blo(unsigned u) { return __builtin_bit_cast(float, u << 16); }
DI float bhi(unsigned u) { return __builtin_bit_cast(float, u & 0xFFFF0000u); }
DI float rbf(float a) { return blo(pk(a, a)); }
DI unsigned nosent(unsigned u) { return u < 0xFFFFFFFEu ? u : 0xFFFFFFFEu; }
DI v4u nosent4(v4u v) { v.x = nosent(v.x); v.y = nosent(v.y); v.z = nosent(v.z); v.w = nosent(v.w); return v; }
DI unsigned umax(unsigned a, unsigned b) { return a > b ? a : b; }
DI unsigned mx4(v4u v) { return umax(umax(v.x, v.y), umax(v.z, v.w)); }
DI bool any_sent(unsigned m) { return __builtin_amdgcn_ballot_w64(m == SENT) != 0ull; }
DI f32x16 mfma32(v4u a, v4u b, f32x16 c) {
  return __builtin_amdgcn_mfma_f32_32x32x16_bf16(__builtin_bit_cast(bf16x8, a), __builtin_bit_cast(bf16x8, b), c, 0, 0, 0);
}
DI f32x4 mfma16(v4u a, v4u b, f32x4 c) {
  return __builtin_amdgcn_mfma_f32_16x16x32_bf16(__builtin_bit_cast(bf16x8, a), __builtin_bit_cast(bf16x8, b), c, 0, 0, 0);
}
template <bool FIRST, bool WA> DI void mfma_w(f32x16& acc, const v4u& wt, const v4u& x) {
  if constexpr (FIRST && WA) asm volatile("v_mfma_f32_32x32x16_bf16 %0, %1, %2, 0" : "=&v"(acc) : "a"(wt), "v"(x));
  if constexpr (FIRST && !WA) asm volatile("v_mfma_f32_32x32x16_bf16 %0, %2, %1, 0" : "=&v"(acc) : "a"(wt), "v"(x));
  if constexpr (!FIRST && WA) asm volatile("v_mfma_f32_32x32x16_bf16 %0, %1, %2, %0" : "+v"(acc) : "a"(wt), "v"(x));
  if constexpr (!FIRST && !WA) asm volatile("v_mfma_f32_32x32x16_bf16 %0, %2, %1, %0" : "+v"(acc) : "a"(wt), "v"(x));
}
template <bool FIRST> DI void mfma_v(f32x16& acc, const v4u& wt, const v4u& x) {
  if constexpr (FIRST) asm volatile("v_mfma_f32_32x32x16_bf16 %0, %2, %1, 0" : "=&v"(acc) : "v"(wt), "v"(x));
  else asm volatile("v_mfma_f32_32x32x16_bf16 %0, %2, %1, %0" : "+v"(acc) : "v"(wt), "v"(x));
}
template <int IMM> DI void vload(v4u& dst, const v4u& srd, int lane16, int so) {
  asm volatile("buffer_load_dwordx4 %0, %1, %2, %3 offen offset:%4" : "=v"(dst) : "v"(lane16), "s"(srd), "s"(so), "i"(IMM));
}
template <int IMM, bool REUSE> DI void wload(v4u& dst, const v4u& srd, int lane16, int so) {
  if constexpr (REUSE) asm volatile("buffer_load_dwordx4 %0, %1, %2, %3 offen offset:%4" : "+a"(dst) : "v"(lane16), "s"(srd), "s"(so), "i"(IMM));
  else asm volatile("buffer_load_dwordx4 %0, %1, %2, %3 offen offset:%4" : "=a"(dst) : "v"(lane16), "s"(srd), "s"(so), "i"(IMM));
}
DI void mfma_done(f32x16& a, f32x16& b, f32x16& c, f32x16& d) { asm volatile("s_nop 12" : "+v"(a), "+v"(b), "+v"(c), "+v"(d)); }
DI v2u trread(LAS unsigned char* p) { return __builtin_bit_cast(v2u, __builtin_amdgcn_ds_read_tr16_b64_v4i16((LAS s16x4*)p)); }
DI v4u tr2(LAS unsigned char* p, int d) { v2u a = trread(p), b = trread(p + d); return v4u{a.x, a.y, b.x, b.y}; }
DI v4u bload(rsrc_t rs, int voff, int soff) { return __builtin_amdgcn_raw_buffer_load_b128(rs, voff, soff, 0); }
DI v4u bload_sc1(rsrc_t rs, int voff, int soff) { return __builtin_amdgcn_raw_buffer_load_b128(rs, voff, soff, SC1); }
DI void bstore(v4u v, rsrc_t rs, int voff, int soff) { __builtin_amdgcn_raw_buffer_store_b128(v, rs, voff + soff, 0, 0); }
DI void bstore_sc1(v4u v, rsrc_t rs, int voff, int soff) { __builtin_amdgcn_raw_buffer_store_b128(v, rs, voff + soff, 0, SC1); }
template <int I> DI v4u fload(rsrc_t rs, int lane16, int base) { return bload(rs, lane16 + (I & 3) * 1024, base + (I >> 2) * 4096); }
template <int I> DI v4u fload_sc1(rsrc_t rs, int lane16, int base) { return bload_sc1(rs, lane16 + (I & 3) * 1024, base + (I >> 2) * 4096); }
#define SB() __builtin_amdgcn_sched_barrier(0)
#define MEMFENCE() asm volatile("" ::: "memory")
#define BAR() asm volatile("s_waitcnt lgkmcnt(0)\n\ts_barrier" ::: "memory")
template <int N> DI void vmwait() { asm volatile("s_waitcnt vmcnt(%0)" :: "i"(N) : "memory"); }
DI void pin(v4u& a) { asm volatile("" : "+v"(a)); }
DI f32x16 zero16() { f32x16 z; for (int i = 0; i < 16; ++i) z[i] = 0.f; return z; }
DI v4u pack8(const f32x16& a, int s, float sc) {
  return v4u{pk(a[8 * s] * sc, a[8 * s + 1] * sc), pk(a[8 * s + 2] * sc, a[8 * s + 3] * sc),
             pk(a[8 * s + 4] * sc, a[8 * s + 5] * sc), pk(a[8 * s + 6] * sc, a[8 * s + 7] * sc)};
}
DI void widen(const f32x16& a, float sc, v4u& o0, v4u& o1) {
  unsigned d[8];
#pragma unroll
  for (int i = 0; i < 8; ++i) d[i] = pk(a[2 * i] * sc, a[2 * i + 1] * sc);
  auto r0 = __builtin_amdgcn_permlane32_swap(d[0], d[2], false, false);
  auto r1 = __builtin_amdgcn_permlane32_swap(d[1], d[3], false, false);
  auto r2 = __builtin_amdgcn_permlane32_swap(d[4], d[6], false, false);
  auto r3 = __builtin_amdgcn_permlane32_swap(d[5], d[7], false, false);
  o0 = v4u{r0[0], r1[0], r0[1], r1[1]};
  o1 = v4u{r2[0], r3[0], r2[1], r3[1]};
}
template <int CTRL> DI float dppf(float v) {
  return __builtin_bit_cast(float, __builtin_amdgcn_update_dpp(0, __builtin_bit_cast(int, v), CTRL, 0xF, 0xF, true));
}
DI float sum32(float v) {
  v += dppf<0xB1>(v); v += dppf<0x4E>(v); v += dppf<0x141>(v); v += dppf<0x140>(v);
  unsigned u = __builtin_bit_cast(unsigned, v);
  auto r = __builtin_amdgcn_permlane16_swap(u, u, false, false);
  return __builtin_bit_cast(float, (unsigned)r[0]) + __builtin_bit_cast(float, (unsigned)r[1]);
}
DI int stat_of(int p) { return ((p >> 2) & 1) * 4096 + (p >> 3) * 16 + (p & 3) * 4; }
DI int token_of(int p) {
  const int l = (p >> 3) & 63, e = p & 7;
  return 32 * (16 * (p >> 9) + 4 * (l >> 4) + (e & 3)) + (l & 15) + 16 * (e >> 2);
}
#define GLDS_POLICY ""
DI void glds16(const void* sbase, unsigned voff, unsigned lds) {
  unsigned keep;
  asm volatile("s_mov_b32 %0, m0\n\ts_mov_b32 m0, %3\n\ts_nop 0\n\tglobal_load_lds_dwordx4 %1, %2" GLDS_POLICY "\n\ts_mov_b32 m0, %0"
               : "=&s"(keep) : "v"(voff), "s"(sbase), "s"(lds) : "memory");
}
template <int IMM> DI void ld2u16(unsigned& a, unsigned& b, int voff, const v4u& sa, const v4u& sb, int soff) {
  asm volatile("buffer_load_ushort %0, %2, %3, %5 offen offset:%6\n\tbuffer_load_ushort %1, %2, %4, %5 offen offset:%6"
               : "=&v"(a), "=&v"(b) : "v"(voff), "s"(sa), "s"(sb), "s"(soff), "i"(IMM));
}
DI v4u cosfrag(float a0, float step, int off) {
  float v[8];
#pragma unroll
  for (int j = 0; j < 8; ++j) v[j] = __builtin_amdgcn_cosf(__builtin_fmaf(step, (float)(off + j), a0));
  return v4u{pk(v[0], v[1]), pk(v[2], v[3]), pk(v[4], v[5]), pk(v[6], v[7])};
}

constexpr int P_XL = 0, P_GL = 33280, P_UL = P_GL + 32768 + 32, P_DL = P_UL + 32768 + 32, P_ZB = P_DL + 33280, P_WB = P_ZB + 4 * 2560,
              P_LDS = P_WB + 64 * 144;

__global__ void __launch_bounds__(512, 2) k_prep(Params p) {
  extern __shared__ __attribute__((aligned(16))) unsigned char smem[];
  LAS unsigned char* L = (LAS unsigned char*)smem;
  const unsigned Lb = (unsigned)(size_t)L;
  const int tid = threadIdx.x, lane = tid & 63, w8 = __builtin_amdgcn_readfirstlane(tid >> 6), w = w8 & 3;
  const int b = blockIdx.x, xcd = b & 7, q = (b >> 3) & 3, tgi = b >> 5;
  const int t1 = xcd * 8 + tgi, tg = tgi * 8 + xcd;
  const bool heavy = tgi < 7;
  const int h = lane >> 5, r = lane & 31, ft = 4 * q + w;
  const rsrc_t rs = __builtin_amdgcn_make_buffer_rsrc((void*)p.ws, 0, WS_BYTES, 0x00020000);
#define PST(i)
  PST(0);
  if (w8 >= 4) {
#pragma unroll
    for (int i = 0; i < 8; ++i)
      glds16(p.x + (size_t)token_of(32 * t1 + 8 * w + i) * 512, lane * 16, Lb + P_XL + (8 * w + i) * 1040);
    if (heavy) {
      const unsigned vo = (lane >> 2) * 3584 + (lane & 3) * 16;
#pragma unroll
      for (int i = 0; i < 8; ++i) {
        glds16(p.wg + (size_t)(8 * w + i) * 16 * 1792 + 32 * tg, vo, Lb + P_GL + (8 * w + i) * 1024);
        glds16(p.wu + (size_t)(8 * w + i) * 16 * 1792 + 32 * tg, vo, Lb + P_UL + (8 * w + i) * 1024);
      }
      vmwait<16>();
    } else vmwait<0>();
    PST(1);
    BAR();
    if (heavy) {
#pragma unroll
      for (int i = 0; i < 8; ++i) glds16(p.wd + (size_t)(32 * tg + 8 * w + i) * 512, lane * 16, Lb + P_DL + (8 * w + i) * 1040);
    }
    const int kk0 = 32 * tg + 8 * w;
    unsigned fr[8], fi[8];
    {
      const v4u sr = {(unsigned)(size_t)p.wr, (unsigned)((size_t)p.wr >> 32) & 0xFFFFu, 2048u * 514u, 0x00020000u};
      const v4u si = {(unsigned)(size_t)p.wi, (unsigned)((size_t)p.wi >> 32) & 0xFFFFu, 2048u * 514u, 0x00020000u};
      const int wc = 64 * q + lane, src = (wc == 0 ? 256 : wc) * 2;
      sfor<0, 8>([&](auto I) { ld2u16<I.v * 514>(fr[I.v], fi[I.v], src, sr, si, kk0 * 514); });
    }
    PST(2);
    {
      const int row = 8 * q + 2 * w + h;
      const v4u u0 = *(LAS v4u*)(L + P_XL + row * 1040 + r * 32), u1 = *(LAS v4u*)(L + P_XL + row * 1040 + r * 32 + 16);
      float v[16];
#pragma unroll
      for (int i = 0; i < 4; ++i) { v[2 * i] = blo(u0[i]); v[2 * i + 1] = bhi(u0[i]); v[8 + 2 * i] = blo(u1[i]); v[9 + 2 * i] = bhi(u1[i]); }
      float s = 0.f;
#pragma unroll
      for (int i = 0; i < 16; ++i) s += v[i];
      const float m = sum32(s) * (1.f / 512.f);
      float ss = 0.f;
#pragma unroll
      for (int i = 0; i < 16; ++i) { const float d = v[i] - m; ss = __builtin_fmaf(d, d, ss); }
      ss = sum32(ss);
      if (r == 0) __builtin_amdgcn_raw_buffer_store_b32(nosent(asu(ss)), rs, stat_of(32 * t1 + row), OFF_R0, 0);
    }
    PST(3);
    vmwait<24>();
    PST(4);
    BAR();
    {
      const v4u sv = {SENT, SENT, SENT, SENT};
      const int t16 = (w * 64 + lane) * 16;
#pragma unroll
      for (int i = 0; i < 4; ++i) bstore_sc1(sv, rs, t16, (i < 2 ? OFF_Z1 : OFF_Z2) + (2 * b + (i & 1)) * ZS);
#pragma unroll
      for (int i = 0; i < 7; ++i) bstore_sc1(sv, rs, t16, OFF_H1 + b * 28672 + i * 4096);
      if (t16 < 256) bstore_sc1(sv, rs, t16, OFF_ST + b * 256);
    }
    PST(5);
    asm volatile("s_waitcnt vmcnt(11)"
                 : "+v"(fr[0]), "+v"(fr[1]), "+v"(fr[2]), "+v"(fr[3]), "+v"(fr[4]), "+v"(fr[5]), "+v"(fr[6]), "+v"(fr[7]),
                   "+v"(fi[0]), "+v"(fi[1]), "+v"(fi[2]), "+v"(fi[3]), "+v"(fi[4]), "+v"(fi[5]), "+v"(fi[6]), "+v"(fi[7]) :: "memory");
    PST(6);
    {
      v4u o0, o1;
#pragma unroll
      for (int i = 0; i < 4; ++i) { o0[i] = fr[i] | (fi[i] << 16); o1[i] = fr[4 + i] | (fi[4 + i] << 16); }
      *(LAS v4u*)(L + P_WB + lane * 144 + w * 32) = o0;
      *(LAS v4u*)(L + P_WB + lane * 144 + w * 32 + 16) = o1;
    }
    BAR();
#pragma unroll
    for (int i = 0; i < 2; ++i) {
      const int col = 16 * w + 8 * i + (lane >> 3);
      bstore_sc1(*(LAS v4u*)(L + P_WB + col * 144 + (lane & 7) * 16), rs, (64 * q + col) * WS_ + (lane & 7) * 16, OFF_WT + tg * 128);
    }
  } else {
    const int feat = 32 * ft + r;
    const float step = (feat == 0 ? 0.f : feat == 1 ? 256.f : (float)(feat >> 1)) * (1.f / 512.f);
    const float ph = (feat > 1 && (feat & 1)) ? 0.25f : 0.f;
    const float a0 = ph + step * (float)(8 * h);
    const unsigned sgn = (feat > 1 && (feat & 2)) ? 0x80008000u : 0u;
    v4u F[32];
#pragma unroll
    for (int ks = 0; ks < 16; ++ks) { F[ks] = cosfrag(a0, step, 16 * ks); F[ks + 16] = F[ks] ^ sgn; }
    const float lsc = feat < 2 ? 0.04419417382415922f : 0.0625f;
#pragma unroll
    for (int ks = 0; ks < 32; ks += 8)
      asm volatile("" : "+v"(F[ks]), "+v"(F[ks + 1]), "+v"(F[ks + 2]), "+v"(F[ks + 3]), "+v"(F[ks + 4]), "+v"(F[ks + 5]), "+v"(F[ks + 6]), "+v"(F[ks + 7]));
    PST(1);
    BAR();
    PST(2);
    {
      f32x16 aZ = zero16();
      LAS unsigned char* xa = L + P_XL + r * 1040 + h * 16;
      v4u a[4];
#pragma unroll
      for (int i = 0; i < 4; ++i) a[i] = *(LAS v4u*)(xa + i * 32);
      sfor<0, 32>([&](auto KS) {
        constexpr int ks = KS.v;
        const v4u cur = a[ks & 3];
        if constexpr (ks + 4 < 32) a[ks & 3] = *(LAS v4u*)(xa + (ks + 4) * 32);
        aZ = mfma32(cur, F[ks], aZ);
        SB();
      });
      v4u o0, o1;
      widen(aZ, lsc, o0, o1);
      LAS unsigned char* zb = L + P_ZB + w * 2560;
      *(LAS v4u*)(zb + r * 80 + h * 16) = nosent4(o0);
      *(LAS v4u*)(zb + r * 80 + h * 16 + 32) = nosent4(o1);
#pragma unroll
      for (int i = 0; i < 2; ++i)
        bstore_sc1(*(LAS v4u*)(zb + (16 * i + (lane >> 2)) * 80 + (lane & 3) * 16), rs, (32 * ft + 16 * i + (lane >> 2)) * ZS + (lane & 3) * 16,
                   OFF_Z0 + t1 * 64);
    }
    v4u I[2];
#pragma unroll
    for (int s = 0; s < 2; ++s) {
      const int e = r - 16 * s - 8 * h;
      const unsigned one = (e & 1) ? 0x3F800000u : 0x00003F80u;
      I[s] = v4u{(e >> 1) == 0 ? one : 0u, (e >> 1) == 1 ? one : 0u, (e >> 1) == 2 ? one : 0u, (e >> 1) == 3 ? one : 0u};
    }
    pin(I[0]); pin(I[1]);
    PST(3);
    BAR();
    PST(4);
    if (heavy) {
      f32x16 aG0 = zero16(), aG1 = zero16();
      LAS unsigned char* ba = L + (((lane >> 4) & 1) ? P_UL : P_GL) + (8 * h + ((lane & 15) >> 2)) * 64 + (lane & 3) * 8;
      v4u b0[2], b1[2];
#pragma unroll
      for (int i = 0; i < 2; ++i) { b0[i] = tr2(ba + i * 1024, 256); b1[i] = tr2(ba + i * 1024 + 32, 256); }
      sfor<0, 32>([&](auto KS) {
        constexpr int ks = KS.v;
        const v4u c0 = b0[ks & 1], c1 = b1[ks & 1];
        aG0 = mfma32(F[ks], c0, aG0);
        if constexpr (ks + 2 < 32) b0[ks & 1] = tr2(ba + (ks + 2) * 1024, 256);
        SB();
        aG1 = mfma32(F[ks], c1, aG1);
        if constexpr (ks + 2 < 32) b1[ks & 1] = tr2(ba + (ks + 2) * 1024 + 32, 256);
        SB();
      });
      PST(5);
      BAR();
      PST(6);
      if (ft == 0 && h == 0) { aG0[0] = 0.f; aG1[0] = 0.f; aG0[1] *= 0.70710678118654752f; aG1[1] *= 0.70710678118654752f; }
      const int g0 = 2 * tg, g1 = 2 * tg + 1;
      const int o0 = OFF_W1 + ((g0 / 7) * 8 + g0 % 7) * 32768 + 2 * ft * 1024, o1 = OFF_W1 + ((g1 / 7) * 8 + g1 % 7) * 32768 + 2 * ft * 1024;
      f32x16 aD = zero16(), aT = zero16();
      LAS unsigned char* da = L + P_DL + r * 1040 + h * 16;
      aT = mfma32(*(LAS v4u*)(da + ft * 64), I[0], aT);
      aT = mfma32(*(LAS v4u*)(da + ft * 64 + 32), I[1], aT);
      v4u a[4];
#pragma unroll
      for (int i = 0; i < 4; ++i) a[i] = *(LAS v4u*)(da + i * 32);
      sfor<0, 32>([&](auto KS) {
        constexpr int ks = KS.v;
        const v4u cur = a[ks & 3];
        if constexpr (ks + 4 < 32) a[ks & 3] = *(LAS v4u*)(da + (ks + 4) * 32);
        aD = mfma32(cur, F[ks], aD);
        if constexpr (ks == 1 || ks == 6) bstore_sc1(pack8(aG0, ks > 1, 0.0625f), rs, lane * 16, o0 + (ks > 1) * 1024);
        if constexpr (ks == 11 || ks == 16) bstore_sc1(pack8(aG1, ks > 11, 0.0625f), rs, lane * 16, o1 + (ks > 11) * 1024);
        if constexpr (ks == 21 || ks == 26) bstore_sc1(pack8(aT, ks > 21, 1.f), rs, lane * 16, OFF_W3 + (ft * 112 + 2 * tg + (ks > 21)) * 1024);
        SB();
      });
#pragma unroll
      for (int s = 0; s < 2; ++s) bstore_sc1(pack8(aD, s, lsc), rs, lane * 16, OFF_W2 + (ft * 112 + 2 * tg + s) * 1024);
    } else {
#pragma unroll
      for (int i = 0; i < 2; ++i) {
        const int dt = 2 * xcd + i;
        const float b0 = __builtin_amdgcn_fractf(__builtin_fmaf(step, (float)(32 * dt), a0));
        f32x16 aI = mfma32(cosfrag(b0, step, 0), I[0], zero16());
        aI = mfma32(cosfrag(b0, step, 16), I[1], aI);
        if (ft == 0 && h == 0) { aI[0] *= 0.70710678118654752f; aI[1] *= 0.70710678118654752f; }
#pragma unroll
        for (int s = 0; s < 2; ++s) bstore_sc1(pack8(aI, s, 0.0625f), rs, lane * 16, OFF_W1 + (dt * 8 + 7) * 32768 + (2 * ft + s) * 1024);
      }
      BAR();
    }
  }
}

constexpr int M_Z = 0;
constexpr int M_RB = 40960;
constexpr int M_TB = 81920;
constexpr int M_S1 = 131072;
constexpr int M_S2 = M_S1 + 2048;
constexpr int M_C1 = M_S2 + 1024;
constexpr int M_C2 = M_C1 + 2 * 4608;
constexpr int M_LDS = M_C2 + 2 * 4608;
#define STAMP(i)
#define KSTAMP(i)
#define CSTAMP(i)
#define RETRY(i)
#define DELAY_A 6
#define DELAY_C 2

template <int IMM> DI void ldx4(v4u& dst, const v4u& srd, int voff, int soff) {
  asm volatile("buffer_load_dwordx4 %0, %1, %2, %3 offen offset:%4 sc1" : "=v"(dst) : "v"(voff), "s"(srd), "s"(soff), "i"(IMM));
}
struct WSrc { v4u srd, srd1; int lane16, base; };
template <int N> DI void load_w(v4u (&W)[64], const WSrc& s) {
  wload<((N >> 1) & 3) * 1024, false>(W[N], (N & 1) ? s.srd1 : s.srd, s.lane16, s.base + (N & 1) * 32768 + (N >> 3) * 4096);
}
template <int LO, int HI> DI void load_ws(v4u (&W)[64], const WSrc& s) { SB(); sfor<LO, HI>([&](auto J) { load_w<J.v>(W, s); }); SB(); }
DI unsigned mxacc(unsigned m, const v4u& v) { return umax(umax(umax(umax(m, v.x), v.y), v.z), v.w); }
DI unsigned long long sent_mask(unsigned m) { return __builtin_amdgcn_ballot_w64(m == SENT); }
DI void pins(unsigned long long& k) { asm volatile("" : "+s"(k)); }
DI void pinf(float& a) { asm volatile("" : "+v"(a)); }
DI void pinu(unsigned& a) { asm volatile("" : "+v"(a)); }

struct ColConst { v4u C1[2], S1[2], N1[2], C2[2], S2[2], N2[2]; float tc[8], ts[8]; };

DI void col_consts(ColConst& k, int w, int lane, v4u (&W)[64], const WSrc& ws) {
  const int g = lane >> 4, r = lane & 15;
  const float k1 = (float)(16 * w + r);
  const unsigned neg = 0x80008000u;
  sfor<0, 2>([&](auto CS) {
    constexpr int cs = CS.v;
    const float st = k1 * (1.f / 64.f), a0 = st * (float)(8 * g);
    load_ws<3 * cs, 3 * cs + 2>(W, ws);
    k.C1[cs] = cosfrag(a0, st, 32 * cs);
    pin(k.C1[cs]);
    load_ws<3 * cs + 2, 3 * cs + 3>(W, ws);
    k.S1[cs] = cosfrag(a0 - 0.25f, st, 32 * cs);
    k.N1[cs] = k.S1[cs] ^ neg;
    pin(k.S1[cs]); pin(k.N1[cs]);
  });
  sfor<0, 2>([&](auto T) {
    constexpr int t = T.v;
    const float st = (float)(16 * t + r) * (1.f / 32.f), a0 = st * (float)(4 * g);
    float c[8], s[8];
    load_ws<6 + 3 * t, 8 + 3 * t>(W, ws);
#pragma unroll
    for (int j = 0; j < 8; ++j) c[j] = __builtin_amdgcn_cosf(__builtin_fmaf(st, (float)(16 * (j >> 2) + (j & 3)), a0));
    k.C2[t] = v4u{pk(c[0], c[1]), pk(c[2], c[3]), pk(c[4], c[5]), pk(c[6], c[7])};
    pin(k.C2[t]);
    load_ws<8 + 3 * t, 9 + 3 * t>(W, ws);
#pragma unroll
    for (int j = 0; j < 8; ++j) s[j] = __builtin_amdgcn_sinf(__builtin_fmaf(st, (float)(16 * (j >> 2) + (j & 3)), a0));
    k.S2[t] = v4u{pk(s[0], s[1]), pk(s[2], s[3]), pk(s[4], s[5]), pk(s[6], s[7])};
    k.N2[t] = k.S2[t] ^ neg;
    pin(k.S2[t]); pin(k.N2[t]);
  });
  sfor<0, 2>([&](auto T) {
    constexpr int t = T.v;
    load_ws<12 + 2 * t, 14 + 2 * t>(W, ws);
#pragma unroll
    for (int e = 4 * t; e < 4 * t + 4; ++e) {
      const float a = k1 * (1.f / 2048.f) * (float)(16 * (e >> 2) + 4 * g + (e & 3));
      k.tc[e] = __builtin_amdgcn_cosf(a); k.ts[e] = __builtin_amdgcn_sinf(a);
      pinf(k.tc[e]); pinf(k.ts[e]);
    }
  });
}

template <bool FIRST> DI void m16(f32x4& acc, const v4u& a, const v4u& b) {
  SB();
  if constexpr (FIRST) asm volatile("v_mfma_f32_16x16x32_bf16 %0, %1, %2, 0" : "=&v"(acc) : "v"(a), "v"(b));
  else asm volatile("v_mfma_f32_16x16x32_bf16 %0, %1, %2, %0" : "+v"(acc) : "v"(a), "v"(b));
  SB();
}
DI void m16_done(f32x4& a, f32x4& b) { asm volatile("s_nop 7" : "+v"(a), "+v"(b)); }
DI void m16_ready(v4u& a, v4u& b) { asm volatile("s_nop 1" : "+v"(a), "+v"(b)); }
template <int PART> DI float cmul(float a, float b, float c, float s) {
  float r = PART == 0 ? __builtin_fmaf(a, c, -(b * s)) : __builtin_fmaf(a, s, b * c);
  pinf(r);
  return r;
}

template <int WB>
DI void col_compute(LAS unsigned char* L, rsrc_t rs, const ColConst& k, const float (&fr)[8], const float (&fi)[8], v4u zr, v4u zi,
                    const float (&rstd)[8], int c, int w, int lane, int out_off, v4u (&W)[64], const WSrc& ws) {
  const int g = lane >> 4, r = lane & 15;
  const int first = c == 0;
  const float f0 = first ? 0.f : 1.f;
  float xr[8], xi[8];
#pragma unroll
  for (int i = 0; i < 4; ++i) { xr[2 * i] = blo(zr[i]); xr[2 * i + 1] = bhi(zr[i]); xi[2 * i] = blo(zi[i]); xi[2 * i + 1] = bhi(zi[i]); }
#pragma unroll
  for (int t = 0; t < 2; ++t) {
    const f32x4 rv = {rstd[4 * t], rstd[4 * t + 1], rstd[4 * t + 2], rstd[4 * t + 3]};
    const f32x4 a = f32x4{xr[4 * t], xr[4 * t + 1], xr[4 * t + 2], xr[4 * t + 3]} * rv;
    const f32x4 bq = f32x4{xi[4 * t], xi[4 * t + 1], xi[4 * t + 2], xi[4 * t + 3]} * (rv * f0);
    LAS unsigned char* d = L + M_C1 + (r + 16 * t) * 144 + (16 * w + 4 * g) * 2;
    *(LAS v2u*)d = v2u{pk(a[0], a[1]), pk(a[2], a[3])};
    *(LAS v2u*)(d + 4608) = v2u{pk(bq[0], bq[1]), pk(bq[2], bq[3])};
  }
  load_ws<WB, WB + 3>(W, ws);
  BAR();
  f32x4 dr[2], di[2];
  float ur[8], ui[8];
  {
    v4u ar[2][2], ai[2][2];
#pragma unroll
    for (int t = 0; t < 2; ++t)
#pragma unroll
      for (int cs = 0; cs < 2; ++cs) {
        LAS unsigned char* s = L + M_C1 + (r + 16 * t) * 144 + (32 * cs + 8 * g) * 2;
        ar[t][cs] = *(LAS v4u*)s; ai[t][cs] = *(LAS v4u*)(s + 4608);
      }
    load_ws<WB + 3, WB + 5>(W, ws);
    sfor<0, 16>([&](auto N) {
      constexpr int n = N.v, t = n >> 3, cs = (n >> 2) & 1, m = n & 3;
      if constexpr (m == 0) m16<cs == 0>(dr[t], ar[t][cs], k.C1[cs]);
      if constexpr (m == 1) m16<cs == 0>(di[t], ai[t][cs], k.C1[cs]);
      if constexpr (m == 2) m16<false>(dr[t], ai[t][cs], k.S1[cs]);
      if constexpr (m == 3) m16<false>(di[t], ar[t][cs], k.N1[cs]);
      if constexpr (m == 3) load_w<WB + 5 + (n >> 2)>(W, ws);
      if constexpr (n >= 9) {
        constexpr int e = (n - 9) >> 1;
        if constexpr (n & 1) ur[e] = cmul<0>(dr[0][e], di[0][e], k.tc[e], -k.ts[e]);
        else ui[e] = cmul<1>(dr[0][e], di[0][e], k.tc[e], -k.ts[e]);
      }
    });
    ui[3] = cmul<1>(dr[0][3], di[0][3], k.tc[3], -k.ts[3]);
  }
  v4u br, bi;
  br.x = pk(ur[0], ur[1]); br.y = pk(ur[2], ur[3]); bi.x = pk(ui[0], ui[1]); bi.y = pk(ui[2], ui[3]);
  SB();
  m16_done(dr[1], di[1]);
  load_ws<WB + 9, WB + 11>(W, ws);
#pragma unroll
  for (int e = 4; e < 8; ++e) {
    ur[e] = dr[1][e - 4] * k.tc[e] + di[1][e - 4] * k.ts[e]; ui[e] = di[1][e - 4] * k.tc[e] - dr[1][e - 4] * k.ts[e];
  }
  br.z = pk(ur[4], ur[5]); br.w = pk(ur[6], ur[7]); bi.z = pk(ui[4], ui[5]); bi.w = pk(ui[6], ui[7]);
  m16_ready(br, bi);
  sfor<0, 8>([&](auto N) {
    constexpr int n = N.v, t = n >> 2, m = n & 3;
    if constexpr (m == 0) m16<true>(dr[t], k.C2[t], br);
    if constexpr (m == 1) m16<true>(di[t], k.C2[t], bi);
    if constexpr (m == 2) m16<false>(dr[t], k.S2[t], bi);
    if constexpr (m == 3) m16<false>(di[t], k.N2[t], br);
    if constexpr (m == 3) load_w<WB + 11 + t>(W, ws);
    if constexpr (n == 5) ur[0] = cmul<0>(dr[0][0], di[0][0], fr[0], fi[0]);
    if constexpr (n == 6) ui[0] = cmul<1>(dr[0][0], di[0][0], fr[0], fi[0]);
  });
#pragma unroll
  for (int e = 1; e < 4; ++e) { ur[e] = dr[0][e] * fr[e] - di[0][e] * fi[e]; ui[e] = dr[0][e] * fi[e] + di[0][e] * fr[e]; }
  br.x = pk(ur[0], ur[1]); br.y = pk(ur[2], ur[3]); bi.x = pk(ui[0], ui[1]); bi.y = pk(ui[2], ui[3]);
  SB();
  m16_done(dr[1], di[1]);
  load_ws<WB + 13, WB + 15>(W, ws);
#pragma unroll
  for (int e = 4; e < 8; ++e) { ur[e] = dr[1][e - 4] * fr[e] - di[1][e - 4] * fi[e]; ui[e] = dr[1][e - 4] * fi[e] + di[1][e - 4] * fr[e]; }
  br.z = pk(ur[4], ur[5]); br.w = pk(ur[6], ur[7]); bi.z = pk(ui[4], ui[5]); bi.w = pk(ui[6], ui[7]);
  m16_ready(br, bi);
  sfor<0, 8>([&](auto N) {
    constexpr int n = N.v, t = n >> 2, m = n & 3;
    if constexpr (m == 0) m16<true>(dr[t], k.C2[t], br);
    if constexpr (m == 1) m16<true>(di[t], k.C2[t], bi);
    if constexpr (m == 2) m16<false>(dr[t], k.N2[t], bi);
    if constexpr (m == 3) m16<false>(di[t], k.S2[t], br);
    if constexpr (m == 3) load_w<WB + 15 + t>(W, ws);
    if constexpr (n == 5) ur[0] = cmul<0>(dr[0][0], di[0][0], k.tc[0], k.ts[0]);
    if constexpr (n == 6) ui[0] = cmul<1>(dr[0][0], di[0][0], k.tc[0], k.ts[0]);
  });
#pragma unroll
  for (int t = 0; t < 2; ++t) {
    if (t == 1) { SB(); m16_done(dr[1], di[1]); load_ws<WB + 17, WB + 19>(W, ws); }
#pragma unroll
    for (int i = (t == 0 ? 1 : 0); i < 4; ++i) {
      const int e = 4 * t + i;
      ur[e] = dr[t][i] * k.tc[e] - di[t][i] * k.ts[e]; ui[e] = di[t][i] * k.tc[e] + dr[t][i] * k.ts[e];
    }
    LAS unsigned char* d = L + M_C2 + (16 * w + r) * 72 + (16 * t + 4 * g) * 2;
    *(LAS v2u*)d = v2u{pk(ur[4 * t], ur[4 * t + 1]), pk(ur[4 * t + 2], ur[4 * t + 3])};
    *(LAS v2u*)(d + 4608) = v2u{pk(ui[4 * t], ui[4 * t + 1]), pk(ui[4 * t + 2], ui[4 * t + 3])};
  }
  load_ws<WB + 19, WB + 21>(W, ws);
  BAR();
  v4u o0, o1;
  {
    v4u tr_[2][2], ti_[2][2];
#pragma unroll
    for (int t = 0; t < 2; ++t)
#pragma unroll
      for (int cs = 0; cs < 2; ++cs) {
        LAS unsigned char* s = L + M_C2 + (32 * cs + 8 * g + (r >> 2)) * 72 + (16 * t + 4 * (r & 3)) * 2;
        tr_[t][cs] = tr2(s, 4 * 72); ti_[t][cs] = tr2(s + 4608, 4 * 72);
      }
    load_ws<WB + 21, WB + 23>(W, ws);
    sfor<0, 16>([&](auto N) {
      constexpr int n = N.v, t = n >> 3, cs = (n >> 2) & 1, m = n & 3;
      if constexpr (m == 0) m16<cs == 0>(dr[t], k.C1[cs], tr_[t][cs]);
      if constexpr (m == 1) m16<cs == 0>(di[t], k.C1[cs], ti_[t][cs]);
      if constexpr (m == 2) m16<false>(dr[t], k.N1[cs], ti_[t][cs]);
      if constexpr (m == 3) m16<false>(di[t], k.S1[cs], tr_[t][cs]);
      if constexpr (m == 3) load_w<WB + 23 + (n >> 2)>(W, ws);
      if constexpr (n >= 9 && n < 13) {
        constexpr int e = n - 9;
        xr[e] += dr[0][e]; xi[e] = __builtin_fmaf(di[0][e], f0, xi[e]);
        pinf(xr[e]); pinf(xi[e]);
      }
      if constexpr (n == 13) { o0.x = nosent(pk(xr[0], xr[1])); pinu(*(unsigned*)&o0); }
      if constexpr (n == 14) { o0.y = nosent(pk(xr[2], xr[3])); o1.x = nosent(pk(xi[0], xi[1])); }
    });
    o1.y = nosent(pk(xi[2], xi[3]));
    SB();
    m16_done(dr[1], di[1]);
#pragma unroll
    for (int e = 4; e < 8; ++e) { xr[e] += dr[1][e - 4]; xi[e] = __builtin_fmaf(di[1][e - 4], f0, xi[e]); }
    o0.z = nosent(pk(xr[4], xr[5])); o0.w = nosent(pk(xr[6], xr[7])); o1.z = nosent(pk(xi[4], xi[5])); o1.w = nosent(pk(xi[6], xi[7]));
  }
  const int vo = (w * 64 + lane) * 16;
  bstore_sc1(o0, rs, vo, out_off + (2 * c + first) * ZS);
  bstore_sc1(o1, rs, vo, out_off + (2 * c + 1 - first) * ZS);
  load_ws<WB + 27, WB + 30>(W, ws);
}

constexpr int NW_LATE = 8;
constexpr int NV_EARLY = 16;
constexpr int ZG[6] = {0, 2, 4, 8, 16, 32};
constexpr int ZR[5] = {-1, 1, 4, 10, 21};
constexpr int z_read_at(int ks) {
  for (int G = 1; G < 5; ++G) if (ks >= ZR[G] && 2 * (ks - ZR[G]) < ZG[G + 1] - ZG[G]) return ZG[G] + 2 * (ks - ZR[G]);
  return -1;
}
constexpr int z_first(int j) { for (int G = 0; G < 5; ++G) if (ZG[G] == j) return 1; return 0; }
constexpr int z_decide_at(int ks) { for (int G = 1; G < 5; ++G) if (ks == ZR[G] + (ZG[G + 1] - ZG[G]) / 2) return G; return -1; }
constexpr int z_requested(int ks) { return ks < 8 ? 9 + ks : ks < 16 ? 2 * ks + 1 : 32; }
constexpr int z_younger(int j, int ks) {
  int n = z_requested(ks) - j - 1;
  for (int k = 0; k < ks; ++k) if ((k < 8 && j < 16 && k >= j - 8) || k >= 16) ++n;
  return n;
}
struct ZSrc { v4u srd; int vo, so; unsigned lds; };
template <int J> DI void z_issue(const ZSrc& z, int so) {
  asm volatile("s_add_u32 m0, %0, %4\n\ts_nop 0\n\tbuffer_load_dwordx4 %1, %2, %3 offen sc1 lds"
               :: "s"(z.lds), "v"(z.vo), "s"(z.srd), "s"(so + (J & 15) * 32 * ZS + (J >> 4) * 128), "i"((J >> 4) * 65536 + (J & 15) * 4096)
               : "memory", "scc");
}
template <int J> DI v4u z_back(LAS unsigned char* zown) { return *(LAS v4u*)(zown + (J >> 4) * 65536 + (J & 15) * 4096); }
template <int G, int KS> DI void z_fix(unsigned long long bad, const ZSrc& z, LAS unsigned char* zown, LAS unsigned char* L, int layer, int w) {
  if (__builtin_expect(bad != 0ull, 0)) {
    unsigned spin = 0, m;
    vmwait<0>();
    do {
      RETRY(G == 0 ? 24 : 25);
      const int zo = opaque(z.so);
      sfor<ZG[G], z_requested(KS)>([&](auto I) { z_issue<I.v>(z, zo); });
      vmwait<0>();
      m = 0;
      sfor<ZG[G], ZG[G + 1]>([&](auto I) { m = mxacc(m, z_back<I.v>(zown)); });
    } while (any_sent(m) && ++spin < SPIN_MAX);
  }
}

#define HA_START 14
#define HB_START 6
#define H_ROOM 16
constexpr int NSTR = 56 - NV_EARLY + 58;
struct Stream { int kind[NSTR], idx[NSTR], end[89]; };
constexpr Stream make_stream() {
  Stream s{};
  int kind[NSTR] = {}, idx[NSTR] = {}, rel[NSTR] = {}, need[NSTR] = {};
  bool done[NSTR] = {};
  int n = 0;
  for (int i = NV_EARLY; i < 56; ++i) { kind[n] = 0; idx[n] = i; rel[n] = (i - NV_EARLY) >> 1; need[n] = i >> 1; ++n; }
  for (int i = 0; i < 28; ++i) { kind[n] = 1; idx[n] = i; rel[n] = HA_START; need[n] = i; ++n; }
  for (int i = 0; i < 28; ++i) { kind[n] = 2; idx[n] = i; rel[n] = 33 + HB_START; need[n] = 28 + i; ++n; }
  for (int i = 0; i < 2; ++i) { kind[n] = 3; idx[n] = i; rel[n] = 33 + HB_START; need[n] = 60; ++n; }
  int e = 0, held = 0;
  for (int sl = 0; sl < 89; ++sl) {
    int cap = sl < 8 ? 1 : sl < 32 ? 2 - (sl % 4 == 3) : sl == 32 ? 6 : (sl - 33) % 7 == 6 ? 0 : 2;
    for (; cap > 0; --cap) {
      int best = -1;
      const int live = held - (sl < 33 ? 0 : sl - 32), room = sl < 32 ? H_ROOM : H_ROOM + 6;
      for (int i = 0; i < NSTR; ++i)
        if (!done[i] && rel[i] <= sl && (kind[i] == 0 || live < room) && (best < 0 || need[i] < need[best])) best = i;
      if (best < 0) break;
      if (kind[best] != 0) ++held;
      done[best] = true; s.kind[e] = kind[best]; s.idx[e] = idx[best]; ++e;
    }
    s.end[sl] = e;
  }
  return s;
}
constexpr Stream STR = make_stream();
static_assert(STR.end[88] == NSTR, "every load is issued");
constexpr int str_pos(int kind, int idx) { for (int i = 0; i < NSTR; ++i) if (STR.kind[i] == kind && STR.idx[i] == idx) return i; return -1; }
constexpr int str_count(int kind, int upto) { int n = 0; for (int i = 0; i < upto; ++i) if (STR.kind[i] == kind) ++n; return n; }
struct Down {
  int vbase, hbase, xvo, xso;
  DI int blk(int g) const { return g * 4096; }
};
template <int SLOT, int PART> DI void stream_issue(v4u (&W)[64], v4u (&hA)[28], v4u (&hB)[28], v4u (&x3)[2], const v4u& srd, int lane16, const Down& d) {
  constexpr int lo = SLOT == 0 ? 0 : STR.end[SLOT - 1], hi = STR.end[SLOT];
  sfor<(PART == 1 ? lo + 1 : lo), (PART == 0 && hi > lo ? lo + 1 : hi)>([&](auto I) {
    constexpr int k = STR.kind[I.v], i = STR.idx[I.v];
    if constexpr (k == 0) wload<((i >> 1) & 3) * 1024, true>(W[i - NV_EARLY], srd, lane16, d.vbase + (i & 1) * 112 * 1024 + d.blk(i >> 3));
    if constexpr (k == 1) ldx4<(i & 3) * 1024>(hA[i], srd, lane16, d.hbase + d.blk(i >> 2));
    if constexpr (k == 2) ldx4<(i & 3) * 1024>(hB[i], srd, lane16, d.hbase + 224 * 1024 + d.blk(i >> 2));
    if constexpr (k == 3) ldx4<0>(x3[i], srd, d.xvo, d.xso + i * 64 * XS);
  });
}
constexpr int dn_need(int g) {
  const int H = g / 28, i = g % 28;
  const int ph = str_pos(1 + H, i), pv = H == 0 && 2 * i >= NV_EARLY ? str_pos(0, 2 * i + 1) : 0;
  return ph > pv ? ph : pv;
}
template <int G, int ISSUED> DI void h_acc(v4u (&hA)[28], v4u (&hB)[28], unsigned& m) {
  static_assert(dn_need(G) < ISSUED, "requested in time");
  vmwait<ISSUED - 1 - dn_need(G)>();
  v4u& x = G < 28 ? hA[G % 28] : hB[G % 28];
  pin(x);
  m = mxacc(m, x);
  pinu(m);
}
template <int G, int ISSUED> DI void h_decide(v4u (&hA)[28], v4u (&hB)[28], unsigned long long bad, const v4u& srd, int lane16, const Down& d,
                                              LAS unsigned char* L, int layer, int w) {
  constexpr int H = G / 28, LO = G % 28;
  v4u (&hh)[28] = H == 0 ? hA : hB;
  if (__builtin_expect(bad != 0ull, 0)) {
    unsigned spin = 0, m;
    vmwait<0>();
    do {
      RETRY(26 + H);
      const int bo = opaque(d.hbase);
      sfor<LO, str_count(1 + H, ISSUED)>([&](auto I) { ldx4<(I.v & 3) * 1024>(hh[I.v], srd, lane16, bo + H * 224 * 1024 + d.blk(I.v >> 2)); });
      vmwait<0>();
      m = 0;
#pragma unroll
      for (int i = LO; i < LO + 7; ++i) { pin(hh[i]); m = mxacc(m, hh[i]); }
    } while (any_sent(m) && ++spin < SPIN_MAX);
  }
}

DI void sumsq1(float& ss, unsigned a) {
  const float x = blo(a), y = bhi(a);
  asm volatile("v_fmac_f32 %0, %1, %1\n\tv_fmac_f32 %0, %2, %2" : "+v"(ss) : "v"(x), "v"(y));
}
DI float mulf(float a, float b) { float r; asm volatile("v_mul_f32 %0, %1, %2" : "=v"(r) : "v"(a), "v"(b)); return r; }
DI void addf4(f32x4& a, const f32x4& b) {
  float x = a.x, y = a.y, z = a.z, t = a.w;
  asm volatile("v_add_f32 %0, %0, %4\n\tv_add_f32 %1, %1, %5\n\tv_add_f32 %2, %2, %6\n\tv_add_f32 %3, %3, %7"
               : "+v"(x), "+v"(y), "+v"(z), "+v"(t) : "v"(b.x), "v"(b.y), "v"(b.z), "v"(b.w));
  a = f32x4{x, y, z, t};
}
DI float relu(float a) { const int i = __builtin_bit_cast(int, a); return __builtin_bit_cast(float, i > 0 ? i : 0); }
DI void h_piece(const f32x16& a, rsrc_t rs, int lane, int so) {
  v4u d;
#pragma unroll
  for (int i = 0; i < 4; ++i) d[i] = nosent(pk(relu(a[2 * i]) * a[2 * i + 8], relu(a[2 * i + 1]) * a[2 * i + 9]));
  bstore_sc1(d, rs, lane * 16, so);
}
DI void x3_piece(const f32x16& a, rsrc_t rs, int lane, int so) {
  v4u o0, o1;
  widen(a, 1.f, o0, o1);
  bstore_sc1(nosent4(o0), rs, (lane & 31) * XS + (lane >> 5) * 16, so);
  bstore_sc1(nosent4(o1), rs, (lane & 31) * XS + (lane >> 5) * 16, so + 32);
}

DI void ffn(LAS unsigned char* L, rsrc_t rs, const WSrc& ws, const Params& p, v4u (&W)[64], int layer, int c, int rb, int fs, int pi, int nb,
            int w, int lane) {
  const v4u& srd = ws.srd;
  const int hoff = OFF_H1 + layer * (OFF_H2 - OFF_H1);
  const int h = lane >> 5, r = lane & 31, q = (lane & 15) >> 2, lane16 = lane * 16;
  ZSrc z;
  z.srd = srd;
  z.vo = (8 * w + (lane >> 3)) * ZS + (((lane & 7) ^ ((lane >> 2) & 4)) * 16);
  z.so = OFF_Z1 + layer * (OFF_Z3 - OFF_Z1) + rb * 256;
  z.lds = (unsigned)(size_t)L + M_Z + w * 1024;
  STAMP(4);
  sfor<0, 8>([&](auto I) { z_issue<I.v>(z, z.so); });
  SB();
  LAS unsigned char* zown = L + M_Z + w * 1024 + lane16;
  LAS unsigned char* zr[2];
#pragma unroll
  for (int s = 0; s < 2; ++s)
    zr[s] = L + M_Z + (4 * h + q) * 128 + (4 * (s ^ (q >> 1)) + 2 * ((lane >> 4) & 1) + ((lane & 3) >> 1)) * 16 + 8 * (lane & 1);
  const unsigned dcmask = (w == 0 && h == 0) ? 0xFFFF0000u : 0xFFFFFFFFu;
  const int pt = 4 * rb + pi;
  Down dn;
  dn.vbase = OFF_W2 + layer * (OFF_W3 - OFF_W2) + (nb * 2 * 112 + 28 * w) * 1024;
  dn.hbase = hoff + (pt * 112 + 28 * w) * 1024;
  dn.xvo = (lane & 31) * XS + h * 16;
  dn.xso = OFF_Z0 + layer * (OFF_X3 - OFF_Z0) + pt * 32 * XS + nb * 128 + w * 32;
  const int ks0 = fs * 7 + 2 * w;
  const bool two = w < 3;
  const bool idft = opaque(w + layer) == 4;
  const rsrc_t rs2 = __builtin_amdgcn_make_buffer_rsrc((void*)p.ws, 0, two ? WS_BYTES : 0, 0x00020000);
  v4u hA[28], hB[28], x3[2], res[2], Ve[NV_EARLY];
  unsigned s0, s1, s2, s3;
  asm volatile("v_mov_b32 %0, -1\n\tv_mov_b32 %1, -1\n\tv_mov_b32 %2, -1\n\tv_mov_b32 %3, -1" : "=v"(s0), "=v"(s1), "=v"(s2), "=v"(s3));
  const v4u sv = {s0, s1, s2, s3};
  const rsrc_t rsf = __builtin_amdgcn_make_buffer_rsrc((void*)p.ws, 0, layer == 0 ? WS_BYTES : 0, 0x00020000);
  const int fo_h = lane16 + OFF_H2 + ((4 * rb + w) * 112 + fs * 7) * 1024, fo_z = (w * 64 + lane) * 16 + OFF_Z3 + 2 * c * ZS,
            fo_x = (w * 16 + (lane >> 2)) * XS + (lane & 3) * 16 + OFF_X3 + 128 * rb * XS + fs * 64;
  STAMP(5);
  vmwait<z_younger(1, -1)>();
  z_fix<0, -1>(sent_mask(mxacc(mxacc(0u, z_back<0>(zown)), z_back<1>(zown))), z, zown, L, layer, w);
  STAMP(6);
  BAR();
  STAMP(7);
  CSTAMP(20);
  f32x16 acc[2][2][2];
  float ss[2] = {0.f, 0.f};
  sfor<0, 2>([&](auto HH) {
    constexpr int H = HH.v;
    v4u bf[2][2], sq[2], hq, rbk[2];
    float hp[4];
    unsigned zm;
    unsigned long long bad;
    LAS unsigned char* zb[2] = {zr[0] + H * 65536, zr[1] + H * 65536};
    asm volatile("" : "+v"(zb[0]), "+v"(zb[1]));
    LAS unsigned char* zq[2];
    if constexpr (H == 1) { zq[0] = (pi ? zr[1] : zr[0]) + w * 16384; zq[1] = zq[0] + 65536; asm volatile("" : "+v"(zq[0]), "+v"(zq[1])); }
#pragma unroll
    for (int s = 0; s < 2; ++s) bf[0][s] = tr2(zb[s], 1024);
    sfor<0, 32>([&](auto KS) {
      constexpr int ks = KS.v;
      constexpr bool edge = H == 0 && (ks == 3 || ks == 7 || ks == 15);
      constexpr int e = ks >> 1, es = e & 1, et = e >> 1;
      constexpr int sr = H == 1 && ks >= 8 && (ks - 8) % 3 < 2 ? 2 * ((ks - 8) / 3) + (ks - 8) % 3 : -1;
      constexpr int sn = H == 1 && ks >= 9 && (ks - 9) % 3 < 2 ? 2 * ((ks - 9) / 3) + (ks - 9) % 3 : -1;
      constexpr int rd = H == 0 ? z_read_at(ks) : -1, rp = H == 0 && ks > 0 ? z_read_at(ks - 1) : -1, dec = H == 0 ? z_decide_at(ks) : -1;
      mfma_w<ks == 0, true>(acc[H][0][0], W[2 * ks], bf[ks & 1][0]);
      SB();
      if constexpr (ks + 1 < 32 && !edge) bf[(ks + 1) & 1][0] = tr2(zb[0] + (ks + 1) * 2048, 1024);
      if constexpr (rp >= 0) { zm = mxacc(z_first(rp) ? 0u : zm, rbk[0]); pinu(zm); }
      if constexpr (H == 1 && ks < 8) hp[0] = mulf(relu(acc[0][et][es][4 * (ks & 1)]), acc[0][et][es][4 * (ks & 1) + 8]);
      if constexpr (H == 1 && ks < 8) hp[1] = mulf(relu(acc[0][et][es][4 * (ks & 1) + 1]), acc[0][et][es][4 * (ks & 1) + 9]);
      if constexpr (sn >= 0) sumsq1(ss[sn & 1], sn < 2 ? sq[sn & 1].x & dcmask : sq[sn & 1].x);
      SB();
      mfma_w<ks == 0, true>(acc[H][1][0], W[2 * ks + 1], bf[ks & 1][0]);
      SB();
      if constexpr (H == 0 && ks < 16) z_issue<(ks < 8 ? 8 + ks : 2 * ks)>(z, z.so);
      if constexpr (H == 1 && ks < 8) hp[2] = mulf(relu(acc[0][et][es][4 * (ks & 1) + 2]), acc[0][et][es][4 * (ks & 1) + 10]);
      if constexpr (H == 1 && ks < 8) hp[3] = mulf(relu(acc[0][et][es][4 * (ks & 1) + 3]), acc[0][et][es][4 * (ks & 1) + 11]);
      if constexpr (sn >= 0) sumsq1(ss[sn & 1], sq[sn & 1].y);
      if constexpr (H == 1 && ks > 0) stream_issue<ks - 1, 1>(W, hA, hB, x3, srd, lane16, dn);
      if constexpr (H == 0 && ks >= 16 && (ks >= 26 || !(ks & 1))) {
        constexpr int i = ks < 26 ? (ks - 16) / 2 : ks - 21;
        if constexpr (i < 7) bstore_sc1(sv, rsf, fo_h, i * 1024);
        else if constexpr (i < 9) bstore_sc1(sv, rsf, fo_z, (i - 7) * ZS);
        else bstore_sc1(sv, rsf, fo_x, (i - 9) * 64 * XS);
      }
      SB();
      mfma_w<ks == 0, true>(acc[H][0][1], W[2 * ks], bf[ks & 1][1]);
      SB();
      if constexpr (ks + 1 < 32 && !edge) bf[(ks + 1) & 1][1] = tr2(zb[1] + (ks + 1) * 2048, 1024);
      if constexpr (rp >= 0) { zm = mxacc(zm, rbk[1]); pinu(zm); }
      if constexpr (dec >= 0) { bad = sent_mask(zm); pins(bad); }
      if constexpr (H == 1 && ks < 8) { hq[2 * (ks & 1)] = nosent(pk(hp[0], hp[1])); hq[2 * (ks & 1) + 1] = nosent(pk(hp[2], hp[3])); }
      if constexpr (sn >= 0) sumsq1(ss[sn & 1], sq[sn & 1].z);
      SB();
      mfma_w<ks == 0, true>(acc[H][1][1], W[2 * ks + 1], bf[ks & 1][1]);
      SB();
      if constexpr (dec >= 0) { pins(bad); z_fix<dec, ks>(bad, z, zown, L, layer, w); }
      if constexpr (rd >= 0) { vmwait<z_younger(rd + 1, ks)>(); rbk[0] = z_back<rd>(zown); rbk[1] = z_back<rd + 1>(zown); }
      if constexpr (sn >= 0) sumsq1(ss[sn & 1], sq[sn & 1].w);
      if constexpr (sr >= 0) sq[sr & 1] = tr2(zq[sr & 1] + (sr >> 1) * 2048, 1024);
      if constexpr (H == 0 && ks < NW_LATE) load_w<64 - NW_LATE + ks>(W, ws);
      if constexpr (H == 0 && ks >= 8 && ks < 16) z_issue<2 * ks + 1>(z, z.so);
      if constexpr (H == 0 && ks >= 16) {
        constexpr int n = ks - 16;
        vload<((n >> 1) & 3) * 1024>(Ve[n], srd, lane16, dn.vbase + (n & 1) * 112 * 1024 + dn.blk(n >> 3));
      }
      if constexpr (H == 1) {
        if constexpr (ks < 8 && (ks & 1)) {
          bstore_sc1(hq, et == 0 ? rs : rs2, lane16, hoff + ((4 * rb + es) * 112 + ks0 + et) * 1024);
          if constexpr (et == 1) { if (idft) x3_piece(acc[0][1][es], rs, lane, OFF_X3 + (128 * rb + 32 * es) * XS + fs * 64); }
        }
        stream_issue<ks, (ks == 31 ? 2 : 0)>(W, hA, hB, x3, srd, lane16, dn);
      }
      if constexpr (edge) {
        BAR();
#pragma unroll
        for (int s = 0; s < 2; ++s) bf[(ks + 1) & 1][s] = tr2(zb[s] + (ks + 1) * 2048, 1024);
      }
      SB();
    });
    STAMP(8 + 2 * H);
    if constexpr (H == 0) BAR();
    STAMP(9 + 2 * H);
  });
  mfma_done(acc[1][0][0], acc[1][0][1], acc[1][1][0], acc[1][1][1]);
#pragma unroll
  for (int s = 0; s < 2; ++s) h_piece(acc[1][0][s], rs, lane, hoff + ((4 * rb + 2 + s) * 112 + ks0) * 1024);
  if (two) {
#pragma unroll
    for (int s = 0; s < 2; ++s) h_piece(acc[1][1][s], rs, lane, hoff + ((4 * rb + 2 + s) * 112 + ks0 + 1) * 1024);
  } else if (idft) {
#pragma unroll
    for (int s = 0; s < 2; ++s) x3_piece(acc[1][1][s], rs, lane, OFF_X3 + (128 * rb + 64 + 32 * s) * XS + fs * 64);
  }
  SB();
  stream_issue<32, 2>(W, hA, hB, x3, srd, lane16, dn);
  {
    auto x = __builtin_amdgcn_permlane32_swap(asu(ss[0]), asu(ss[1]), false, false);
    *(LAS float*)(L + M_S1 + w * 256 + lane * 4) = asf(x[0]) + asf(x[1]);
  }
  const int row = 16 * w + (lane >> 2);
  {
    const int n = nb * 64 + row;
    LAS unsigned char* ra = L + M_Z + n * 128 + (((4 * pi + (lane & 3)) ^ ((n & 2) << 1)) * 16);
    res[0] = *(LAS v4u*)ra; res[1] = *(LAS v4u*)(ra + 65536);
  }
  STAMP(12);
  CSTAMP(21);
  unsigned hm = 0;
  sfor<0, 7>([&](auto G) { h_acc<G.v, STR.end[32]>(hA, hB, hm); });
  h_decide<0, STR.end[32]>(hA, hB, sent_mask(hm), srd, lane16, dn, L, layer, w);
  hm = 0;
  BAR();
  STAMP(13);
  LAS unsigned char* rw = L + M_Z + w * 9216 + r * 144 + h * 16;
  LAS unsigned char* rr = L + M_Z + row * 144 + (lane & 3) * 32;
  f32x16 dac[2][2];
  f32x4 fa[2][2], tq[2];
  auto red_write = [&](auto HH, auto K) {
    constexpr int H = HH.v, t = K.v >> 2, g = K.v & 3;
    *(LAS f32x4*)(rw + H * M_RB + t * 32 * 144 + g * 32) = f32x4{dac[H][t][4 * g], dac[H][t][4 * g + 1], dac[H][t][4 * g + 2], dac[H][t][4 * g + 3]};
  };
  sfor<0, 56>([&](auto GG) {
    constexpr int g = GG.v, H = g / 28, i = g % 28;
    v4u& hx = H == 0 ? hA[i] : hB[i];
    unsigned long long bad = 0ull;
    if constexpr (2 * i < NV_EARLY) mfma_v<i == 0>(dac[H][0], Ve[2 * i], hx);
    else mfma_w<false, false>(dac[H][0], W[2 * i - NV_EARLY], hx);
    SB();
    if constexpr (g + 7 < 56) h_acc<g + 7, STR.end[32 + g]>(hA, hB, hm);
    if constexpr (g + 7 < 56 && g % 7 == 6) { bad = sent_mask(hm); pins(bad); hm = 0; }
    if constexpr (g == 1) {
      LAS float* sp = (LAS float*)(L + M_S1 + lane * 4);
      *(LAS float*)(L + M_S1 + 1024 + w * 256 + lane * 4) =
          __builtin_amdgcn_rcpf(__builtin_fmaf((sp[0] + sp[64]) + (sp[128] + sp[192]), 1.f / 512.f, 1e-5f));
    }
    if constexpr (g >= 51 && g <= 53) addf4(fa[0][0], tq[0]);
    SB();
    if constexpr (2 * i < NV_EARLY) mfma_v<i == 0>(dac[H][1], Ve[2 * i + 1], hx);
    else mfma_w<false, false>(dac[H][1], W[2 * i + 1 - NV_EARLY], hx);
    SB();
    if constexpr (g + 7 < 56 && g % 7 == 6) { pins(bad); h_decide<g + 1, STR.end[32 + g]>(hA, hB, bad, srd, lane16, dn, L, layer, w); }
    stream_issue<33 + g, 2>(W, hA, hB, x3, srd, lane16, dn);
    if constexpr (g >= 30 && g <= 46 && g % 2 == 0 && g != 34) red_write(IC<0>{}, IC<(g < 34 ? (g - 30) / 2 : (g - 32) / 2)>{});
    if constexpr (g == 47) BAR();
    if constexpr (g == 49) { fa[0][0] = *(LAS f32x4*)rr; fa[0][1] = *(LAS f32x4*)(rr + 16); }
    if constexpr (g >= 51 && g <= 53) addf4(fa[0][1], tq[1]);
    if constexpr (g >= 50 && g <= 52) { tq[0] = *(LAS f32x4*)(rr + (g - 49) * 9216); tq[1] = *(LAS f32x4*)(rr + (g - 49) * 9216 + 16); }
    SB();
    if constexpr (g == 27) STAMP(14);
  });
  STAMP(15);
  asm volatile("s_nop 12" : "+v"(dac[1][0]), "+v"(dac[1][1]));
  sfor<0, 8>([&](auto K) { red_write(IC<1>{}, K); });
  vmwait<0>();
  pin(x3[0]); pin(x3[1]);
  if (__builtin_expect(layer == 1 && any_sent(mxacc(mxacc(0u, x3[0]), x3[1])), 0)) {
    unsigned spin = 0;
    do {
      RETRY(29);
      ldx4<0>(x3[0], srd, dn.xvo, dn.xso); ldx4<0>(x3[1], srd, dn.xvo, dn.xso + 64 * XS);
      vmwait<0>();
      pin(x3[0]); pin(x3[1]);
    } while (any_sent(mxacc(mxacc(0u, x3[0]), x3[1])) && ++spin < SPIN_MAX);
  }
  STAMP(16);
  BAR();
  STAMP(17);
  fa[1][0] = *(LAS f32x4*)(rr + M_RB); fa[1][1] = *(LAS f32x4*)(rr + M_RB + 16);
#pragma unroll
  for (int ww = 1; ww < 4; ++ww) { fa[1][0] += *(LAS f32x4*)(rr + M_RB + ww * 9216); fa[1][1] += *(LAS f32x4*)(rr + M_RB + ww * 9216 + 16); }
  f32x4 sc[2][2];
#pragma unroll
  for (int H = 0; H < 2; ++H)
#pragma unroll
    for (int x = 0; x < 2; ++x) sc[H][x] = *(LAS f32x4*)(L + M_S1 + 1024 + w * 256 + H * 128 + (lane & 3) * 32 + x * 16);
  if (layer == 0) {
    const bool dc = nb == 0 && row == 0;
#pragma unroll
    for (int H = 0; H < 2; ++H) {
      v4u o;
      float sq[8];
#pragma unroll
      for (int j = 0; j < 4; ++j) {
        o[j] = nosent(pk(__builtin_fmaf(fa[H][j >> 1][(2 * j) & 3], sc[H][j >> 1][(2 * j) & 3], blo(res[H][j])),
                         __builtin_fmaf(fa[H][j >> 1][(2 * j + 1) & 3], sc[H][j >> 1][(2 * j + 1) & 3], bhi(res[H][j]))));
      }
      bstore_sc1(o, rs, row * ZS + (lane & 3) * 16, OFF_Z2 + nb * 64 * ZS + pt * 64 + H * 128);
#pragma unroll
      for (int j = 0; j < 4; ++j) {
        const unsigned u = dc ? 0u : o[j];
        sq[2 * j] = blo(u) * blo(u); sq[2 * j + 1] = bhi(u) * bhi(u);
      }
      float t4[4], t2[2];
#pragma unroll
      for (int j = 0; j < 4; ++j) {
        auto x = __builtin_amdgcn_permlane32_swap(asu(sq[j]), asu(sq[j + 4]), false, false);
        t4[j] = asf(x[0]) + asf(x[1]);
      }
#pragma unroll
      for (int j = 0; j < 2; ++j) {
        auto x = __builtin_amdgcn_permlane16_swap(asu(t4[j]), asu(t4[j + 2]), false, false);
        t2[j] = asf(x[0]) + asf(x[1]);
        t2[j] += dppf<0x124>(t2[j]);
        t2[j] += dppf<0x128>(t2[j]);
      }
      *(LAS f32x2*)(L + M_S2 + w * 256 + H * 128 + (8 * (lane & 3) + 4 * h + 2 * ((lane >> 4) & 1)) * 4) = f32x2{t2[0], t2[1]};
    }
    BAR();
    if (w == 0) {
      LAS float* sp = (LAS float*)(L + M_S2 + lane * 4);
      const float t = (sp[0] + sp[64]) + (sp[128] + sp[192]);
      __builtin_amdgcn_raw_buffer_store_b32(nosent(asu(t)), rs, stat_of(h * 64 + r), OFF_ST + nb * 8192 + pt * 64, SC1);
    }
  } else {
    LAS unsigned char* tb = L + M_TB + w * 2560;
#pragma unroll
    for (int H = 0; H < 2; ++H) {
      const f32x4 a = fa[H][0] * sc[H][0], bq = fa[H][1] * sc[H][1];
      *(LAS v4u*)(tb + H * 1280 + (lane >> 2) * 80 + (lane & 3) * 16) = v4u{pk(a.x, a.y), pk(a.z, a.w), pk(bq.x, bq.y), pk(bq.z, bq.w)};
    }
#pragma unroll
    for (int H = 0; H < 2; ++H) {
      const v4u y = tr2(tb + H * 1280 + (8 * h + q) * 80 + ((lane >> 4) & 1) * 32 + (lane & 3) * 8, 4 * 80);
      v4u o;
#pragma unroll
      for (int j = 0; j < 4; ++j) o[j] = pk(blo(x3[H][j]) + blo(y[j]), bhi(x3[H][j]) + bhi(y[j]));
      const int tok = token_of(32 * (pt + 2 * H) + r);
      *(v4u*)(p.out + (size_t)tok * 512 + nb * 64 + 16 * w + 8 * h) = o;
    }
  }
  SB();
}

#define XMAP 1
__global__ void __launch_bounds__(256, 1) k_main(Params p) {
  extern __shared__ __attribute__((aligned(16))) unsigned char smem[];
  LAS unsigned char* L = (LAS unsigned char*)smem;
  const rsrc_t rs = __builtin_amdgcn_make_buffer_rsrc((void*)p.ws, 0, WS_BYTES, 0x00020000);
  const v4u srd = {(unsigned)(size_t)p.ws, (unsigned)((size_t)p.ws >> 32) & 0xFFFFu, (unsigned)WS_BYTES, 0x00020000u};
#pragma unroll 1
  for (int layer = 0; layer < 2; ++layer) {
    int tid = threadIdx.x;
    asm volatile("" : "+v"(tid));
    const int c = opaque(blockIdx.x);
    const int lane = tid & 63, w = __builtin_amdgcn_readfirstlane(tid >> 6);
    const int rb = 4 * (c & 3) + (c >> 6), fs = 8 * ((c >> 2) & 1) + ((c >> 3) & 7), pi = fs >> 3, nb = fs & 7;
    WSrc ws;
    ws.srd = srd; ws.srd1 = srd;
    if (w == 3 && layer == 0) ws.srd1.z = 0u;
    ws.lane16 = lane * 16; ws.base = OFF_W1 + (fs * 8 + 2 * w) * 32768;
    v4u W[64];
    unsigned wf[8];
    const int wfvo = (16 * w + (lane & 15) + 64 * 4 * (lane >> 4)) * 4;
    STAMP(0);
    CSTAMP(22);
    v4u zr, zi, sa, sb;
    asm volatile("" : "=v"(zr), "=v"(zi), "=v"(sa), "=v"(sb));
    const int first = c == 0;
    if (layer == 0) {
      ldx4<0>(zr, srd, tid * 16, OFF_Z0 + (2 * c + first) * ZS); ldx4<0>(zi, srd, tid * 16, OFF_Z0 + (2 * c + 1 - first) * ZS);
      ldx4<0>(sa, srd, tid * 16, OFF_R0); ldx4<0>(sb, srd, tid * 16, OFF_R0 + 4096);
    }
    asm volatile("buffer_load_dword %0, %8, %9, %10 offen\n\tbuffer_load_dword %1, %8, %9, %10 offen offset:256\n\t"
                 "buffer_load_dword %2, %8, %9, %10 offen offset:512\n\tbuffer_load_dword %3, %8, %9, %10 offen offset:768\n\t"
                 "buffer_load_dword %4, %8, %9, %11 offen\n\tbuffer_load_dword %5, %8, %9, %11 offen offset:256\n\t"
                 "buffer_load_dword %6, %8, %9, %11 offen offset:512\n\tbuffer_load_dword %7, %8, %9, %11 offen offset:768"
                 : "=&v"(wf[0]), "=&v"(wf[1]), "=&v"(wf[2]), "=&v"(wf[3]), "=&v"(wf[4]), "=&v"(wf[5]), "=&v"(wf[6]), "=&v"(wf[7])
                 : "v"(wfvo), "s"(srd), "s"(OFF_WT + c * WS_), "s"(OFF_WT + c * WS_ + 4096));
    SB();
    ColConst k;
    col_consts(k, w, lane, W, ws);
    SB();
    vmwait<16>();
    asm volatile("" : "+v"(wf[0]), "+v"(wf[1]), "+v"(wf[2]), "+v"(wf[3]), "+v"(wf[4]), "+v"(wf[5]), "+v"(wf[6]), "+v"(wf[7]));
    pin(zr); pin(zi); pin(sa); pin(sb);
    float rstd[8], fr[8], fi[8];
#pragma unroll
    for (int e = 0; e < 8; ++e) { fr[e] = blo(wf[e]) * (1.f / 2048.f); fi[e] = bhi(wf[e]) * (1.f / 2048.f); pinf(fr[e]); pinf(fi[e]); }
    STAMP(1);
    if (layer == 1) {
      __builtin_amdgcn_s_sleep(DELAY_C);
      unsigned spin = 0;
      bool again;
      do {
        RETRY(28);
        v4u st[16];
        ldx4<0>(zr, srd, tid * 16, OFF_Z2 + (2 * c + first) * ZS); ldx4<0>(zi, srd, tid * 16, OFF_Z2 + (2 * c + 1 - first) * ZS);
        sfor<0, 16>([&](auto I) { ldx4<0>(st[I.v], srd, tid * 16, OFF_ST + I.v * 4096); });
        vmwait<0>();
        pin(zr); pin(zi);
#pragma unroll
        for (int i = 0; i < 16; ++i) pin(st[i]);
        f32x4 a = __builtin_bit_cast(f32x4, st[0]), bq = __builtin_bit_cast(f32x4, st[1]);
#pragma unroll
        for (int n = 1; n < 8; ++n) { a += __builtin_bit_cast(f32x4, st[2 * n]); bq += __builtin_bit_cast(f32x4, st[2 * n + 1]); }
        sa = __builtin_bit_cast(v4u, a); sb = __builtin_bit_cast(v4u, bq);
        const f32x4 t = a + bq;
        const float u = (t.x + t.y) + (t.z + t.w);
        unsigned m = mxacc(mxacc(0u, zr), zi);
        if (__builtin_expect(__builtin_amdgcn_ballot_w64(u != u) != 0ull, 0)) {
#pragma unroll
          for (int i = 0; i < 16; ++i) m = mxacc(m, st[i]);
        }
        again = any_sent(m);
      } while (__builtin_expect(again, 0) && ++spin < SPIN_MAX);
    }
    load_ws<16, 18>(W, ws);
#pragma unroll
    for (int e = 0; e < 4; ++e) {
      rstd[e] = __builtin_amdgcn_rsqf(__builtin_fmaf(asf(sa[e]), 1.f / 512.f, 1e-5f));
      rstd[4 + e] = __builtin_amdgcn_rsqf(__builtin_fmaf(asf(sb[e]), 1.f / 512.f, 1e-5f));
    }
    load_ws<18, 20>(W, ws);
    STAMP(2);
    col_compute<20>(L, rs, k, fr, fi, zr, zi, rstd, c, w, lane, OFF_Z1 + layer * (OFF_Z3 - OFF_Z1), W, ws);
    STAMP(3);
    CSTAMP(23);
    load_ws<50, 64 - NW_LATE>(W, ws);
    __builtin_amdgcn_s_sleep(DELAY_A);
    ffn(L, rs, ws, p, W, layer, c, rb, fs, pi, nb, w, lane);
    STAMP(18);
  }
}

extern "C" void kernel_launch(void* const* d_in, const int* in_sizes, int n_in, void* d_out, int out_size, void* d_ws, size_t ws_size,
                              hipStream_t stream) {
  Params p;
  p.x = (const u16*)d_in[0]; p.wr = (const u16*)d_in[1]; p.wi = (const u16*)d_in[2];
  p.wg = (const u16*)d_in[4]; p.wu = (const u16*)d_in[5]; p.wd = (const u16*)d_in[6];
  p.out = (u16*)d_out; p.ws = (unsigned char*)d_ws;
  (void)hipFuncSetAttribute((const void*)k_prep, hipFuncAttributeMaxDynamicSharedMemorySize, P_LDS);
  (void)hipFuncSetAttribute((const void*)k_main, hipFuncAttributeMaxDynamicSharedMemorySize, M_LDS);
  k_prep<<<256, 512, P_LDS, stream>>>(p);
  k_main<<<256, 256, M_LDS, stream>>>(p);
}
```

```cpp
#include <hip/hip_runtime.h>
#include <hip/hip_bf16.h>

#define LAS __attribute__((address_space(3)))
#define DI __device__ __forceinline__
typedef unsigned short u16;
typedef unsigned v4u __attribute__((ext_vector_type(4)));
typedef unsigned v2u __attribute__((ext_vector_type(2)));
typedef float f32x2 __attribute__((ext_vector_type(2)));
typedef float f32x4 __attribute__((ext_vector_type(4)));
typedef float f32x16 __attribute__((ext_vector_type(16)));
typedef short s16x4 __attribute__((ext_vector_type(4)));
typedef __bf16 bf16x2 __attribute__((ext_vector_type(2)));
typedef __bf16 bf16x8 __attribute__((ext_vector_type(8)));
typedef __amdgpu_buffer_rsrc_t rsrc_t;

constexpr int MB = 1 << 20;
constexpr int ZS = 4224, XS = 1152, WS_ = 8320;
constexpr int OFF_Z0 = 0, OFF_Z1 = 9 * MB / 4, OFF_Z2 = 9 * MB / 2, OFF_Z3 = 27 * MB / 4;
constexpr int OFF_WT = 9 * MB;
constexpr int OFF_W1 = 12 * MB;
constexpr int OFF_W2 = 16 * MB;
constexpr int OFF_W3 = 18 * MB;
constexpr int OFF_H1 = 20 * MB;
constexpr int OFF_H2 = 28 * MB;
constexpr int OFF_X3 = 36 * MB;
constexpr int OFF_R0 = 39 * MB;
constexpr int OFF_ST = 39 * MB + 65536;
constexpr int WS_BYTES = 40 * MB;
constexpr int SC1 = 16;
constexpr unsigned SENT = 0xFFFFFFFFu;
constexpr unsigned SPIN_MAX = 1u << 16;

struct Params {
  const u16* x; const u16* wr; const u16* wi; const u16* wg; const u16* wu; const u16* wd;
  u16* out; unsigned char* ws;
};

DI unsigned pk(float a, float b) { f32x2 v = {a, b}; return __builtin_bit_cast(unsigned, __builtin_convertvector(v, bf16x2)); }
DI float asf(unsigned u) { return __builtin_bit_cast(float, u); }
DI unsigned asu(float f) { return __builtin_bit_cast(unsigned, f); }
DI bf16x2 asb2(unsigned u) { return __builtin_bit_cast(bf16x2, u); }
template <int V> struct IC { static constexpr int v = V; };
template <int I, int N, class Fn> DI void sfor(Fn&& fn) { if constexpr (I < N) { fn(IC<I>{}); sfor<I + 1, N>(fn); } }
DI int opaque(int x) { asm volatile("" : "+s"(x)); return x; }
DI float blo(unsigned u) { return __builtin_bit_cast(float, u << 16); }
DI float bhi(unsigned u) { return __builtin_bit_cast(float, u & 0xFFFF0000u); }
DI float rbf(float a) { return blo(pk(a, a)); }
DI unsigned nosent(unsigned u) { return u < 0xFFFFFFFEu ? u : 0xFFFFFFFEu; }
DI v4u nosent4(v4u v) { v.x = nosent(v.x); v.y = nosent(v.y); v.z = nosent(v.z); v.w = nosent(v.w); return v; }
DI unsigned umax(unsigned a, unsigned b) { return a > b ? a : b; }
DI unsigned mx4(v4u v) { return umax(umax(v.x, v.y), umax(v.z, v.w)); }
DI bool any_sent(unsigned m) { return __builtin_amdgcn_ballot_w64(m == SENT) != 0ull; }
DI f32x16 mfma32(v4u a, v4u b, f32x16 c) {
  return __builtin_amdgcn_mfma_f32_32x32x16_bf16(__builtin_bit_cast(bf16x8, a), __builtin_bit_cast(bf16x8, b), c, 0, 0, 0);
}
DI f32x4 mfma16(v4u a, v4u b, f32x4 c) {
  return __builtin_amdgcn_mfma_f32_16x16x32_bf16(__builtin_bit_cast(bf16x8, a), __builtin_bit_cast(bf16x8, b), c, 0, 0, 0);
}
template <bool FIRST, bool WA> DI void mfma_w(f32x16& acc, const v4u& wt, const v4u& x) {
  if constexpr (FIRST && WA) asm volatile("v_mfma_f32_32x32x16_bf16 %0, %1, %2, 0" : "=&v"(acc) : "a"(wt), "v"(x));
  if constexpr (FIRST && !WA) asm volatile("v_mfma_f32_32x32x16_bf16 %0, %2, %1, 0" : "=&v"(acc) : "a"(wt), "v"(x));
  if constexpr (!FIRST && WA) asm volatile("v_mfma_f32_32x32x16_bf16 %0, %1, %2, %0" : "+v"(acc) : "a"(wt), "v"(x));
  if constexpr (!FIRST && !WA) asm volatile("v_mfma_f32_32x32x16_bf16 %0, %2, %1, %0" : "+v"(acc) : "a"(wt), "v"(x));
}
template <bool FIRST> DI void mfma_v(f32x16& acc, const v4u& wt, const v4u& x) {
  if constexpr (FIRST) asm volatile("v_mfma_f32_32x32x16_bf16 %0, %2, %1, 0" : "=&v"(acc) : "v"(wt), "v"(x));
  else asm volatile("v_mfma_f32_32x32x16_bf16 %0, %2, %1, %0" : "+v"(acc) : "v"(wt), "v"(x));
}
template <int IMM> DI void vload(v4u& dst, const v4u& srd, int lane16, int so) {
  asm volatile("buffer_load_dwordx4 %0, %1, %2, %3 offen offset:%4" : "=v"(dst) : "v"(lane16), "s"(srd), "s"(so), "i"(IMM));
}
template <int IMM, bool REUSE> DI void wload(v4u& dst, const v4u& srd, int lane16, int so) {
  if constexpr (REUSE) asm volatile("buffer_load_dwordx4 %0, %1, %2, %3 offen offset:%4" : "+a"(dst) : "v"(lane16), "s"(srd), "s"(so), "i"(IMM));
  else asm volatile("buffer_load_dwordx4 %0, %1, %2, %3 offen offset:%4" : "=a"(dst) : "v"(lane16), "s"(srd), "s"(so), "i"(IMM));
}
DI void mfma_done(f32x16& a, f32x16& b, f32x16& c, f32x16& d) { asm volatile("s_nop 12" : "+v"(a), "+v"(b), "+v"(c), "+v"(d)); }
DI v2u trread(LAS unsigned char* p) { return __builtin_bit_cast(v2u, __builtin_amdgcn_ds_read_tr16_b64_v4i16((LAS s16x4*)p)); }
DI v4u tr2(LAS unsigned char* p, int d) { v2u a = trread(p), b = trread(p + d); return v4u{a.x, a.y, b.x, b.y}; }
DI v4u bload(rsrc_t rs, int voff, int soff) { return __builtin_amdgcn_raw_buffer_load_b128(rs, voff, soff, 0); }
DI v4u bload_sc1(rsrc_t rs, int voff, int soff) { return __builtin_amdgcn_raw_buffer_load_b128(rs, voff, soff, SC1); }
DI void bstore(v4u v, rsrc_t rs, int voff, int soff) { __builtin_amdgcn_raw_buffer_store_b128(v, rs, voff + soff, 0, 0); }
DI void bstore_sc1(v4u v, rsrc_t rs, int voff, int soff) { __builtin_amdgcn_raw_buffer_store_b128(v, rs, voff + soff, 0, SC1); }
template <int I> DI v4u fload(rsrc_t rs, int lane16, int base) { return bload(rs, lane16 + (I & 3) * 1024, base + (I >> 2) * 4096); }
template <int I> DI v4u fload_sc1(rsrc_t rs, int lane16, int base) { return bload_sc1(rs, lane16 + (I & 3) * 1024, base + (I >> 2) * 4096); }
#define SB() __builtin_amdgcn_sched_barrier(0)
#define MEMFENCE() asm volatile("" ::: "memory")
#define BAR() asm volatile("s_waitcnt lgkmcnt(0)\n\ts_barrier" ::: "memory")
template <int N> DI void vmwait() { asm volatile("s_waitcnt vmcnt(%0)" :: "i"(N) : "memory"); }
DI void pin(v4u& a) { asm volatile("" : "+v"(a)); }
DI f32x16 zero16() { f32x16 z; for (int i = 0; i < 16; ++i) z[i] = 0.f; return z; }
DI v4u pack8(const f32x16& a, int s, float sc) {
  return v4u{pk(a[8 * s] * sc, a[8 * s + 1] * sc), pk(a[8 * s + 2] * sc, a[8 * s + 3] * sc),
             pk(a[8 * s + 4] * sc, a[8 * s + 5] * sc), pk(a[8 * s + 6] * sc, a[8 * s + 7] * sc)};
}
DI void widen(const f32x16& a, float sc, v4u& o0, v4u& o1) {
  unsigned d[8];
#pragma unroll
  for (int i = 0; i < 8; ++i) d[i] = pk(a[2 * i] * sc, a[2 * i + 1] * sc);
  auto r0 = __builtin_amdgcn_permlane32_swap(d[0], d[2], false, false);
  auto r1 = __builtin_amdgcn_permlane32_swap(d[1], d[3], false, false);
  auto r2 = __builtin_amdgcn_permlane32_swap(d[4], d[6], false, false);
  auto r3 = __builtin_amdgcn_permlane32_swap(d[5], d[7], false, false);
  o0 = v4u{r0[0], r1[0], r0[1], r1[1]};
  o1 = v4u{r2[0], r3[0], r2[1], r3[1]};
}
template <int CTRL> DI float dppf(float v) {
  return __builtin_bit_cast(float, __builtin_amdgcn_update_dpp(0, __builtin_bit_cast(int, v), CTRL, 0xF, 0xF, true));
}
DI float sum32(float v) {
  v += dppf<0xB1>(v); v += dppf<0x4E>(v); v += dppf<0x141>(v); v += dppf<0x140>(v);
  unsigned u = __builtin_bit_cast(unsigned, v);
  auto r = __builtin_amdgcn_permlane16_swap(u, u, false, false);
  return __builtin_bit_cast(float, (unsigned)r[0]) + __builtin_bit_cast(float, (unsigned)r[1]);
}
DI int stat_of(int p) { return ((p >> 2) & 1) * 4096 + (p >> 3) * 16 + (p & 3) * 4; }
DI int token_of(int p) {
  const int l = (p >> 3) & 63, e = p & 7;
  return 32 * (16 * (p >> 9) + 4 * (l >> 4) + (e & 3)) + (l & 15) + 16 * (e >> 2);
}
#define GLDS_POLICY ""
DI void glds16(const void* sbase, unsigned voff, unsigned lds) {
  unsigned keep;
  asm volatile("s_mov_b32 %0, m0\n\ts_mov_b32 m0, %3\n\ts_nop 0\n\tglobal_load_lds_dwordx4 %1, %2" GLDS_POLICY "\n\ts_mov_b32 m0, %0"
               : "=&s"(keep) : "v"(voff), "s"(sbase), "s"(lds) : "memory");
}
template <int IMM> DI void ld2u16(unsigned& a, unsigned& b, int voff, const v4u& sa, const v4u& sb, int soff) {
  asm volatile("buffer_load_ushort %0, %2, %3, %5 offen offset:%6\n\tbuffer_load_ushort %1, %2, %4, %5 offen offset:%6"
               : "=&v"(a), "=&v"(b) : "v"(voff), "s"(sa), "s"(sb), "s"(soff), "i"(IMM));
}
DI v4u cosfrag(float a0, float step, int off) {
  float v[8];
#pragma unroll
  for (int j = 0; j < 8; ++j) v[j] = __builtin_amdgcn_cosf(__builtin_fmaf(step, (float)(off + j), a0));
  return v4u{pk(v[0], v[1]), pk(v[2], v[3]), pk(v[4], v[5]), pk(v[6], v[7])};
}

constexpr int P_XL = 0, P_GL = 33280, P_UL = P_GL + 32768 + 32, P_DL = P_UL + 32768 + 32, P_ZB = P_DL + 33280, P_WB = P_ZB + 4 * 2560,
              P_LDS = P_WB + 64 * 144;

__global__ void __launch_bounds__(512, 2) k_prep(Params p) {
  extern __shared__ __attribute__((aligned(16))) unsigned char smem[];
  LAS unsigned char* L = (LAS unsigned char*)smem;
  const unsigned Lb = (unsigned)(size_t)L;
  const int tid = threadIdx.x, lane = tid & 63, w8 = __builtin_amdgcn_readfirstlane(tid >> 6), w = w8 & 3;
  const int b = blockIdx.x, xcd = b & 7, q = (b >> 3) & 3, tgi = b >> 5;
  const int t1 = xcd * 8 + tgi, tg = tgi * 8 + xcd;
  const bool heavy = tgi < 7;
  const int h = lane >> 5, r = lane & 31, ft = 4 * q + w;
  const rsrc_t rs = __builtin_amdgcn_make_buffer_rsrc((void*)p.ws, 0, WS_BYTES, 0x00020000);
#define PST(i)
  PST(0);
  if (w8 >= 4) {
#pragma unroll
    for (int i = 0; i < 8; ++i)
      glds16(p.x + (size_t)token_of(32 * t1 + 8 * w + i) * 512, lane * 16, Lb + P_XL + (8 * w + i) * 1040);
    if (heavy) {
      const unsigned vo = (lane >> 2) * 3584 + (lane & 3) * 16;
#pragma unroll
      for (int i = 0; i < 8; ++i) {
        glds16(p.wg + (size_t)(8 * w + i) * 16 * 1792 + 32 * tg, vo, Lb + P_GL + (8 * w + i) * 1024);
        glds16(p.wu + (size_t)(8 * w + i) * 16 * 1792 + 32 * tg, vo, Lb + P_UL + (8 * w + i) * 1024);
      }
      vmwait<16>();
    } else vmwait<0>();
    PST(1);
    BAR();
    if (heavy) {
#pragma unroll
      for (int i = 0; i < 8; ++i) glds16(p.wd + (size_t)(32 * tg + 8 * w + i) * 512, lane * 16, Lb + P_DL + (8 * w + i) * 1040);
    }
    const int kk0 = 32 * tg + 8 * w;
    unsigned fr[8], fi[8];
    {
      const v4u sr = {(unsigned)(size_t)p.wr, (unsigned)((size_t)p.wr >> 32) & 0xFFFFu, 2048u * 514u, 0x00020000u};
      const v4u si = {(unsigned)(size_t)p.wi, (unsigned)((size_t)p.wi >> 32) & 0xFFFFu, 2048u * 514u, 0x00020000u};
      const int wc = 64 * q + lane, src = (wc == 0 ? 256 : wc) * 2;
      sfor<0, 8>([&](auto I) { ld2u16<I.v * 514>(fr[I.v], fi[I.v], src, sr, si, kk0 * 514); });
    }
    PST(2);
    {
      const int row = 8 * q + 2 * w + h;
      const v4u u0 = *(LAS v4u*)(L + P_XL + row * 1040 + r * 32), u1 = *(LAS v4u*)(L + P_XL + row * 1040 + r * 32 + 16);
      float v[16];
#pragma unroll
      for (int i = 0; i < 4; ++i) { v[2 * i] = blo(u0[i]); v[2 * i + 1] = bhi(u0[i]); v[8 + 2 * i] = blo(u1[i]); v[9 + 2 * i] = bhi(u1[i]); }
      float s = 0.f;
#pragma unroll
      for (int i = 0; i < 16; ++i) s += v[i];
      const float m = sum32(s) * (1.f / 512.f);
      float ss = 0.f;
#pragma unroll
      for (int i = 0; i < 16; ++i) { const float d = v[i] - m; ss = __builtin_fmaf(d, d, ss); }
      ss = sum32(ss);
      if (r == 0) __builtin_amdgcn_raw_buffer_store_b32(nosent(asu(ss)), rs, stat_of(32 * t1 + row), OFF_R0, 0);
    }
    PST(3);
    vmwait<24>();
    PST(4);
    BAR();
    {
      const v4u sv = {SENT, SENT, SENT, SENT};
      const int t16 = (w * 64 + lane) * 16;
#pragma unroll
      for (int i = 0; i < 4; ++i) bstore_sc1(sv, rs, t16, (i < 2 ? OFF_Z1 : OFF_Z2) + (2 * b + (i & 1)) * ZS);
#pragma unroll
      for (int i = 0; i < 7; ++i) bstore_sc1(sv, rs, t16, OFF_H1 + b * 28672 + i * 4096);
      if (t16 < 256) bstore_sc1(sv, rs, t16, OFF_ST + b * 256);
    }
    PST(5);
    asm volatile("s_waitcnt vmcnt(11)"
                 : "+v"(fr[0]), "+v"(fr[1]), "+v"(fr[2]), "+v"(fr[3]), "+v"(fr[4]), "+v"(fr[5]), "+v"(fr[6]), "+v"(fr[7]),
                   "+v"(fi[0]), "+v"(fi[1]), "+v"(fi[2]), "+v"(fi[3]), "+v"(fi[4]), "+v"(fi[5]), "+v"(fi[6]), "+v"(fi[7]) :: "memory");
    PST(6);
    {
      v4u o0, o1;
#pragma unroll
      for (int i = 0; i < 4; ++i) { o0[i] = fr[i] | (fi[i] << 16); o1[i] = fr[4 + i] | (fi[4 + i] << 16); }
      *(LAS v4u*)(L + P_WB + lane * 144 + w * 32) = o0;
      *(LAS v4u*)(L + P_WB + lane * 144 + w * 32 + 16) = o1;
    }
    BAR();
#pragma unroll
    for (int i = 0; i < 2; ++i) {
      const int col = 16 * w + 8 * i + (lane >> 3);
      bstore_sc1(*(LAS v4u*)(L + P_WB + col * 144 + (lane & 7) * 16), rs, (64 * q + col) * WS_ + (lane & 7) * 16, OFF_WT + tg * 128);
    }
  } else {
    unsigned touch;
    {
      unsigned lo, hi;
      asm volatile("s_getpc_b64 s[20:21]\n\ts_add_u32 %0, s20, _Z6k_prep6Params@rel32@lo+4\n\ts_addc_u32 %1, s21, _Z6k_prep6Params@rel32@hi+12"
                   : "=s"(lo), "=s"(hi) :: "s20", "s21", "scc");
      const unsigned char* code = (const unsigned char*)(((unsigned long long)hi << 32) | lo);
      const int part = w < 2 ? w : 2 + ((2 * (b >> 3) + w) % 3);
      asm volatile("global_load_dword %0, %1, %2" : "=v"(touch) : "v"(part * 8192 + lane * 128 - (part == 4 ? 1536 : 0)), "s"(code));
    }
    const int feat = 32 * ft + r;
    const float step = (feat == 0 ? 0.f : feat == 1 ? 256.f : (float)(feat >> 1)) * (1.f / 512.f);
    const float ph = (feat > 1 && (feat & 1)) ? 0.25f : 0.f;
    const float a0 = ph + step * (float)(8 * h);
    const unsigned sgn = (feat > 1 && (feat & 2)) ? 0x80008000u : 0u;
    v4u F[32];
#pragma unroll
    for (int ks = 0; ks < 16; ++ks) { F[ks] = cosfrag(a0, step, 16 * ks); F[ks + 16] = F[ks] ^ sgn; }
    const float lsc = feat < 2 ? 0.04419417382415922f : 0.0625f;
#pragma unroll
    for (int ks = 0; ks < 32; ks += 8)
      asm volatile("" : "+v"(F[ks]), "+v"(F[ks + 1]), "+v"(F[ks + 2]), "+v"(F[ks + 3]), "+v"(F[ks + 4]), "+v"(F[ks + 5]), "+v"(F[ks + 6]), "+v"(F[ks + 7]));
    asm volatile("s_waitcnt vmcnt(0)" : "+v"(touch));
    PST(1);
    BAR();
    PST(2);
    {
      f32x16 aZ = zero16();
      LAS unsigned char* xa = L + P_XL + r * 1040 + h * 16;
      v4u a[4];
#pragma unroll
      for (int i = 0; i < 4; ++i) a[i] = *(LAS v4u*)(xa + i * 32);
      sfor<0, 32>([&](auto KS) {
        constexpr int ks = KS.v;
        const v4u cur = a[ks & 3];
        if constexpr (ks + 4 < 32) a[ks & 3] = *(LAS v4u*)(xa + (ks + 4) * 32);
        aZ = mfma32(cur, F[ks], aZ);
        SB();
      });
      v4u o0, o1;
      widen(aZ, lsc, o0, o1);
      LAS unsigned char* zb = L + P_ZB + w * 2560;
      *(LAS v4u*)(zb + r * 80 + h * 16) = nosent4(o0);
      *(LAS v4u*)(zb + r * 80 + h * 16 + 32) = nosent4(o1);
#pragma unroll
      for (int i = 0; i < 2; ++i)
        bstore_sc1(*(LAS v4u*)(zb + (16 * i + (lane >> 2)) * 80 + (lane & 3) * 16), rs, (32 * ft + 16 * i + (lane >> 2)) * ZS + (lane & 3) * 16,
                   OFF_Z0 + t1 * 64);
    }
    v4u I[2];
#pragma unroll
    for (int s = 0; s < 2; ++s) {
      const int e = r - 16 * s - 8 * h;
      const unsigned one = (e & 1) ? 0x3F800000u : 0x00003F80u;
      I[s] = v4u{(e >> 1) == 0 ? one : 0u, (e >> 1) == 1 ? one : 0u, (e >> 1) == 2 ? one : 0u, (e >> 1) == 3 ? one : 0u};
    }
    pin(I[0]); pin(I[1]);
    PST(3);
    BAR();
    PST(4);
    if (heavy) {
      f32x16 aG0 = zero16(), aG1 = zero16();
      LAS unsigned char* ba = L + (((lane >> 4) & 1) ? P_UL : P_GL) + (8 * h + ((lane & 15) >> 2)) * 64 + (lane & 3) * 8;
      v4u b0[2], b1[2];
#pragma unroll
      for (int i = 0; i < 2; ++i) { b0[i] = tr2(ba + i * 1024, 256); b1[i] = tr2(ba + i * 1024 + 32, 256); }
      sfor<0, 32>([&](auto KS) {
        constexpr int ks = KS.v;
        const v4u c0 = b0[ks & 1], c1 = b1[ks & 1];
        aG0 = mfma32(F[ks], c0, aG0);
        if constexpr (ks + 2 < 32) b0[ks & 1] = tr2(ba + (ks + 2) * 1024, 256);
        SB();
        aG1 = mfma32(F[ks], c1, aG1);
        if constexpr (ks + 2 < 32) b1[ks & 1] = tr2(ba + (ks + 2) * 1024 + 32, 256);
        SB();
      });
      PST(5);
      BAR();
      PST(6);
      if (ft == 0 && h == 0) { aG0[0] = 0.f; aG1[0] = 0.f; aG0[1] *= 0.70710678118654752f; aG1[1] *= 0.70710678118654752f; }
      const int g0 = 2 * tg, g1 = 2 * tg + 1;
      const int o0 = OFF_W1 + ((g0 / 7) * 8 + g0 % 7) * 32768 + 2 * ft * 1024, o1 = OFF_W1 + ((g1 / 7) * 8 + g1 % 7) * 32768 + 2 * ft * 1024;
      f32x16 aD = zero16(), aT = zero16();
      LAS unsigned char* da = L + P_DL + r * 1040 + h * 16;
      aT = mfma32(*(LAS v4u*)(da + ft * 64), I[0], aT);
      aT = mfma32(*(LAS v4u*)(da + ft * 64 + 32), I[1], aT);
      v4u a[4];
#pragma unroll
      for (int i = 0; i < 4; ++i) a[i] = *(LAS v4u*)(da + i * 32);
      sfor<0, 32>([&](auto KS) {
        constexpr int ks = KS.v;
        const v4u cur = a[ks & 3];
        if constexpr (ks + 4 < 32) a[ks & 3] = *(LAS v4u*)(da + (ks + 4) * 32);
        aD = mfma32(cur, F[ks], aD);
        if constexpr (ks == 1 || ks == 6) bstore_sc1(pack8(aG0, ks > 1, 0.0625f), rs, lane * 16, o0 + (ks > 1) * 1024);
        if constexpr (ks == 11 || ks == 16) bstore_sc1(pack8(aG1, ks > 11, 0.0625f), rs, lane * 16, o1 + (ks > 11) * 1024);
        if constexpr (ks == 21 || ks == 26) bstore_sc1(pack8(aT, ks > 21, 1.f), rs, lane * 16, OFF_W3 + (ft * 112 + 2 * tg + (ks > 21)) * 1024);
        SB();
      });
#pragma unroll
      for (int s = 0; s < 2; ++s) bstore_sc1(pack8(aD, s, lsc), rs, lane * 16, OFF_W2 + (ft * 112 + 2 * tg + s) * 1024);
    } else {
#pragma unroll
      for (int i = 0; i < 2; ++i) {
        const int dt = 2 * xcd + i;
        const float b0 = __builtin_amdgcn_fractf(__builtin_fmaf(step, (float)(32 * dt), a0));
        f32x16 aI = mfma32(cosfrag(b0, step, 0), I[0], zero16());
        aI = mfma32(cosfrag(b0, step, 16), I[1], aI);
        if (ft == 0 && h == 0) { aI[0] *= 0.70710678118654752f; aI[1] *= 0.70710678118654752f; }
#pragma unroll
        for (int s = 0; s < 2; ++s) bstore_sc1(pack8(aI, s, 0.0625f), rs, lane * 16, OFF_W1 + (dt * 8 + 7) * 32768 + (2 * ft + s) * 1024);
      }
      BAR();
    }
  }
}

constexpr int M_Z = 0;
constexpr int M_RB = 40960;
constexpr int M_TB = 81920;
constexpr int M_S1 = 131072;
constexpr int M_S2 = M_S1 + 2048;
constexpr int M_C1 = M_S2 + 1024;
constexpr int M_C2 = M_C1 + 2 * 4608;
constexpr int M_LDS = M_C2 + 2 * 4608;
#define STAMP(i)
#define KSTAMP(i)
#define CSTAMP(i)
#define RETRY(i)
#define DELAY_A 6
#define DELAY_C 2

template <int IMM> DI void ldx4(v4u& dst, const v4u& srd, int voff, int soff) {
  asm volatile("buffer_load_dwordx4 %0, %1, %2, %3 offen offset:%4 sc1" : "=v"(dst) : "v"(voff), "s"(srd), "s"(soff), "i"(IMM));
}
struct WSrc { v4u srd, srd1; int lane16, base; };
template <int N> DI void load_w(v4u (&W)[64], const WSrc& s) {
  wload<((N >> 1) & 3) * 1024, false>(W[N], (N & 1) ? s.srd1 : s.srd, s.lane16, s.base + (N & 1) * 32768 + (N >> 3) * 4096);
}
template <int LO, int HI> DI void load_ws(v4u (&W)[64], const WSrc& s) { SB(); sfor<LO, HI>([&](auto J) { load_w<J.v>(W, s); }); SB(); }
DI unsigned mxacc(unsigned m, const v4u& v) { return umax(umax(umax(umax(m, v.x), v.y), v.z), v.w); }
DI unsigned long long sent_mask(unsigned m) { return __builtin_amdgcn_ballot_w64(m == SENT); }
DI void pins(unsigned long long& k) { asm volatile("" : "+s"(k)); }
DI void pinf(float& a) { asm volatile("" : "+v"(a)); }
DI void pinu(unsigned& a) { asm volatile("" : "+v"(a)); }

struct ColConst { v4u C1[2], S1[2], N1[2], C2[2], S2[2], N2[2]; float tc[8], ts[8]; };

DI void col_consts(ColConst& k, int w, int lane, v4u (&W)[64], const WSrc& ws) {
  const int g = lane >> 4, r = lane & 15;
  const float k1 = (float)(16 * w + r);
  const unsigned neg = 0x80008000u;
  sfor<0, 2>([&](auto CS) {
    constexpr int cs = CS.v;
    const float st = k1 * (1.f / 64.f), a0 = st * (float)(8 * g);
    load_ws<3 * cs, 3 * cs + 2>(W, ws);
    k.C1[cs] = cosfrag(a0, st, 32 * cs);
    pin(k.C1[cs]);
    load_ws<3 * cs + 2, 3 * cs + 3>(W, ws);
    k.S1[cs] = cosfrag(a0 - 0.25f, st, 32 * cs);
    k.N1[cs] = k.S1[cs] ^ neg;
    pin(k.S1[cs]); pin(k.N1[cs]);
  });
  sfor<0, 2>([&](auto T) {
    constexpr int t = T.v;
    const float st = (float)(16 * t + r) * (1.f / 32.f), a0 = st * (float)(4 * g);
    float c[8], s[8];
    load_ws<6 + 3 * t, 8 + 3 * t>(W, ws);
#pragma unroll
    for (int j = 0; j < 8; ++j) c[j] = __builtin_amdgcn_cosf(__builtin_fmaf(st, (float)(16 * (j >> 2) + (j & 3)), a0));
    k.C2[t] = v4u{pk(c[0], c[1]), pk(c[2], c[3]), pk(c[4], c[5]), pk(c[6], c[7])};
    pin(k.C2[t]);
    load_ws<8 + 3 * t, 9 + 3 * t>(W, ws);
#pragma unroll
    for (int j = 0; j < 8; ++j) s[j] = __builtin_amdgcn_sinf(__builtin_fmaf(st, (float)(16 * (j >> 2) + (j & 3)), a0));
    k.S2[t] = v4u{pk(s[0], s[1]), pk(s[2], s[3]), pk(s[4], s[5]), pk(s[6], s[7])};
    k.N2[t] = k.S2[t] ^ neg;
    pin(k.S2[t]); pin(k.N2[t]);
  });
  sfor<0, 2>([&](auto T) {
    constexpr int t = T.v;
    load_ws<12 + 2 * t, 14 + 2 * t>(W, ws);
#pragma unroll
    for (int e = 4 * t; e < 4 * t + 4; ++e) {
      const float a = k1 * (1.f / 2048.f) * (float)(16 * (e >> 2) + 4 * g + (e & 3));
      k.tc[e] = __builtin_amdgcn_cosf(a); k.ts[e] = __builtin_amdgcn_sinf(a);
      pinf(k.tc[e]); pinf(k.ts[e]);
    }
  });
}

template <bool FIRST> DI void m16(f32x4& acc, const v4u& a, const v4u& b) {
  SB();
  if constexpr (FIRST) asm volatile("v_mfma_f32_16x16x32_bf16 %0, %1, %2, 0" : "=&v"(acc) : "v"(a), "v"(b));
  else asm volatile("v_mfma_f32_16x16x32_bf16 %0, %1, %2, %0" : "+v"(acc) : "v"(a), "v"(b));
  SB();
}
DI void m16_done(f32x4& a, f32x4& b) { asm volatile("s_nop 7" : "+v"(a), "+v"(b)); }
DI void m16_ready(v4u& a, v4u& b) { asm volatile("s_nop 1" : "+v"(a), "+v"(b)); }
template <int PART> DI float cmul(float a, float b, float c, float s) {
  float r = PART == 0 ? __builtin_fmaf(a, c, -(b * s)) : __builtin_fmaf(a, s, b * c);
  pinf(r);
  return r;
}

template <int WB>
DI void col_compute(LAS unsigned char* L, rsrc_t rs, const ColConst& k, const float (&fr)[8], const float (&fi)[8], v4u zr, v4u zi,
                    const float (&rstd)[8], int c, int w, int lane, int out_off, v4u (&W)[64], const WSrc& ws) {
  const int g = lane >> 4, r = lane & 15;
  const int first = c == 0;
  const float f0 = first ? 0.f : 1.f;
  float xr[8], xi[8];
#pragma unroll
  for (int i = 0; i < 4; ++i) { xr[2 * i] = blo(zr[i]); xr[2 * i + 1] = bhi(zr[i]); xi[2 * i] = blo(zi[i]); xi[2 * i + 1] = bhi(zi[i]); }
#pragma unroll
  for (int t = 0; t < 2; ++t) {
    const f32x4 rv = {rstd[4 * t], rstd[4 * t + 1], rstd[4 * t + 2], rstd[4 * t + 3]};
    const f32x4 a = f32x4{xr[4 * t], xr[4 * t + 1], xr[4 * t + 2], xr[4 * t + 3]} * rv;
    const f32x4 bq = f32x4{xi[4 * t], xi[4 * t + 1], xi[4 * t + 2], xi[4 * t + 3]} * (rv * f0);
    LAS unsigned char* d = L + M_C1 + (r + 16 * t) * 144 + (16 * w + 4 * g) * 2;
    *(LAS v2u*)d = v2u{pk(a[0], a[1]), pk(a[2], a[3])};
    *(LAS v2u*)(d + 4608) = v2u{pk(bq[0], bq[1]), pk(bq[2], bq[3])};
  }
  load_ws<WB, WB + 3>(W, ws);
  BAR();
  f32x4 dr[2], di[2];
  float ur[8], ui[8];
  {
    v4u ar[2][2], ai[2][2];
#pragma unroll
    for (int t = 0; t < 2; ++t)
#pragma unroll
      for (int cs = 0; cs < 2; ++cs) {
        LAS unsigned char* s = L + M_C1 + (r + 16 * t) * 144 + (32 * cs + 8 * g) * 2;
        ar[t][cs] = *(LAS v4u*)s; ai[t][cs] = *(LAS v4u*)(s + 4608);
      }
    load_ws<WB + 3, WB + 5>(W, ws);
    sfor<0, 16>([&](auto N) {
      constexpr int n = N.v, t = n >> 3, cs = (n >> 2) & 1, m = n & 3;
      if constexpr (m == 0) m16<cs == 0>(dr[t], ar[t][cs], k.C1[cs]);
      if constexpr (m == 1) m16<cs == 0>(di[t], ai[t][cs], k.C1[cs]);
      if constexpr (m == 2) m16<false>(dr[t], ai[t][cs], k.S1[cs]);
      if constexpr (m == 3) m16<false>(di[t], ar[t][cs], k.N1[cs]);
      if constexpr (m == 3) load_w<WB + 5 + (n >> 2)>(W, ws);
      if constexpr (n >= 9) {
        constexpr int e = (n - 9) >> 1;
        if constexpr (n & 1) ur[e] = cmul<0>(dr[0][e], di[0][e], k.tc[e], -k.ts[e]);
        else ui[e] = cmul<1>(dr[0][e], di[0][e], k.tc[e], -k.ts[e]);
      }
    });
    ui[3] = cmul<1>(dr[0][3], di[0][3], k.tc[3], -k.ts[3]);
  }
  v4u br, bi;
  br.x = pk(ur[0], ur[1]); br.y = pk(ur[2], ur[3]); bi.x = pk(ui[0], ui[1]); bi.y = pk(ui[2], ui[3]);
  SB();
  m16_done(dr[1], di[1]);
  load_ws<WB + 9, WB + 11>(W, ws);
#pragma unroll
  for (int e = 4; e < 8; ++e) {
    ur[e] = dr[1][e - 4] * k.tc[e] + di[1][e - 4] * k.ts[e]; ui[e] = di[1][e - 4] * k.tc[e] - dr[1][e - 4] * k.ts[e];
  }
  br.z = pk(ur[4], ur[5]); br.w = pk(ur[6], ur[7]); bi.z = pk(ui[4], ui[5]); bi.w = pk(ui[6], ui[7]);
  m16_ready(br, bi);
  sfor<0, 8>([&](auto N) {
    constexpr int n = N.v, t = n >> 2, m = n & 3;
    if constexpr (m == 0) m16<true>(dr[t], k.C2[t], br);
    if constexpr (m == 1) m16<true>(di[t], k.C2[t], bi);
    if constexpr (m == 2) m16<false>(dr[t], k.S2[t], bi);
    if constexpr (m == 3) m16<false>(di[t], k.N2[t], br);
    if constexpr (m == 3) load_w<WB + 11 + t>(W, ws);
    if constexpr (n == 5) ur[0] = cmul<0>(dr[0][0], di[0][0], fr[0], fi[0]);
    if constexpr (n == 6) ui[0] = cmul<1>(dr[0][0], di[0][0], fr[0], fi[0]);
  });
#pragma unroll
  for (int e = 1; e < 4; ++e) { ur[e] = dr[0][e] * fr[e] - di[0][e] * fi[e]; ui[e] = dr[0][e] * fi[e] + di[0][e] * fr[e]; }
  br.x = pk(ur[0], ur[1]); br.y = pk(ur[2], ur[3]); bi.x = pk(ui[0], ui[1]); bi.y = pk(ui[2], ui[3]);
  SB();
  m16_done(dr[1], di[1]);
  load_ws<WB + 13, WB + 15>(W, ws);
#pragma unroll
  for (int e = 4; e < 8; ++e) { ur[e] = dr[1][e - 4] * fr[e] - di[1][e - 4] * fi[e]; ui[e] = dr[1][e - 4] * fi[e] + di[1][e - 4] * fr[e]; }
  br.z = pk(ur[4], ur[5]); br.w = pk(ur[6], ur[7]); bi.z = pk(ui[4], ui[5]); bi.w = pk(ui[6], ui[7]);
  m16_ready(br, bi);
  sfor<0, 8>([&](auto N) {
    constexpr int n = N.v, t = n >> 2, m = n & 3;
    if constexpr (m == 0) m16<true>(dr[t], k.C2[t], br);
    if constexpr (m == 1) m16<true>(di[t], k.C2[t], bi);
    if constexpr (m == 2) m16<false>(dr[t], k.N2[t], bi);
    if constexpr (m == 3) m16<false>(di[t], k.S2[t], br);
    if constexpr (m == 3) load_w<WB + 15 + t>(W, ws);
    if constexpr (n == 5) ur[0] = cmul<0>(dr[0][0], di[0][0], k.tc[0], k.ts[0]);
    if constexpr (n == 6) ui[0] = cmul<1>(dr[0][0], di[0][0], k.tc[0], k.ts[0]);
  });
#pragma unroll
  for (int t = 0; t < 2; ++t) {
    if (t == 1) { SB(); m16_done(dr[1], di[1]); load_ws<WB + 17, WB + 19>(W, ws); }
#pragma unroll
    for (int i = (t == 0 ? 1 : 0); i < 4; ++i) {
      const int e = 4 * t + i;
      ur[e] = dr[t][i] * k.tc[e] - di[t][i] * k.ts[e]; ui[e] = di[t][i] * k.tc[e] + dr[t][i] * k.ts[e];
    }
    LAS unsigned char* d = L + M_C2 + (16 * w + r) * 72 + (16 * t + 4 * g) * 2;
    *(LAS v2u*)d = v2u{pk(ur[4 * t], ur[4 * t + 1]), pk(ur[4 * t + 2], ur[4 * t + 3])};
    *(LAS v2u*)(d + 4608) = v2u{pk(ui[4 * t], ui[4 * t + 1]), pk(ui[4 * t + 2], ui[4 * t + 3])};
  }
  load_ws<WB + 19, WB + 21>(W, ws);
  BAR();
  v4u o0, o1;
  {
    v4u tr_[2][2], ti_[2][2];
#pragma unroll
    for (int t = 0; t < 2; ++t)
#pragma unroll
      for (int cs = 0; cs < 2; ++cs) {
        LAS unsigned char* s = L + M_C2 + (32 * cs + 8 * g + (r >> 2)) * 72 + (16 * t + 4 * (r & 3)) * 2;
        tr_[t][cs] = tr2(s, 4 * 72); ti_[t][cs] = tr2(s + 4608, 4 * 72);
      }
    load_ws<WB + 21, WB + 23>(W, ws);
    sfor<0, 16>([&](auto N) {
      constexpr int n = N.v, t = n >> 3, cs = (n >> 2) & 1, m = n & 3;
      if constexpr (m == 0) m16<cs == 0>(dr[t], k.C1[cs], tr_[t][cs]);
      if constexpr (m == 1) m16<cs == 0>(di[t], k.C1[cs], ti_[t][cs]);
      if constexpr (m == 2) m16<false>(dr[t], k.N1[cs], ti_[t][cs]);
      if constexpr (m == 3) m16<false>(di[t], k.S1[cs], tr_[t][cs]);
      if constexpr (m == 3) load_w<WB + 23 + (n >> 2)>(W, ws);
      if constexpr (n >= 9 && n < 13) {
        constexpr int e = n - 9;
        xr[e] += dr[0][e]; xi[e] = __builtin_fmaf(di[0][e], f0, xi[e]);
        pinf(xr[e]); pinf(xi[e]);
      }
      if constexpr (n == 13) { o0.x = nosent(pk(xr[0], xr[1])); pinu(*(unsigned*)&o0); }
      if constexpr (n == 14) { o0.y = nosent(pk(xr[2], xr[3])); o1.x = nosent(pk(xi[0], xi[1])); }
    });
    o1.y = nosent(pk(xi[2], xi[3]));
    SB();
    m16_done(dr[1], di[1]);
#pragma unroll
    for (int e = 4; e < 8; ++e) { xr[e] += dr[1][e - 4]; xi[e] = __builtin_fmaf(di[1][e - 4], f0, xi[e]); }
    o0.z = nosent(pk(xr[4], xr[5])); o0.w = nosent(pk(xr[6], xr[7])); o1.z = nosent(pk(xi[4], xi[5])); o1.w = nosent(pk(xi[6], xi[7]));
  }
  const int vo = (w * 64 + lane) * 16;
  bstore_sc1(o0, rs, vo, out_off + (2 * c + first) * ZS);
  bstore_sc1(o1, rs, vo, out_off + (2 * c + 1 - first) * ZS);
  load_ws<WB + 27, WB + 30>(W, ws);
}

constexpr int NW_LATE = 8;
constexpr int NV_EARLY = 16;
constexpr int ZG[6] = {0, 2, 4, 8, 16, 32};
constexpr int ZR[5] = {-1, 1, 4, 10, 21};
constexpr int z_read_at(int ks) {
  for (int G = 1; G < 5; ++G) if (ks >= ZR[G] && 2 * (ks - ZR[G]) < ZG[G + 1] - ZG[G]) return ZG[G] + 2 * (ks - ZR[G]);
  return -1;
}
constexpr int z_first(int j) { for (int G = 0; G < 5; ++G) if (ZG[G] == j) return 1; return 0; }
constexpr int z_decide_at(int ks) { for (int G = 1; G < 5; ++G) if (ks == ZR[G] + (ZG[G + 1] - ZG[G]) / 2) return G; return -1; }
constexpr int z_requested(int ks) { return ks < 8 ? 9 + ks : ks < 16 ? 2 * ks + 1 : 32; }
constexpr int z_younger(int j, int ks) {
  int n = z_requested(ks) - j - 1;
  for (int k = 0; k < ks; ++k) if ((k < 8 && j < 16 && k >= j - 8) || k >= 16) ++n;
  return n;
}
struct ZSrc { v4u srd; int vo, so; unsigned lds; };
template <int J> DI void z_issue(const ZSrc& z, int so) {
  asm volatile("s_add_u32 m0, %0, %4\n\ts_nop 0\n\tbuffer_load_dwordx4 %1, %2, %3 offen sc1 lds"
               :: "s"(z.lds), "v"(z.vo), "s"(z.srd), "s"(so + (J & 15) * 32 * ZS + (J >> 4) * 128), "i"((J >> 4) * 65536 + (J & 15) * 4096)
               : "memory", "scc");
}
template <int J> DI v4u z_back(LAS unsigned char* zown) { return *(LAS v4u*)(zown + (J >> 4) * 65536 + (J & 15) * 4096); }
template <int G, int KS> DI void z_fix(unsigned long long bad, const ZSrc& z, LAS unsigned char* zown, LAS unsigned char* L, int layer, int w) {
  if (__builtin_expect(bad != 0ull, 0)) {
    unsigned spin = 0, m;
    vmwait<0>();
    do {
      RETRY(G == 0 ? 24 : 25);
      const int zo = opaque(z.so);
      sfor<ZG[G], z_requested(KS)>([&](auto I) { z_issue<I.v>(z, zo); });
      vmwait<0>();
      m = 0;
      sfor<ZG[G], ZG[G + 1]>([&](auto I) { m = mxacc(m, z_back<I.v>(zown)); });
    } while (any_sent(m) && ++spin < SPIN_MAX);
  }
}

#define HA_START 14
#define HB_START 6
#define H_ROOM 16
constexpr int NSTR = 56 - NV_EARLY + 58;
struct Stream { int kind[NSTR], idx[NSTR], end[89]; };
constexpr Stream make_stream() {
  Stream s{};
  int kind[NSTR] = {}, idx[NSTR] = {}, rel[NSTR] = {}, need[NSTR] = {};
  bool done[NSTR] = {};
  int n = 0;
  for (int i = NV_EARLY; i < 56; ++i) { kind[n] = 0; idx[n] = i; rel[n] = (i - NV_EARLY) >> 1; need[n] = i >> 1; ++n; }
  for (int i = 0; i < 28; ++i) { kind[n] = 1; idx[n] = i; rel[n] = HA_START; need[n] = i; ++n; }
  for (int i = 0; i < 28; ++i) { kind[n] = 2; idx[n] = i; rel[n] = 33 + HB_START; need[n] = 28 + i; ++n; }
  for (int i = 0; i < 2; ++i) { kind[n] = 3; idx[n] = i; rel[n] = 33 + HB_START; need[n] = 60; ++n; }
  int e = 0, held = 0;
  for (int sl = 0; sl < 89; ++sl) {
    int cap = sl < 8 ? 1 : sl < 32 ? 2 - (sl % 4 == 3) : sl == 32 ? 6 : (sl - 33) % 7 == 6 ? 0 : 2;
    for (; cap > 0; --cap) {
      int best = -1;
      const int live = held - (sl < 33 ? 0 : sl - 32), room = sl < 32 ? H_ROOM : H_ROOM + 6;
      for (int i = 0; i < NSTR; ++i)
        if (!done[i] && rel[i] <= sl && (kind[i] == 0 || live < room) && (best < 0 || need[i] < need[best])) best = i;
      if (best < 0) break;
      if (kind[best] != 0) ++held;
      done[best] = true; s.kind[e] = kind[best]; s.idx[e] = idx[best]; ++e;
    }
    s.end[sl] = e;
  }
  return s;
}
constexpr Stream STR = make_stream();
static_assert(STR.end[88] == NSTR, "every load is issued");
constexpr int str_pos(int kind, int idx) { for (int i = 0; i < NSTR; ++i) if (STR.kind[i] == kind && STR.idx[i] == idx) return i; return -1; }
constexpr int str_count(int kind, int upto) { int n = 0; for (int i = 0; i < upto; ++i) if (STR.kind[i] == kind) ++n; return n; }
struct Down {
  int vbase, hbase, xvo, xso;
  DI int blk(int g) const { return g * 4096; }
};
template <int SLOT, int PART> DI void stream_issue(v4u (&W)[64], v4u (&hA)[28], v4u (&hB)[28], v4u (&x3)[2], const v4u& srd, int lane16, const Down& d) {
  constexpr int lo = SLOT == 0 ? 0 : STR.end[SLOT - 1], hi = STR.end[SLOT];
  sfor<(PART == 1 ? lo + 1 : lo), (PART == 0 && hi > lo ? lo + 1 : hi)>([&](auto I) {
    constexpr int k = STR.kind[I.v], i = STR.idx[I.v];
    if constexpr (k == 0) wload<((i >> 1) & 3) * 1024, true>(W[i - NV_EARLY], srd, lane16, d.vbase + (i & 1) * 112 * 1024 + d.blk(i >> 3));
    if constexpr (k == 1) ldx4<(i & 3) * 1024>(hA[i], srd, lane16, d.hbase + d.blk(i >> 2));
    if constexpr (k == 2) ldx4<(i & 3) * 1024>(hB[i], srd, lane16, d.hbase + 224 * 1024 + d.blk(i >> 2));
    if constexpr (k == 3) ldx4<0>(x3[i], srd, d.xvo, d.xso + i * 64 * XS);
  });
}
constexpr int dn_need(int g) {
  const int H = g / 28, i = g % 28;
  const int ph = str_pos(1 + H, i), pv = H == 0 && 2 * i >= NV_EARLY ? str_pos(0, 2 * i + 1) : 0;
  return ph > pv ? ph : pv;
}
template <int G, int ISSUED> DI void h_acc(v4u (&hA)[28], v4u (&hB)[28], unsigned& m) {
  static_assert(dn_need(G) < ISSUED, "requested in time");
  vmwait<ISSUED - 1 - dn_need(G)>();
  v4u& x = G < 28 ? hA[G % 28] : hB[G % 28];
  pin(x);
  m = mxacc(m, x);
  pinu(m);
}
template <int G, int ISSUED> DI void h_decide(v4u (&hA)[28], v4u (&hB)[28], unsigned long long bad, const v4u& srd, int lane16, const Down& d,
                                              LAS unsigned char* L, int layer, int w) {
  constexpr int H = G / 28, LO = G % 28;
  v4u (&hh)[28] = H == 0 ? hA : hB;
  if (__builtin_expect(bad != 0ull, 0)) {
    unsigned spin = 0, m;
    vmwait<0>();
    do {
      RETRY(26 + H);
      const int bo = opaque(d.hbase);
      sfor<LO, str_count(1 + H, ISSUED)>([&](auto I) { ldx4<(I.v & 3) * 1024>(hh[I.v], srd, lane16, bo + H * 224 * 1024 + d.blk(I.v >> 2)); });
      vmwait<0>();
      m = 0;
#pragma unroll
      for (int i = LO; i < LO + 7; ++i) { pin(hh[i]); m = mxacc(m, hh[i]); }
    } while (any_sent(m) && ++spin < SPIN_MAX);
  }
}

DI void sumsq1(float& ss, unsigned a) {
  const float x = blo(a), y = bhi(a);
  asm volatile("v_fmac_f32 %0, %1, %1\n\tv_fmac_f32 %0, %2, %2" : "+v"(ss) : "v"(x), "v"(y));
}
DI float mulf(float a, float b) { float r; asm volatile("v_mul_f32 %0, %1, %2" : "=v"(r) : "v"(a), "v"(b)); return r; }
DI void addf4(f32x4& a, const f32x4& b) {
  float x = a.x, y = a.y, z = a.z, t = a.w;
  asm volatile("v_add_f32 %0, %0, %4\n\tv_add_f32 %1, %1, %5\n\tv_add_f32 %2, %2, %6\n\tv_add_f32 %3, %3, %7"
               : "+v"(x), "+v"(y), "+v"(z), "+v"(t) : "v"(b.x), "v"(b.y), "v"(b.z), "v"(b.w));
  a = f32x4{x, y, z, t};
}
DI float relu(float a) { const int i = __builtin_bit_cast(int, a); return __builtin_bit_cast(float, i > 0 ? i : 0); }
DI void h_piece(const f32x16& a, rsrc_t rs, int lane, int so) {
  v4u d;
#pragma unroll
  for (int i = 0; i < 4; ++i) d[i] = nosent(pk(relu(a[2 * i]) * a[2 * i + 8], relu(a[2 * i + 1]) * a[2 * i + 9]));
  bstore_sc1(d, rs, lane * 16, so);
}
DI void x3_piece(const f32x16& a, rsrc_t rs, int lane, int so) {
  v4u o0, o1;
  widen(a, 1.f, o0, o1);
  bstore_sc1(nosent4(o0), rs, (lane & 31) * XS + (lane >> 5) * 16, so);
  bstore_sc1(nosent4(o1), rs, (lane & 31) * XS + (lane >> 5) * 16, so + 32);
}

DI void ffn(LAS unsigned char* L, rsrc_t rs, const WSrc& ws, const Params& p, v4u (&W)[64], int layer, int c, int rb, int fs, int pi, int nb,
            int w, int lane) {
  const v4u& srd = ws.srd;
  const int hoff = OFF_H1 + layer * (OFF_H2 - OFF_H1);
  const int h = lane >> 5, r = lane & 31, q = (lane & 15) >> 2, lane16 = lane * 16;
  ZSrc z;
  z.srd = srd;
  z.vo = (8 * w + (lane >> 3)) * ZS + (((lane & 7) ^ ((lane >> 2) & 4)) * 16);
  z.so = OFF_Z1 + layer * (OFF_Z3 - OFF_Z1) + rb * 256;
  z.lds = (unsigned)(size_t)L + M_Z + w * 1024;
  STAMP(4);
  sfor<0, 8>([&](auto I) { z_issue<I.v>(z, z.so); });
  SB();
  LAS unsigned char* zown = L + M_Z + w * 1024 + lane16;
  LAS unsigned char* zr[2];
#pragma unroll
  for (int s = 0; s < 2; ++s)
    zr[s] = L + M_Z + (4 * h + q) * 128 + (4 * (s ^ (q >> 1)) + 2 * ((lane >> 4) & 1) + ((lane & 3) >> 1)) * 16 + 8 * (lane & 1);
  const unsigned dcmask = (w == 0 && h == 0) ? 0xFFFF0000u : 0xFFFFFFFFu;
  const int pt = 4 * rb + pi;
  Down dn;
  dn.vbase = OFF_W2 + layer * (OFF_W3 - OFF_W2) + (nb * 2 * 112 + 28 * w) * 1024;
  dn.hbase = hoff + (pt * 112 + 28 * w) * 1024;
  dn.xvo = (lane & 31) * XS + h * 16;
  dn.xso = OFF_Z0 + layer * (OFF_X3 - OFF_Z0) + pt * 32 * XS + nb * 128 + w * 32;
  const int ks0 = fs * 7 + 2 * w;
  const bool two = w < 3;
  const bool idft = opaque(w + layer) == 4;
  const rsrc_t rs2 = __builtin_amdgcn_make_buffer_rsrc((void*)p.ws, 0, two ? WS_BYTES : 0, 0x00020000);
  v4u hA[28], hB[28], x3[2], res[2], Ve[NV_EARLY];
  unsigned s0, s1, s2, s3;
  asm volatile("v_mov_b32 %0, -1\n\tv_mov_b32 %1, -1\n\tv_mov_b32 %2, -1\n\tv_mov_b32 %3, -1" : "=v"(s0), "=v"(s1), "=v"(s2), "=v"(s3));
  const v4u sv = {s0, s1, s2, s3};
  const rsrc_t rsf = __builtin_amdgcn_make_buffer_rsrc((void*)p.ws, 0, layer == 0 ? WS_BYTES : 0, 0x00020000);
  const int fo_h = lane16 + OFF_H2 + ((4 * rb + w) * 112 + fs * 7) * 1024, fo_z = (w * 64 + lane) * 16 + OFF_Z3 + 2 * c * ZS,
            fo_x = (w * 16 + (lane >> 2)) * XS + (lane & 3) * 16 + OFF_X3 + 128 * rb * XS + fs * 64;
  STAMP(5);
  vmwait<z_younger(1, -1)>();
  z_fix<0, -1>(sent_mask(mxacc(mxacc(0u, z_back<0>(zown)), z_back<1>(zown))), z, zown, L, layer, w);
  STAMP(6);
  BAR();
  STAMP(7);
  CSTAMP(20);
  f32x16 acc[2][2][2];
  float ss[2] = {0.f, 0.f};
  sfor<0, 2>([&](auto HH) {
    constexpr int H = HH.v;
    v4u bf[2][2], sq[2], hq, rbk[2];
    float hp[4];
    unsigned zm;
    unsigned long long bad;
    LAS unsigned char* zb[2] = {zr[0] + H * 65536, zr[1] + H * 65536};
    asm volatile("" : "+v"(zb[0]), "+v"(zb[1]));
    LAS unsigned char* zq[2];
    if constexpr (H == 1) { zq[0] = (pi ? zr[1] : zr[0]) + w * 16384; zq[1] = zq[0] + 65536; asm volatile("" : "+v"(zq[0]), "+v"(zq[1])); }
#pragma unroll
    for (int s = 0; s < 2; ++s) bf[0][s] = tr2(zb[s], 1024);
    sfor<0, 32>([&](auto KS) {
      constexpr int ks = KS.v;
      constexpr bool edge = H == 0 && (ks == 3 || ks == 7 || ks == 15);
      constexpr int e = ks >> 1, es = e & 1, et = e >> 1;
      constexpr int sr = H == 1 && ks >= 8 && (ks - 8) % 3 < 2 ? 2 * ((ks - 8) / 3) + (ks - 8) % 3 : -1;
      constexpr int sn = H == 1 && ks >= 9 && (ks - 9) % 3 < 2 ? 2 * ((ks - 9) / 3) + (ks - 9) % 3 : -1;
      constexpr int rd = H == 0 ? z_read_at(ks) : -1, rp = H == 0 && ks > 0 ? z_read_at(ks - 1) : -1, dec = H == 0 ? z_decide_at(ks) : -1;
      mfma_w<ks == 0, true>(acc[H][0][0], W[2 * ks], bf[ks & 1][0]);
      SB();
      if constexpr (ks + 1 < 32 && !edge) bf[(ks + 1) & 1][0] = tr2(zb[0] + (ks + 1) * 2048, 1024);
      if constexpr (rp >= 0) { zm = mxacc(z_first(rp) ? 0u : zm, rbk[0]); pinu(zm); }
      if constexpr (H == 1 && ks < 8) hp[0] = mulf(relu(acc[0][et][es][4 * (ks & 1)]), acc[0][et][es][4 * (ks & 1) + 8]);
      if constexpr (H == 1 && ks < 8) hp[1] = mulf(relu(acc[0][et][es][4 * (ks & 1) + 1]), acc[0][et][es][4 * (ks & 1) + 9]);
      if constexpr (sn >= 0) sumsq1(ss[sn & 1], sn < 2 ? sq[sn & 1].x & dcmask : sq[sn & 1].x);
      SB();
      mfma_w<ks == 0, true>(acc[H][1][0], W[2 * ks + 1], bf[ks & 1][0]);
      SB();
      if constexpr (H == 0 && ks < 16) z_issue<(ks < 8 ? 8 + ks : 2 * ks)>(z, z.so);
      if constexpr (H == 1 && ks < 8) hp[2] = mulf(relu(acc[0][et][es][4 * (ks & 1) + 2]), acc[0][et][es][4 * (ks & 1) + 10]);
      if constexpr (H == 1 && ks < 8) hp[3] = mulf(relu(acc[0][et][es][4 * (ks & 1) + 3]), acc[0][et][es][4 * (ks & 1) + 11]);
      if constexpr (sn >= 0) sumsq1(ss[sn & 1], sq[sn & 1].y);
      if constexpr (H == 1 && ks > 0) stream_issue<ks - 1, 1>(W, hA, hB, x3, srd, lane16, dn);
      if constexpr (H == 0 && ks >= 16 && (ks >= 26 || !(ks & 1))) {
        constexpr int i = ks < 26 ? (ks - 16) / 2 : ks - 21;
        if constexpr (i < 7) bstore_sc1(sv, rsf, fo_h, i * 1024);
        else if constexpr (i < 9) bstore_sc1(sv, rsf, fo_z, (i - 7) * ZS);
        else bstore_sc1(sv, rsf, fo_x, (i - 9) * 64 * XS);
      }
      SB();
      mfma_w<ks == 0, true>(acc[H][0][1], W[2 * ks], bf[ks & 1][1]);
      SB();
      if constexpr (ks + 1 < 32 && !edge) bf[(ks + 1) & 1][1] = tr2(zb[1] + (ks + 1) * 2048, 1024);
      if constexpr (rp >= 0) { zm = mxacc(zm, rbk[1]); pinu(zm); }
      if constexpr (dec >= 0) { bad = sent_mask(zm); pins(bad); }
      if constexpr (H == 1 && ks < 8) { hq[2 * (ks & 1)] = nosent(pk(hp[0], hp[1])); hq[2 * (ks & 1) + 1] = nosent(pk(hp[2], hp[3])); }
      if constexpr (sn >= 0) sumsq1(ss[sn & 1], sq[sn & 1].z);
      SB();
      mfma_w<ks == 0, true>(acc[H][1][1], W[2 * ks + 1], bf[ks & 1][1]);
      SB();
      if constexpr (dec >= 0) { pins(bad); z_fix<dec, ks>(bad, z, zown, L, layer, w); }
      if constexpr (rd >= 0) { vmwait<z_younger(rd + 1, ks)>(); rbk[0] = z_back<rd>(zown); rbk[1] = z_back<rd + 1>(zown); }
      if constexpr (sn >= 0) sumsq1(ss[sn & 1], sq[sn & 1].w);
      if constexpr (sr >= 0) sq[sr & 1] = tr2(zq[sr & 1] + (sr >> 1) * 2048, 1024);
      if constexpr (H == 0 && ks < NW_LATE) load_w<64 - NW_LATE + ks>(W, ws);
      if constexpr (H == 0 && ks >= 8 && ks < 16) z_issue<2 * ks + 1>(z, z.so);
      if constexpr (H == 0 && ks >= 16) {
        constexpr int n = ks - 16;
        vload<((n >> 1) & 3) * 1024>(Ve[n], srd, lane16, dn.vbase + (n & 1) * 112 * 1024 + dn.blk(n >> 3));
      }
      if constexpr (H == 1) {
        if constexpr (ks < 8 && (ks & 1)) {
          bstore_sc1(hq, et == 0 ? rs : rs2, lane16, hoff + ((4 * rb + es) * 112 + ks0 + et) * 1024);
          if constexpr (et == 1) { if (idft) x3_piece(acc[0][1][es], rs, lane, OFF_X3 + (128 * rb + 32 * es) * XS + fs * 64); }
        }
        stream_issue<ks, (ks == 31 ? 2 : 0)>(W, hA, hB, x3, srd, lane16, dn);
      }
      if constexpr (edge) {
        BAR();
#pragma unroll
        for (int s = 0; s < 2; ++s) bf[(ks + 1) & 1][s] = tr2(zb[s] + (ks + 1) * 2048, 1024);
      }
      SB();
    });
    STAMP(8 + 2 * H);
    if constexpr (H == 0) BAR();
    STAMP(9 + 2 * H);
  });
  mfma_done(acc[1][0][0], acc[1][0][1], acc[1][1][0], acc[1][1][1]);
#pragma unroll
  for (int s = 0; s < 2; ++s) h_piece(acc[1][0][s], rs, lane, hoff + ((4 * rb + 2 + s) * 112 + ks0) * 1024);
  if (two) {
#pragma unroll
    for (int s = 0; s < 2; ++s) h_piece(acc[1][1][s], rs, lane, hoff + ((4 * rb + 2 + s) * 112 + ks0 + 1) * 1024);
  } else if (idft) {
#pragma unroll
    for (int s = 0; s < 2; ++s) x3_piece(acc[1][1][s], rs, lane, OFF_X3 + (128 * rb + 64 + 32 * s) * XS + fs * 64);
  }
  SB();
  stream_issue<32, 2>(W, hA, hB, x3, srd, lane16, dn);
  {
    auto x = __builtin_amdgcn_permlane32_swap(asu(ss[0]), asu(ss[1]), false, false);
    *(LAS float*)(L + M_S1 + w * 256 + lane * 4) = asf(x[0]) + asf(x[1]);
  }
  const int row = 16 * w + (lane >> 2);
  {
    const int n = nb * 64 + row;
    LAS unsigned char* ra = L + M_Z + n * 128 + (((4 * pi + (lane & 3)) ^ ((n & 2) << 1)) * 16);
    res[0] = *(LAS v4u*)ra; res[1] = *(LAS v4u*)(ra + 65536);
  }
  STAMP(12);
  CSTAMP(21);
  unsigned hm = 0;
  sfor<0, 7>([&](auto G) { h_acc<G.v, STR.end[32]>(hA, hB, hm); });
  h_decide<0, STR.end[32]>(hA, hB, sent_mask(hm), srd, lane16, dn, L, layer, w);
  hm = 0;
  BAR();
  STAMP(13);
  LAS unsigned char* rw = L + M_Z + w * 9216 + r * 144 + h * 16;
  LAS unsigned char* rr = L + M_Z + row * 144 + (lane & 3) * 32;
  f32x16 dac[2][2];
  f32x4 fa[2][2], tq[2];
  auto red_write = [&](auto HH, auto K) {
    constexpr int H = HH.v, t = K.v >> 2, g = K.v & 3;
    *(LAS f32x4*)(rw + H * M_RB + t * 32 * 144 + g * 32) = f32x4{dac[H][t][4 * g], dac[H][t][4 * g + 1], dac[H][t][4 * g + 2], dac[H][t][4 * g + 3]};
  };
  sfor<0, 56>([&](auto GG) {
    constexpr int g = GG.v, H = g / 28, i = g % 28;
    v4u& hx = H == 0 ? hA[i] : hB[i];
    unsigned long long bad = 0ull;
    if constexpr (2 * i < NV_EARLY) mfma_v<i == 0>(dac[H][0], Ve[2 * i], hx);
    else mfma_w<false, false>(dac[H][0], W[2 * i - NV_EARLY], hx);
    SB();
    if constexpr (g + 7 < 56) h_acc<g + 7, STR.end[32 + g]>(hA, hB, hm);
    if constexpr (g + 7 < 56 && g % 7 == 6) { bad = sent_mask(hm); pins(bad); hm = 0; }
    if constexpr (g == 1) {
      LAS float* sp = (LAS float*)(L + M_S1 + lane * 4);
      *(LAS float*)(L + M_S1 + 1024 + w * 256 + lane * 4) =
          __builtin_amdgcn_rcpf(__builtin_fmaf((sp[0] + sp[64]) + (sp[128] + sp[192]), 1.f / 512.f, 1e-5f));
    }
    if constexpr (g >= 51 && g <= 53) addf4(fa[0][0], tq[0]);
    SB();
    if constexpr (2 * i < NV_EARLY) mfma_v<i == 0>(dac[H][1], Ve[2 * i + 1], hx);
    else mfma_w<false, false>(dac[H][1], W[2 * i + 1 - NV_EARLY], hx);
    SB();
    if constexpr (g + 7 < 56 && g % 7 == 6) { pins(bad); h_decide<g + 1, STR.end[32 + g]>(hA, hB, bad, srd, lane16, dn, L, layer, w); }
    stream_issue<33 + g, 2>(W, hA, hB, x3, srd, lane16, dn);
    if constexpr (g >= 30 && g <= 46 && g % 2 == 0 && g != 34) red_write(IC<0>{}, IC<(g < 34 ? (g - 30) / 2 : (g - 32) / 2)>{});
    if constexpr (g == 47) BAR();
    if constexpr (g == 49) { fa[0][0] = *(LAS f32x4*)rr; fa[0][1] = *(LAS f32x4*)(rr + 16); }
    if constexpr (g >= 51 && g <= 53) addf4(fa[0][1], tq[1]);
    if constexpr (g >= 50 && g <= 52) { tq[0] = *(LAS f32x4*)(rr + (g - 49) * 9216); tq[1] = *(LAS f32x4*)(rr + (g - 49) * 9216 + 16); }
    SB();
    if constexpr (g == 27) STAMP(14);
  });
  STAMP(15);
  asm volatile("s_nop 12" : "+v"(dac[1][0]), "+v"(dac[1][1]));
  sfor<0, 8>([&](auto K) { red_write(IC<1>{}, K); });
  vmwait<0>();
  pin(x3[0]); pin(x3[1]);
  if (__builtin_expect(layer == 1 && any_sent(mxacc(mxacc(0u, x3[0]), x3[1])), 0)) {
    unsigned spin = 0;
    do {
      RETRY(29);
      ldx4<0>(x3[0], srd, dn.xvo, dn.xso); ldx4<0>(x3[1], srd, dn.xvo, dn.xso + 64 * XS);
      vmwait<0>();
      pin(x3[0]); pin(x3[1]);
    } while (any_sent(mxacc(mxacc(0u, x3[0]), x3[1])) && ++spin < SPIN_MAX);
  }
  STAMP(16);
  BAR();
  STAMP(17);
  fa[1][0] = *(LAS f32x4*)(rr + M_RB); fa[1][1] = *(LAS f32x4*)(rr + M_RB + 16);
#pragma unroll
  for (int ww = 1; ww < 4; ++ww) { fa[1][0] += *(LAS f32x4*)(rr + M_RB + ww * 9216); fa[1][1] += *(LAS f32x4*)(rr + M_RB + ww * 9216 + 16); }
  f32x4 sc[2][2];
#pragma unroll
  for (int H = 0; H < 2; ++H)
#pragma unroll
    for (int x = 0; x < 2; ++x) sc[H][x] = *(LAS f32x4*)(L + M_S1 + 1024 + w * 256 + H * 128 + (lane & 3) * 32 + x * 16);
  if (layer == 0) {
    const bool dc = nb == 0 && row == 0;
#pragma unroll
    for (int H = 0; H < 2; ++H) {
      v4u o;
      float sq[8];
#pragma unroll
      for (int j = 0; j < 4; ++j) {
        o[j] = nosent(pk(__builtin_fmaf(fa[H][j >> 1][(2 * j) & 3], sc[H][j >> 1][(2 * j) & 3], blo(res[H][j])),
                         __builtin_fmaf(fa[H][j >> 1][(2 * j + 1) & 3], sc[H][j >> 1][(2 * j + 1) & 3], bhi(res[H][j]))));
      }
      bstore_sc1(o, rs, row * ZS + (lane & 3) * 16, OFF_Z2 + nb * 64 * ZS + pt * 64 + H * 128);
#pragma unroll
      for (int j = 0; j < 4; ++j) {
        const unsigned u = dc ? 0u : o[j];
        sq[2 * j] = blo(u) * blo(u); sq[2 * j + 1] = bhi(u) * bhi(u);
      }
      float t4[4], t2[2];
#pragma unroll
      for (int j = 0; j < 4; ++j) {
        auto x = __builtin_amdgcn_permlane32_swap(asu(sq[j]), asu(sq[j + 4]), false, false);
        t4[j] = asf(x[0]) + asf(x[1]);
      }
#pragma unroll
      for (int j = 0; j < 2; ++j) {
        auto x = __builtin_amdgcn_permlane16_swap(asu(t4[j]), asu(t4[j + 2]), false, false);
        t2[j] = asf(x[0]) + asf(x[1]);
        t2[j] += dppf<0x124>(t2[j]);
        t2[j] += dppf<0x128>(t2[j]);
      }
      *(LAS f32x2*)(L + M_S2 + w * 256 + H * 128 + (8 * (lane & 3) + 4 * h + 2 * ((lane >> 4) & 1)) * 4) = f32x2{t2[0], t2[1]};
    }
    BAR();
    if (w == 0) {
      LAS float* sp = (LAS float*)(L + M_S2 + lane * 4);
      const float t = (sp[0] + sp[64]) + (sp[128] + sp[192]);
      __builtin_amdgcn_raw_buffer_store_b32(nosent(asu(t)), rs, stat_of(h * 64 + r), OFF_ST + nb * 8192 + pt * 64, SC1);
    }
  } else {
    LAS unsigned char* tb = L + M_TB + w * 2560;
#pragma unroll
    for (int H = 0; H < 2; ++H) {
      const f32x4 a = fa[H][0] * sc[H][0], bq = fa[H][1] * sc[H][1];
      *(LAS v4u*)(tb + H * 1280 + (lane >> 2) * 80 + (lane & 3) * 16) = v4u{pk(a.x, a.y), pk(a.z, a.w), pk(bq.x, bq.y), pk(bq.z, bq.w)};
    }
#pragma unroll
    for (int H = 0; H < 2; ++H) {
      const v4u y = tr2(tb + H * 1280 + (8 * h + q) * 80 + ((lane >> 4) & 1) * 32 + (lane & 3) * 8, 4 * 80);
      v4u o;
#pragma unroll
      for (int j = 0; j < 4; ++j) o[j] = pk(blo(x3[H][j]) + blo(y[j]), bhi(x3[H][j]) + bhi(y[j]));
      const int tok = token_of(32 * (pt + 2 * H) + r);
      *(v4u*)(p.out + (size_t)tok * 512 + nb * 64 + 16 * w + 8 * h) = o;
    }
  }
  SB();
}

#define XMAP 1
__global__ void __launch_bounds__(256, 1) k_main(Params p) {
  extern __shared__ __attribute__((aligned(16))) unsigned char smem[];
  LAS unsigned char* L = (LAS unsigned char*)smem;
  const rsrc_t rs = __builtin_amdgcn_make_buffer_rsrc((void*)p.ws, 0, WS_BYTES, 0x00020000);
  const v4u srd = {(unsigned)(size_t)p.ws, (unsigned)((size_t)p.ws >> 32) & 0xFFFFu, (unsigned)WS_BYTES, 0x00020000u};
#pragma unroll 1
  for (int layer = 0; layer < 2; ++layer) {
    int tid = threadIdx.x;
    asm volatile("" : "+v"(tid));
    const int c = opaque(blockIdx.x);
    const int lane = tid & 63, w = __builtin_amdgcn_readfirstlane(tid >> 6);
    const int rb = 4 * (c & 3) + (c >> 6), fs = 8 * ((c >> 2) & 1) + ((c >> 3) & 7), pi = fs >> 3, nb = fs & 7;
    WSrc ws;
    ws.srd = srd; ws.srd1 = srd;
    if (w == 3 && layer == 0) ws.srd1.z = 0u;
    ws.lane16 = lane * 16; ws.base = OFF_W1 + (fs * 8 + 2 * w) * 32768;
    v4u W[64];
    unsigned wf[8];
    const int wfvo = (16 * w + (lane & 15) + 64 * 4 * (lane >> 4)) * 4;
    STAMP(0);
    CSTAMP(22);
    v4u zr, zi, sa, sb;
    asm volatile("" : "=v"(zr), "=v"(zi), "=v"(sa), "=v"(sb));
    const int first = c == 0;
    if (layer == 0) {
      ldx4<0>(zr, srd, tid * 16, OFF_Z0 + (2 * c + first) * ZS); ldx4<0>(zi, srd, tid * 16, OFF_Z0 + (2 * c + 1 - first) * ZS);
      ldx4<0>(sa, srd, tid * 16, OFF_R0); ldx4<0>(sb, srd, tid * 16, OFF_R0 + 4096);
    }
    asm volatile("buffer_load_dword %0, %8, %9, %10 offen\n\tbuffer_load_dword %1, %8, %9, %10 offen offset:256\n\t"
                 "buffer_load_dword %2, %8, %9, %10 offen offset:512\n\tbuffer_load_dword %3, %8, %9, %10 offen offset:768\n\t"
                 "buffer_load_dword %4, %8, %9, %11 offen\n\tbuffer_load_dword %5, %8, %9, %11 offen offset:256\n\t"
                 "buffer_load_dword %6, %8, %9, %11 offen offset:512\n\tbuffer_load_dword %7, %8, %9, %11 offen offset:768"
                 : "=&v"(wf[0]), "=&v"(wf[1]), "=&v"(wf[2]), "=&v"(wf[3]), "=&v"(wf[4]), "=&v"(wf[5]), "=&v"(wf[6]), "=&v"(wf[7])
                 : "v"(wfvo), "s"(srd), "s"(OFF_WT + c * WS_), "s"(OFF_WT + c * WS_ + 4096));
    SB();
    ColConst k;
    col_consts(k, w, lane, W, ws);
    SB();
    vmwait<16>();
    asm volatile("" : "+v"(wf[0]), "+v"(wf[1]), "+v"(wf[2]), "+v"(wf[3]), "+v"(wf[4]), "+v"(wf[5]), "+v"(wf[6]), "+v"(wf[7]));
    pin(zr); pin(zi); pin(sa); pin(sb);
    float rstd[8], fr[8], fi[8];
#pragma unroll
    for (int e = 0; e < 8; ++e) { fr[e] = blo(wf[e]) * (1.f / 2048.f); fi[e] = bhi(wf[e]) * (1.f / 2048.f); pinf(fr[e]); pinf(fi[e]); }
    STAMP(1);
    if (layer == 1) {
      __builtin_amdgcn_s_sleep(DELAY_C);
      unsigned spin = 0;
      bool again;
      do {
        RETRY(28);
        v4u st[16];
        ldx4<0>(zr, srd, tid * 16, OFF_Z2 + (2 * c + first) * ZS); ldx4<0>(zi, srd, tid * 16, OFF_Z2 + (2 * c + 1 - first) * ZS);
        sfor<0, 16>([&](auto I) { ldx4<0>(st[I.v], srd, tid * 16, OFF_ST + I.v * 4096); });
        vmwait<0>();
        pin(zr); pin(zi);
#pragma unroll
        for (int i = 0; i < 16; ++i) pin(st[i]);
        f32x4 a = __builtin_bit_cast(f32x4, st[0]), bq = __builtin_bit_cast(f32x4, st[1]);
#pragma unroll
        for (int n = 1; n < 8; ++n) { a += __builtin_bit_cast(f32x4, st[2 * n]); bq += __builtin_bit_cast(f32x4, st[2 * n + 1]); }
        sa = __builtin_bit_cast(v4u, a); sb = __builtin_bit_cast(v4u, bq);
        const f32x4 t = a + bq;
        const float u = (t.x + t.y) + (t.z + t.w);
        unsigned m = mxacc(mxacc(0u, zr), zi);
        if (__builtin_expect(__builtin_amdgcn_ballot_w64(u != u) != 0ull, 0)) {
#pragma unroll
          for (int i = 0; i < 16; ++i) m = mxacc(m, st[i]);
        }
        again = any_sent(m);
      } while (__builtin_expect(again, 0) && ++spin < SPIN_MAX);
    }
    load_ws<16, 18>(W, ws);
#pragma unroll
    for (int e = 0; e < 4; ++e) {
      rstd[e] = __builtin_amdgcn_rsqf(__builtin_fmaf(asf(sa[e]), 1.f / 512.f, 1e-5f));
      rstd[4 + e] = __builtin_amdgcn_rsqf(__builtin_fmaf(asf(sb[e]), 1.f / 512.f, 1e-5f));
    }
    load_ws<18, 20>(W, ws);
    STAMP(2);
    col_compute<20>(L, rs, k, fr, fi, zr, zi, rstd, c, w, lane, OFF_Z1 + layer * (OFF_Z3 - OFF_Z1), W, ws);
    STAMP(3);
    CSTAMP(23);
    load_ws<50, 64 - NW_LATE>(W, ws);
    __builtin_amdgcn_s_sleep(DELAY_A);
    ffn(L, rs, ws, p, W, layer, c, rb, fs, pi, nb, w, lane);
    STAMP(18);
  }
}

extern "C" void kernel_launch(void* const* d_in, const int* in_sizes, int n_in, void* d_out, int out_size, void* d_ws, size_t ws_size,
                              hipStream_t stream) {
  Params p;
  p.x = (const u16*)d_in[0]; p.wr = (const u16*)d_in[1]; p.wi = (const u16*)d_in[2];
  p.wg = (const u16*)d_in[4]; p.wu = (const u16*)d_in[5]; p.wd = (const u16*)d_in[6];
  p.out = (u16*)d_out; p.ws = (unsigned char*)d_ws;
  (void)hipFuncSetAttribute((const void*)k_prep, hipFuncAttributeMaxDynamicSharedMemorySize, P_LDS);
  (void)hipFuncSetAttribute((const void*)k_main, hipFuncAttributeMaxDynamicSharedMemorySize, M_LDS);
  k_prep<<<256, 512, P_LDS, stream>>>(p);
  k_main<<<256, 256, M_LDS, stream>>>(p);
}
```

```cpp
#include <hip/hip_runtime.h>
#include <hip/hip_bf16.h>

#define LAS __attribute__((address_space(3)))
#define DI __device__ __forceinline__
typedef unsigned short u16;
typedef unsigned v4u __attribute__((ext_vector_type(4)));
typedef unsigned v2u __attribute__((ext_vector_type(2)));
typedef float f32x2 __attribute__((ext_vector_type(2)));
typedef float f32x4 __attribute__((ext_vector_type(4)));
typedef float f32x16 __attribute__((ext_vector_type(16)));
typedef short s16x4 __attribute__((ext_vector_type(4)));
typedef __bf16 bf16x2 __attribute__((ext_vector_type(2)));
typedef __bf16 bf16x8 __attribute__((ext_vector_type(8)));
typedef __amdgpu_buffer_rsrc_t rsrc_t;

constexpr int MB = 1 << 20;
constexpr int ZS = 4224, XS = 1152, WS_ = 8320;
constexpr int OFF_ZA = 0, OFF_ZB = 9 * MB / 4;
constexpr int OFF_WT = 9 * MB;
constexpr int OFF_W1 = 12 * MB;
constexpr int OFF_W2 = 16 * MB;
constexpr int OFF_W3 = 18 * MB;
constexpr int OFF_H = 20 * MB;
constexpr int OFF_X3 = 36 * MB;
constexpr int OFF_R0 = 39 * MB;
constexpr int OFF_ST = 39 * MB + 65536;
constexpr int WS_BYTES = 40 * MB;
constexpr int SC1 = 16;
constexpr unsigned SENT = 0xFFFFFFFFu;
constexpr unsigned SPIN_MAX = 1u << 16;

struct Params {
  const u16* x; const u16* wr; const u16* wi; const u16* wg; const u16* wu; const u16* wd;
  u16* out; unsigned char* ws;
};

DI unsigned pk(float a, float b) { f32x2 v = {a, b}; return __builtin_bit_cast(unsigned, __builtin_convertvector(v, bf16x2)); }
DI float asf(unsigned u) { return __builtin_bit_cast(float, u); }
DI unsigned asu(float f) { return __builtin_bit_cast(unsigned, f); }
DI bf16x2 asb2(unsigned u) { return __builtin_bit_cast(bf16x2, u); }
template <int V> struct IC { static constexpr int v = V; };
template <int I, int N, class Fn> DI void sfor(Fn&& fn) { if constexpr (I < N) { fn(IC<I>{}); sfor<I + 1, N>(fn); } }
DI int opaque(int x) { asm volatile("" : "+s"(x)); return x; }
DI float blo(unsigned u) { return __builtin_bit_cast(float, u << 16); }
DI float bhi(unsigned u) { return __builtin_bit_cast(float, u & 0xFFFF0000u); }
DI float rbf(float a) { return blo(pk(a, a)); }
DI void pk_tag(float a0, float b0, float a1, float b1, unsigned tag, unsigned& d0, unsigned& d1) {
  const unsigned c = pk(b0, b1), up = (tag ^ 1u) << 16;
  d0 = (__builtin_amdgcn_perm(c, asu(a0) + up, 0x05040302u) & ~1u) | tag;
  d1 = (__builtin_amdgcn_perm(c, asu(a1) + up, 0x07060302u) & ~1u) | tag;
}
DI unsigned nosent(unsigned u) { return u < 0xFFFFFFFEu ? u : 0xFFFFFFFEu; }
DI v4u nosent4(v4u v) { v.x = nosent(v.x); v.y = nosent(v.y); v.z = nosent(v.z); v.w = nosent(v.w); return v; }
DI unsigned umax(unsigned a, unsigned b) { return a > b ? a : b; }
DI unsigned mx4(v4u v) { return umax(umax(v.x, v.y), umax(v.z, v.w)); }
DI bool any_sent(unsigned m) { return __builtin_amdgcn_ballot_w64(m == SENT) != 0ull; }
DI f32x16 mfma32(v4u a, v4u b, f32x16 c) {
  return __builtin_amdgcn_mfma_f32_32x32x16_bf16(__builtin_bit_cast(bf16x8, a), __builtin_bit_cast(bf16x8, b), c, 0, 0, 0);
}
DI f32x4 mfma16(v4u a, v4u b, f32x4 c) {
  return __builtin_amdgcn_mfma_f32_16x16x32_bf16(__builtin_bit_cast(bf16x8, a), __builtin_bit_cast(bf16x8, b), c, 0, 0, 0);
}
template <bool FIRST, bool WA> DI void mfma_w(f32x16& acc, const v4u& wt, const v4u& x) {
  if constexpr (FIRST && WA) asm volatile("v_mfma_f32_32x32x16_bf16 %0, %1, %2, 0" : "=&v"(acc) : "a"(wt), "v"(x));
  if constexpr (FIRST && !WA) asm volatile("v_mfma_f32_32x32x16_bf16 %0, %2, %1, 0" : "=&v"(acc) : "a"(wt), "v"(x));
  if constexpr (!FIRST && WA) asm volatile("v_mfma_f32_32x32x16_bf16 %0, %1, %2, %0" : "+v"(acc) : "a"(wt), "v"(x));
  if constexpr (!FIRST && !WA) asm volatile("v_mfma_f32_32x32x16_bf16 %0, %2, %1, %0" : "+v"(acc) : "a"(wt), "v"(x));
}
template <bool FIRST> DI void mfma_v(f32x16& acc, const v4u& wt, const v4u& x) {
  if constexpr (FIRST) asm volatile("v_mfma_f32_32x32x16_bf16 %0, %2, %1, 0" : "=&v"(acc) : "v"(wt), "v"(x));
  else asm volatile("v_mfma_f32_32x32x16_bf16 %0, %2, %1, %0" : "+v"(acc) : "v"(wt), "v"(x));
}
template <bool FIRST> DI void mfma_vv(f32x16& acc, const v4u& a, const v4u& b) {
  if constexpr (FIRST) asm volatile("v_mfma_f32_32x32x16_bf16 %0, %1, %2, 0" : "=&v"(acc) : "v"(a), "v"(b));
  else asm volatile("v_mfma_f32_32x32x16_bf16 %0, %1, %2, %0" : "+v"(acc) : "v"(a), "v"(b));
}
template <int IMM> DI void vload(v4u& dst, const v4u& srd, int lane16, int so) {
  asm volatile("buffer_load_dwordx4 %0, %1, %2, %3 offen offset:%4" : "=v"(dst) : "v"(lane16), "s"(srd), "s"(so), "i"(IMM));
}
template <int IMM, bool REUSE> DI void wload(v4u& dst, const v4u& srd, int lane16, int so) {
  if constexpr (REUSE) asm volatile("buffer_load_dwordx4 %0, %1, %2, %3 offen offset:%4" : "+a"(dst) : "v"(lane16), "s"(srd), "s"(so), "i"(IMM));
  else asm volatile("buffer_load_dwordx4 %0, %1, %2, %3 offen offset:%4" : "=a"(dst) : "v"(lane16), "s"(srd), "s"(so), "i"(IMM));
}
DI void mfma_done(f32x16& a, f32x16& b, f32x16& c, f32x16& d) { asm volatile("s_nop 12" : "+v"(a), "+v"(b), "+v"(c), "+v"(d)); }
DI v2u trread(LAS unsigned char* p) { return __builtin_bit_cast(v2u, __builtin_amdgcn_ds_read_tr16_b64_v4i16((LAS s16x4*)p)); }
DI v4u tr2(LAS unsigned char* p, int d) { v2u a = trread(p), b = trread(p + d); return v4u{a.x, a.y, b.x, b.y}; }
DI v4u bload(rsrc_t rs, int voff, int soff) { return __builtin_amdgcn_raw_buffer_load_b128(rs, voff, soff, 0); }
DI v4u bload_sc1(rsrc_t rs, int voff, int soff) { return __builtin_amdgcn_raw_buffer_load_b128(rs, voff, soff, SC1); }
DI void bstore(v4u v, rsrc_t rs, int voff, int soff) { __builtin_amdgcn_raw_buffer_store_b128(v, rs, voff + soff, 0, 0); }
DI void bstore_sc1(v4u v, rsrc_t rs, int voff, int soff) { __builtin_amdgcn_raw_buffer_store_b128(v, rs, voff + soff, 0, SC1); }
template <int I> DI v4u fload(rsrc_t rs, int lane16, int base) { return bload(rs, lane16 + (I & 3) * 1024, base + (I >> 2) * 4096); }
template <int I> DI v4u fload_sc1(rsrc_t rs, int lane16, int base) { return bload_sc1(rs, lane16 + (I & 3) * 1024, base + (I >> 2) * 4096); }
#define SB() __builtin_amdgcn_sched_barrier(0)
#define MEMFENCE() asm volatile("" ::: "memory")
#define BAR() asm volatile("s_waitcnt lgkmcnt(0)\n\ts_barrier" ::: "memory")
template <int N> DI void vmwait() { asm volatile("s_waitcnt vmcnt(%0)" :: "i"(N) : "memory"); }
DI void pin(v4u& a) { asm volatile("" : "+v"(a)); }
DI f32x16 zero16() { f32x16 z; for (int i = 0; i < 16; ++i) z[i] = 0.f; return z; }
DI v4u pack8(const f32x16& a, int s, float sc) {
  return v4u{pk(a[8 * s] * sc, a[8 * s + 1] * sc), pk(a[8 * s + 2] * sc, a[8 * s + 3] * sc),
             pk(a[8 * s + 4] * sc, a[8 * s + 5] * sc), pk(a[8 * s + 6] * sc, a[8 * s + 7] * sc)};
}
DI void widen(const f32x16& a, float sc, v4u& o0, v4u& o1) {
  unsigned d[8];
#pragma unroll
  for (int i = 0; i < 8; ++i) d[i] = pk(a[2 * i] * sc, a[2 * i + 1] * sc);
  auto r0 = __builtin_amdgcn_permlane32_swap(d[0], d[2], false, false);
  auto r1 = __builtin_amdgcn_permlane32_swap(d[1], d[3], false, false);
  auto r2 = __builtin_amdgcn_permlane32_swap(d[4], d[6], false, false);
  auto r3 = __builtin_amdgcn_permlane32_swap(d[5], d[7], false, false);
  o0 = v4u{r0[0], r1[0], r0[1], r1[1]};
  o1 = v4u{r2[0], r3[0], r2[1], r3[1]};
}
template <int CTRL> DI float dppf(float v) {
  return __builtin_bit_cast(float, __builtin_amdgcn_update_dpp(0, __builtin_bit_cast(int, v), CTRL, 0xF, 0xF, true));
}
DI float sum32(float v) {
  v += dppf<0xB1>(v); v += dppf<0x4E>(v); v += dppf<0x141>(v); v += dppf<0x140>(v);
  unsigned u = __builtin_bit_cast(unsigned, v);
  auto r = __builtin_amdgcn_permlane16_swap(u, u, false, false);
  return __builtin_bit_cast(float, (unsigned)r[0]) + __builtin_bit_cast(float, (unsigned)r[1]);
}
DI int stat_of(int p) { return ((p >> 2) & 1) * 4096 + (p >> 3) * 16 + (p & 3) * 4; }
DI int token_of(int p) {
  const int l = (p >> 3) & 63, e = p & 7;
  return 32 * (16 * (p >> 9) + 4 * (l >> 4) + (e & 3)) + (l & 15) + 16 * (e >> 2);
}
#define TOUCH_POLICY " nt"
#define GLDS_POLICY " nt"
DI void glds16(const void* sbase, unsigned voff, unsigned lds) {
  unsigned keep;
  asm volatile("s_mov_b32 %0, m0\n\ts_mov_b32 m0, %3\n\ts_nop 0\n\tglobal_load_lds_dwordx4 %1, %2" GLDS_POLICY "\n\ts_mov_b32 m0, %0"
               : "=&s"(keep) : "v"(voff), "s"(sbase), "s"(lds) : "memory");
}
template <int IMM> DI void ld2u16(unsigned& a, unsigned& b, int voff, const v4u& sa, const v4u& sb, int soff) {
  asm volatile("buffer_load_ushort %0, %2, %3, %5 offen offset:%6\n\tbuffer_load_ushort %1, %2, %4, %5 offen offset:%6"
               : "=&v"(a), "=&v"(b) : "v"(voff), "s"(sa), "s"(sb), "s"(soff), "i"(IMM));
}
DI v4u cosfrag(float a0, float step, int off) {
  float v[8];
#pragma unroll
  for (int j = 0; j < 8; ++j) v[j] = __builtin_amdgcn_cosf(__builtin_fmaf(step, (float)(off + j), a0));
  return v4u{pk(v[0], v[1]), pk(v[2], v[3]), pk(v[4], v[5]), pk(v[6], v[7])};
}

constexpr int CODE_BYTES = 40 * 1024;
constexpr int P_XL = 0, P_GL = 33280, P_DL = P_GL + 65536, P_ZB = P_DL + 33280, P_WB = P_ZB + 4 * 2560,
              P_FL = P_WB + 64 * 144, P_LDS = P_FL + 64;

__global__ void __launch_bounds__(512, 2) k_prep(Params p) {
  extern __shared__ __attribute__((aligned(16))) unsigned char smem[];
  LAS unsigned char* L = (LAS unsigned char*)smem;
  const unsigned Lb = (unsigned)(size_t)L;
  const int tid = threadIdx.x, lane = tid & 63, w8 = __builtin_amdgcn_readfirstlane(tid >> 6), w = w8 & 3;
  const int b = blockIdx.x, xcd = b & 7, q = (b >> 3) & 3, tgi = b >> 5;
  const int t1 = xcd * 8 + tgi, tg = xcd * 7 + tgi;
  const bool heavy = tgi < 7;
  const int up = tg >= 28, cb = tg - 28 * up;
#define ROTQ q
  const int h = lane >> 5, r = lane & 31, ft = 4 * q + w;
  const rsrc_t rs = __builtin_amdgcn_make_buffer_rsrc((void*)p.ws, 0, WS_BYTES, 0x00020000);
#define PST(i)
  PST(0);
  auto landed = [&](int stage) { *(volatile LAS unsigned*)(L + P_FL + stage * 16 + w * 4) = 1u; };
  auto await = [&](int stage) {
    v4u f;
    for (;;) {
      f = *(volatile LAS v4u*)(L + P_FL + stage * 16);
      if (__builtin_amdgcn_readfirstlane(f.x & f.y & f.z & f.w) != 0u) break;
      __builtin_amdgcn_s_sleep(1);
    }
    MEMFENCE();
  };
  if (__builtin_expect(w8 >= 4, 1)) {
    if (lane < 4) *(LAS unsigned*)(L + P_FL + lane * 16 + w * 4) = 0u;
    const unsigned vo = (lane >> 3) * 3584 + (((lane & 7) ^ ((lane >> 2) & 4)) * 16);
    const u16* gsrc = (up ? p.wu : p.wg) + 64 * cb;
    auto gu = [&](int i) {
      const int blk = 16 * (((i >> 1) + ROTQ) & 3) + 4 * w + 2 * (i & 1);
      glds16(gsrc + (size_t)blk * 8 * 1792, vo, Lb + P_GL + blk * 1024);
      glds16(gsrc + (size_t)(blk + 1) * 8 * 1792, vo, Lb + P_GL + (blk + 1) * 1024);
    };
#pragma unroll
    for (int i = 0; i < 8; ++i) {
      const int row = 8 * (((i >> 1) + ROTQ) & 3) + 2 * w + (i & 1);
      glds16(p.x + (size_t)token_of(32 * t1 + row) * 512, lane * 16, Lb + P_XL + row * 1040);
    }
    if (heavy) {
#pragma unroll
      for (int i = 0; i < 8; ++i) {
        const int row = 8 * (((i >> 1) + ROTQ) & 3) + 2 * w + (i & 1);
        glds16(p.wd + (size_t)(32 * tg + row) * 512, lane * 16, Lb + P_DL + row * 1040);
      }
      gu(0); gu(1);
    }
    BAR();
    if (heavy) vmwait<12>(); else vmwait<0>();
    PST(1);
    landed(0);
    if (heavy) { gu(2); gu(3); vmwait<8>(); }
    PST(2);
    landed(1);
    if (heavy) { gu(4); gu(5); gu(6); gu(7); }
    const int kk0 = 32 * t1 + 8 * w;
    unsigned fr[8], fi[8];
    {
      const v4u sr = {(unsigned)(size_t)p.wr, (unsigned)((size_t)p.wr >> 32) & 0xFFFFu, 2048u * 514u, 0x00020000u};
      const v4u si = {(unsigned)(size_t)p.wi, (unsigned)((size_t)p.wi >> 32) & 0xFFFFu, 2048u * 514u, 0x00020000u};
      const int wc = 64 * q + lane, src = (wc == 0 ? 256 : wc) * 2;
      sfor<0, 8>([&](auto I) { ld2u16<I.v * 514>(fr[I.v], fi[I.v], src, sr, si, kk0 * 514); });
    }
    PST(3);
    vmwait<16>();
    landed(2);
    PST(4);
    await(0);
    {
      const int row = 8 * q + 2 * w + h;
      const v4u u0 = *(LAS v4u*)(L + P_XL + row * 1040 + r * 32), u1 = *(LAS v4u*)(L + P_XL + row * 1040 + r * 32 + 16);
      float v[16];
#pragma unroll
      for (int i = 0; i < 4; ++i) { v[2 * i] = blo(u0[i]); v[2 * i + 1] = bhi(u0[i]); v[8 + 2 * i] = blo(u1[i]); v[9 + 2 * i] = bhi(u1[i]); }
      float s = 0.f;
#pragma unroll
      for (int i = 0; i < 16; ++i) s += v[i];
      const float m = sum32(s) * (1.f / 512.f);
      float ss = 0.f;
#pragma unroll
      for (int i = 0; i < 16; ++i) { const float d = v[i] - m; ss = __builtin_fmaf(d, d, ss); }
      ss = sum32(ss);
      if (r == 0) __builtin_amdgcn_raw_buffer_store_b32(asu(ss), rs, stat_of(32 * t1 + row), OFF_R0, 0);
    }
    {
      const v4u sv = {SENT, SENT, SENT, SENT};
      const int t16 = (w * 64 + lane) * 16;
#pragma unroll
      for (int i = 0; i < 7; ++i) bstore_sc1(sv, rs, t16, OFF_H + b * 28672 + i * 4096);
      if (t16 < 256) bstore_sc1(sv, rs, t16, OFF_ST + b * 256);
    }
    PST(5);
    asm volatile("s_waitcnt vmcnt(8)"
                 : "+v"(fr[0]), "+v"(fr[1]), "+v"(fr[2]), "+v"(fr[3]), "+v"(fr[4]), "+v"(fr[5]), "+v"(fr[6]), "+v"(fr[7]),
                   "+v"(fi[0]), "+v"(fi[1]), "+v"(fi[2]), "+v"(fi[3]), "+v"(fi[4]), "+v"(fi[5]), "+v"(fi[6]), "+v"(fi[7]) :: "memory");
    PST(6);
    {
      v4u o0, o1;
#pragma unroll
      for (int i = 0; i < 4; ++i) { o0[i] = fr[i] | (fi[i] << 16); o1[i] = fr[4 + i] | (fi[4 + i] << 16); }
      *(LAS v4u*)(L + P_WB + lane * 144 + w * 32) = o0;
      *(LAS v4u*)(L + P_WB + lane * 144 + w * 32 + 16) = o1;
    }
    asm volatile("s_waitcnt lgkmcnt(0)" ::: "memory");
    landed(3);
    await(3);
#pragma unroll
    for (int i = 0; i < 2; ++i) {
      const int col = 16 * w + 8 * i + (lane >> 3);
      bstore_sc1(*(LAS v4u*)(L + P_WB + col * 144 + (lane & 7) * 16), rs, (64 * q + col) * WS_ + (lane & 7) * 16, OFF_WT + t1 * 128);
    }
  } else {
    unsigned touch, touch2;
    {
      const char* base = (const char*)(up ? p.wu : p.wg) + (128 * q + 64 * (w & 1)) * 3584 + (heavy ? 128 * cb : 0);
      int off = lane * 3584;
      if (w >= 2) { base = (const char*)(w == 2 ? p.wr : p.wi) + (32 * t1 * 257 + 64 * q) * 2; off = r * 514; }
      asm volatile("global_load_ushort %0, %1, %2" TOUCH_POLICY : "=v"(touch) : "v"(off), "s"(base));
      unsigned lo, hi;
      asm volatile("s_getpc_b64 s[20:21]\n\ts_add_u32 %0, s20, _Z6k_prep6Params@rel32@lo+4\n\ts_addc_u32 %1, s21, _Z6k_prep6Params@rel32@hi+12"
                   : "=s"(lo), "=s"(hi) :: "s20", "s21", "scc");
      off = (w < 2 ? w : 2 + (2 * (b >> 3) + w) % 3) * 8192 + lane * 128;
      asm volatile("global_load_dword %0, %1, %2" : "=v"(touch2) : "v"(off < CODE_BYTES ? off : 0), "s"(((unsigned long long)hi << 32) | lo));
    }
    SB();
    const int feat = 32 * ft + r;
    const float step = (feat == 0 ? 0.f : feat == 1 ? 256.f : (float)(feat >> 1)) * (1.f / 512.f);
    const float ph = (feat > 1 && (feat & 1)) ? 0.25f : 0.f;
    const float a0 = ph + step * (float)(8 * h);
    const unsigned sgn = (feat > 1 && (feat & 2)) ? 0x80008000u : 0u;
    v4u F[32];
#pragma unroll
    for (int ks = 0; ks < 16; ++ks) { F[ks] = cosfrag(a0, step, 16 * ks); F[ks + 16] = F[ks] ^ sgn; }
    const float lsc = feat < 2 ? 0.04419417382415922f : 0.0625f;
#pragma unroll
    for (int ks = 0; ks < 32; ks += 8)
      asm volatile("" : "+v"(F[ks]), "+v"(F[ks + 1]), "+v"(F[ks + 2]), "+v"(F[ks + 3]), "+v"(F[ks + 4]), "+v"(F[ks + 5]), "+v"(F[ks + 6]), "+v"(F[ks + 7]));
    v4u I[2];
#pragma unroll
    for (int s = 0; s < 2; ++s) {
      const int e = r - 16 * s - 8 * h;
      const unsigned one = (e & 1) ? 0x3F800000u : 0x00003F80u;
      I[s] = v4u{(e >> 1) == 0 ? one : 0u, (e >> 1) == 1 ? one : 0u, (e >> 1) == 2 ? one : 0u, (e >> 1) == 3 ? one : 0u};
    }
    pin(I[0]); pin(I[1]);
    PST(1);
    BAR();
    await(0);
    PST(2);
    f32x16 aZ;
    {
      LAS unsigned char* xa = L + P_XL + r * 1040 + h * 16;
      v4u a[4];
#pragma unroll
      for (int i = 0; i < 4; ++i) a[i] = *(LAS v4u*)(xa + i * 32);
      sfor<0, 32>([&](auto KS) {
        constexpr int ks = KS.v;
        mfma_vv<ks == 0>(aZ, a[ks & 3], F[ks]);
        SB();
        if constexpr (ks + 4 < 32) a[ks & 3] = *(LAS v4u*)(xa + (ks + 4) * 32);
        SB();
      });
    }
    unsigned zd[8];
    auto z_out = [&](auto STEP, v4u (&o)[2]) {
      constexpr int n = STEP.v;
      LAS unsigned char* zb = L + P_ZB + w * 2560;
      if constexpr (n < 4) {
        pk_tag(aZ[4 * n] * lsc, aZ[4 * n + 1] * lsc, aZ[4 * n + 2] * lsc, aZ[4 * n + 3] * lsc, 0u, zd[2 * n], zd[2 * n + 1]);
        asm volatile("" : "+v"(zd[2 * n]), "+v"(zd[2 * n + 1]));
      }
      if constexpr (n == 4 || n == 5) {
        auto r0 = __builtin_amdgcn_permlane32_swap(zd[4 * (n - 4)], zd[4 * (n - 4) + 2], false, false);
        auto r1 = __builtin_amdgcn_permlane32_swap(zd[4 * (n - 4) + 1], zd[4 * (n - 4) + 3], false, false);
        *(LAS v4u*)(zb + r * 80 + h * 16 + 32 * (n - 4)) = v4u{r0[0], r1[0], r0[1], r1[1]};
      }
      if constexpr (n == 6) { o[0] = *(LAS v4u*)(zb + (lane >> 2) * 80 + (lane & 3) * 16); o[1] = *(LAS v4u*)(zb + (16 + (lane >> 2)) * 80 + (lane & 3) * 16); }
      if constexpr (n == 7 || n == 8) bstore_sc1(o[n - 7], rs, (32 * ft + 16 * (n - 7) + (lane >> 2)) * ZS + (lane & 3) * 16, OFF_ZA + t1 * 64);
    };
    auto f_out = [&](auto N, const f32x16& a, float sc, v4u& o, int vo, int so) {
      constexpr int k = 8 * (N.v >> 1) + 4 * (N.v & 1);
      o[2 * (N.v & 1)] = pk(a[k] * sc, a[k + 1] * sc); o[2 * (N.v & 1) + 1] = pk(a[k + 2] * sc, a[k + 3] * sc);
      if constexpr (N.v & 1) bstore_sc1(o, rs, vo, so + (N.v >> 1) * 1024); else pin(o);
    };
    v4u zo[2];
    asm volatile("s_nop 12\n\ts_waitcnt vmcnt(0)" : "+v"(aZ) : "v"(touch), "v"(touch2));
    PST(3);
    await(1);
    PST(4);
    if (heavy) {
      f32x16 aD, aT;
      v4u fo = {0u, 0u, 0u, 0u};
      {
        LAS unsigned char* da = L + P_DL + r * 1040 + h * 16;
        const v4u t0 = *(LAS v4u*)(da + ft * 64), t1_ = *(LAS v4u*)(da + ft * 64 + 32);
        v4u a[4];
#pragma unroll
        for (int i = 0; i < 4; ++i) a[i] = *(LAS v4u*)(da + i * 32);
        mfma_vv<true>(aT, t0, I[0]);
        mfma_vv<false>(aT, t1_, I[1]);
        sfor<0, 32>([&](auto KS) {
          constexpr int ks = KS.v;
          mfma_vv<ks == 0>(aD, a[ks & 3], F[ks]);
          SB();
          if constexpr (ks + 4 < 32) a[ks & 3] = *(LAS v4u*)(da + (ks + 4) * 32);
          if constexpr (ks < 18 && (ks & 1)) z_out(IC<(ks >> 1)>{}, zo);
          if constexpr (ks >= 20 && ks < 28 && (ks & 1)) f_out(IC<((ks - 20) >> 1)>{}, aT, 1.f, fo, lane * 16, OFF_W3 + (ft * 112 + 2 * tg) * 1024);
          SB();
        });
      }
      PST(5);
      await(2);
      PST(6);
      f32x16 aG0, aG1;
      const int q4 = (lane & 15) >> 2;
      LAS unsigned char* ba = L + P_GL + (8 * h + q4) * 128 + (4 * (q4 >> 1) + 2 * ((lane >> 4) & 1) + ((lane & 3) >> 1)) * 16 + 8 * (lane & 1);
      LAS unsigned char* bb = L + P_GL + (8 * h + q4) * 128 + (4 * (1 - (q4 >> 1)) + 2 * ((lane >> 4) & 1) + ((lane & 3) >> 1)) * 16 + 8 * (lane & 1);
      v4u b0[2], b1[2];
#pragma unroll
      for (int i = 0; i < 2; ++i) { b0[i] = tr2(ba + i * 2048, 512); b1[i] = tr2(bb + i * 2048, 512); }
      sfor<0, 32>([&](auto KS) {
        constexpr int ks = KS.v;
        mfma_vv<ks == 0>(aG0, F[ks], b0[ks & 1]);
        SB();
        if constexpr (ks + 2 < 32) b0[ks & 1] = tr2(ba + (ks + 2) * 2048, 512);
        if constexpr (ks >= 2 && ks < 6) f_out(IC<ks - 2>{}, aD, lsc, fo, lane * 16, OFF_W2 + (ft * 112 + 2 * tg) * 1024);
        SB();
        mfma_vv<ks == 0>(aG1, F[ks], b1[ks & 1]);
        SB();
        if constexpr (ks + 2 < 32) b1[ks & 1] = tr2(bb + (ks + 2) * 2048, 512);
        SB();
      });
      PST(7);
      asm volatile("s_nop 12" : "+v"(aG0), "+v"(aG1));
      if (ft == 0 && h == 0) { aG0[0] = 0.f; aG1[0] = 0.f; aG0[1] *= 0.70710678118654752f; aG1[1] *= 0.70710678118654752f; }
      const int g0 = 4 * cb + (r >> 4), g1 = g0 + 2, slot = ((r & 15) + 16 * up + 32 * h) * 16;
      const int o0 = ((g0 / 7) * 8 + g0 % 7) * 32768 + slot, o1 = ((g1 / 7) * 8 + g1 % 7) * 32768 + slot;
#pragma unroll
      for (int s = 0; s < 2; ++s) {
        bstore_sc1(pack8(aG0, s, 0.0625f), rs, o0, OFF_W1 + (2 * ft + s) * 1024);
        bstore_sc1(pack8(aG1, s, 0.0625f), rs, o1, OFF_W1 + (2 * ft + s) * 1024);
      }
    } else {
      sfor<0, 9>([&](auto N) { z_out(N, zo); });
#pragma unroll
      for (int i = 0; i < 2; ++i) {
        const int dt = 2 * xcd + i;
        const float b0 = __builtin_amdgcn_fractf(__builtin_fmaf(step, (float)(32 * dt), a0));
        f32x16 aI = mfma32(cosfrag(b0, step, 0), I[0], zero16());
        aI = mfma32(cosfrag(b0, step, 16), I[1], aI);
        if (ft == 0 && h == 0) { aI[0] *= 0.70710678118654752f; aI[1] *= 0.70710678118654752f; }
#pragma unroll
        for (int s = 0; s < 2; ++s) bstore_sc1(pack8(aI, s, 0.0625f), rs, lane * 16, OFF_W1 + (dt * 8 + 7) * 32768 + (2 * ft + s) * 1024);
      }
    }
  }
}

constexpr int M_Z = 0;
constexpr int M_RB = 40960;
constexpr int M_TB = 81920;
constexpr int M_S1 = 131072;
constexpr int M_S2 = M_S1 + 2048;
constexpr int M_C1 = M_S2 + 1024;
constexpr int M_C2 = M_C1 + 2 * 4608;
constexpr int M_LDS = M_C2 + 2 * 4608;
#define STAMP(i)
#define KSTAMP(i)
#define CSTAMP(i)
#define RETRY(i)
#define DELAY_A 6
#define DELAY_C 2

template <int IMM> DI void ldx4(v4u& dst, const v4u& srd, int voff, int soff) {
  asm volatile("buffer_load_dwordx4 %0, %1, %2, %3 offen offset:%4 sc1" : "=v"(dst) : "v"(voff), "s"(srd), "s"(soff), "i"(IMM));
}
struct WSrc { v4u srd, srd1; int lane16, base; };
template <int N> DI void load_w(v4u (&W)[64], const WSrc& s) {
  wload<((N >> 1) & 3) * 1024, false>(W[N], (N & 1) ? s.srd1 : s.srd, s.lane16, s.base + (N & 1) * 32768 + (N >> 3) * 4096);
}
template <int LO, int HI> DI void load_ws(v4u (&W)[64], const WSrc& s) { SB(); sfor<LO, HI>([&](auto J) { load_w<J.v>(W, s); }); SB(); }
DI unsigned mxacc(unsigned m, const v4u& v) { return umax(umax(umax(umax(m, v.x), v.y), v.z), v.w); }
DI unsigned tagacc(unsigned m, const v4u& v, unsigned tag) { return m | ((v.x ^ tag) | (v.y ^ tag)) | ((v.z ^ tag) | (v.w ^ tag)); }
DI unsigned andacc(unsigned m, const v4u& v) { return (m & v.x & v.y) & (v.z & v.w); }
DI unsigned long long bit0_set(unsigned m) { return __builtin_amdgcn_ballot_w64((m & 1u) != 0u); }
DI unsigned long long bit0_clear(unsigned m) { return __builtin_amdgcn_ballot_w64((m & 1u) == 0u); }
DI void pins(unsigned long long& k) { asm volatile("" : "+s"(k)); }
DI void pinf(float& a) { asm volatile("" : "+v"(a)); }
DI void pinu(unsigned& a) { asm volatile("" : "+v"(a)); }

struct ColConst { v4u C1[2], S1[2], N1[2], C2[2], S2[2], N2[2]; float tc[8], ts[8]; };

DI void col_consts(ColConst& k, int w, int lane, v4u (&W)[64], const WSrc& ws) {
  const int g = lane >> 4, r = lane & 15;
  const float k1 = (float)(16 * w + r);
  const unsigned neg = 0x80008000u;
  sfor<0, 2>([&](auto CS) {
    constexpr int cs = CS.v;
    const float st = k1 * (1.f / 64.f), a0 = st * (float)(8 * g);
    load_ws<3 * cs, 3 * cs + 2>(W, ws);
    k.C1[cs] = cosfrag(a0, st, 32 * cs);
    pin(k.C1[cs]);
    load_ws<3 * cs + 2, 3 * cs + 3>(W, ws);
    k.S1[cs] = cosfrag(a0 - 0.25f, st, 32 * cs);
    k.N1[cs] = k.S1[cs] ^ neg;
    pin(k.S1[cs]); pin(k.N1[cs]);
  });
  sfor<0, 2>([&](auto T) {
    constexpr int t = T.v;
    const float st = (float)(16 * t + r) * (1.f / 32.f), a0 = st * (float)(4 * g);
    float c[8], s[8];
    load_ws<6 + 3 * t, 8 + 3 * t>(W, ws);
#pragma unroll
    for (int j = 0; j < 8; ++j) c[j] = __builtin_amdgcn_cosf(__builtin_fmaf(st, (float)(16 * (j >> 2) + (j & 3)), a0));
    k.C2[t] = v4u{pk(c[0], c[1]), pk(c[2], c[3]), pk(c[4], c[5]), pk(c[6], c[7])};
    pin(k.C2[t]);
    load_ws<8 + 3 * t, 9 + 3 * t>(W, ws);
#pragma unroll
    for (int j = 0; j < 8; ++j) s[j] = __builtin_amdgcn_sinf(__builtin_fmaf(st, (float)(16 * (j >> 2) + (j & 3)), a0));
    k.S2[t] = v4u{pk(s[0], s[1]), pk(s[2], s[3]), pk(s[4], s[5]), pk(s[6], s[7])};
    k.N2[t] = k.S2[t] ^ neg;
    pin(k.S2[t]); pin(k.N2[t]);
  });
  sfor<0, 2>([&](auto T) {
    constexpr int t = T.v;
    load_ws<12 + 2 * t, 14 + 2 * t>(W, ws);
#pragma unroll
    for (int e = 4 * t; e < 4 * t + 4; ++e) {
      const float a = k1 * (1.f / 2048.f) * (float)(16 * (e >> 2) + 4 * g + (e & 3));
      k.tc[e] = __builtin_amdgcn_cosf(a); k.ts[e] = __builtin_amdgcn_sinf(a);
      pinf(k.tc[e]); pinf(k.ts[e]);
    }
  });
}

template <bool FIRST> DI void m16(f32x4& acc, const v4u& a, const v4u& b) {
  SB();
  if constexpr (FIRST) asm volatile("v_mfma_f32_16x16x32_bf16 %0, %1, %2, 0" : "=&v"(acc) : "v"(a), "v"(b));
  else asm volatile("v_mfma_f32_16x16x32_bf16 %0, %1, %2, %0" : "+v"(acc) : "v"(a), "v"(b));
  SB();
}
DI void m16_done(f32x4& a, f32x4& b) { asm volatile("s_nop 7" : "+v"(a), "+v"(b)); }
DI void m16_ready(v4u& a, v4u& b) { asm volatile("s_nop 1" : "+v"(a), "+v"(b)); }
template <int PART> DI float cmul(float a, float b, float c, float s) {
  float r = PART == 0 ? __builtin_fmaf(a, c, -(b * s)) : __builtin_fmaf(a, s, b * c);
  pinf(r);
  return r;
}

template <int WB>
DI void col_compute(LAS unsigned char* L, rsrc_t rs, const ColConst& k, const float (&fr)[8], const float (&fi)[8], v4u zr, v4u zi,
                    const float (&rstd)[8], int c, int w, int lane, int out_off, v4u (&W)[64], const WSrc& ws) {
  const int g = lane >> 4, r = lane & 15;
  const int first = c == 0;
  const float f0 = first ? 0.f : 1.f;
  float xr[8], xi[8];
#pragma unroll
  for (int i = 0; i < 4; ++i) { xr[2 * i] = blo(zr[i]); xr[2 * i + 1] = bhi(zr[i]); xi[2 * i] = blo(zi[i]); xi[2 * i + 1] = bhi(zi[i]); }
#pragma unroll
  for (int t = 0; t < 2; ++t) {
    const f32x4 rv = {rstd[4 * t], rstd[4 * t + 1], rstd[4 * t + 2], rstd[4 * t + 3]};
    const f32x4 a = f32x4{xr[4 * t], xr[4 * t + 1], xr[4 * t + 2], xr[4 * t + 3]} * rv;
    const f32x4 bq = f32x4{xi[4 * t], xi[4 * t + 1], xi[4 * t + 2], xi[4 * t + 3]} * (rv * f0);
    LAS unsigned char* d = L + M_C1 + (r + 16 * t) * 144 + (16 * w + 4 * g) * 2;
    *(LAS v2u*)d = v2u{pk(a[0], a[1]), pk(a[2], a[3])};
    *(LAS v2u*)(d + 4608) = v2u{pk(bq[0], bq[1]), pk(bq[2], bq[3])};
  }
  load_ws<WB, WB + 3>(W, ws);
  BAR();
  f32x4 dr[2], di[2];
  float ur[8], ui[8];
  {
    v4u ar[2][2], ai[2][2];
#pragma unroll
    for (int t = 0; t < 2; ++t)
#pragma unroll
      for (int cs = 0; cs < 2; ++cs) {
        LAS unsigned char* s = L + M_C1 + (r + 16 * t) * 144 + (32 * cs + 8 * g) * 2;
        ar[t][cs] = *(LAS v4u*)s; ai[t][cs] = *(LAS v4u*)(s + 4608);
      }
    load_ws<WB + 3, WB + 5>(W, ws);
    sfor<0, 16>([&](auto N) {
      constexpr int n = N.v, t = n >> 3, cs = (n >> 2) & 1, m = n & 3;
      if constexpr (m == 0) m16<cs == 0>(dr[t], ar[t][cs], k.C1[cs]);
      if constexpr (m == 1) m16<cs == 0>(di[t], ai[t][cs], k.C1[cs]);
      if constexpr (m == 2) m16<false>(dr[t], ai[t][cs], k.S1[cs]);
      if constexpr (m == 3) m16<false>(di[t], ar[t][cs], k.N1[cs]);
      if constexpr (m == 3) load_w<WB + 5 + (n >> 2)>(W, ws);
      if constexpr (n >= 9) {
        constexpr int e = (n - 9) >> 1;
        if constexpr (n & 1) ur[e] = cmul<0>(dr[0][e], di[0][e], k.tc[e], -k.ts[e]);
        else ui[e] = cmul<1>(dr[0][e], di[0][e], k.tc[e], -k.ts[e]);
      }
    });
    ui[3] = cmul<1>(dr[0][3], di[0][3], k.tc[3], -k.ts[3]);
  }
  v4u br, bi;
  br.x = pk(ur[0], ur[1]); br.y = pk(ur[2], ur[3]); bi.x = pk(ui[0], ui[1]); bi.y = pk(ui[2], ui[3]);
  SB();
  m16_done(dr[1], di[1]);
  load_ws<WB + 9, WB + 11>(W, ws);
#pragma unroll
  for (int e = 4; e < 8; ++e) {
    ur[e] = dr[1][e - 4] * k.tc[e] + di[1][e - 4] * k.ts[e]; ui[e] = di[1][e - 4] * k.tc[e] - dr[1][e - 4] * k.ts[e];
  }
  br.z = pk(ur[4], ur[5]); br.w = pk(ur[6], ur[7]); bi.z = pk(ui[4], ui[5]); bi.w = pk(ui[6], ui[7]);
  m16_ready(br, bi);
  sfor<0, 8>([&](auto N) {
    constexpr int n = N.v, t = n >> 2, m = n & 3;
    if constexpr (m == 0) m16<true>(dr[t], k.C2[t], br);
    if constexpr (m == 1) m16<true>(di[t], k.C2[t], bi);
    if constexpr (m == 2) m16<false>(dr[t], k.S2[t], bi);
    if constexpr (m == 3) m16<false>(di[t], k.N2[t], br);
    if constexpr (m == 3) load_w<WB + 11 + t>(W, ws);
    if constexpr (n == 5) ur[0] = cmul<0>(dr[0][0], di[0][0], fr[0], fi[0]);
    if constexpr (n == 6) ui[0] = cmul<1>(dr[0][0], di[0][0], fr[0], fi[0]);
  });
#pragma unroll
  for (int e = 1; e < 4; ++e) { ur[e] = dr[0][e] * fr[e] - di[0][e] * fi[e]; ui[e] = dr[0][e] * fi[e] + di[0][e] * fr[e]; }
  br.x = pk(ur[0], ur[1]); br.y = pk(ur[2], ur[3]); bi.x = pk(ui[0], ui[1]); bi.y = pk(ui[2], ui[3]);
  SB();
  m16_done(dr[1], di[1]);
  load_ws<WB + 13, WB + 15>(W, ws);
#pragma unroll
  for (int e = 4; e < 8; ++e) { ur[e] = dr[1][e - 4] * fr[e] - di[1][e - 4] * fi[e]; ui[e] = dr[1][e - 4] * fi[e] + di[1][e - 4] * fr[e]; }
  br.z = pk(ur[4], ur[5]); br.w = pk(ur[6], ur[7]); bi.z = pk(ui[4], ui[5]); bi.w = pk(ui[6], ui[7]);
  m16_ready(br, bi);
  sfor<0, 8>([&](auto N) {
    constexpr int n = N.v, t = n >> 2, m = n & 3;
    if constexpr (m == 0) m16<true>(dr[t], k.C2[t], br);
    if constexpr (m == 1) m16<true>(di[t], k.C2[t], bi);
    if constexpr (m == 2) m16<false>(dr[t], k.N2[t], bi);
    if constexpr (m == 3) m16<false>(di[t], k.S2[t], br);
    if constexpr (m == 3) load_w<WB + 15 + t>(W, ws);
    if constexpr (n == 5) ur[0] = cmul<0>(dr[0][0], di[0][0], k.tc[0], k.ts[0]);
    if constexpr (n == 6) ui[0] = cmul<1>(dr[0][0], di[0][0], k.tc[0], k.ts[0]);
  });
#pragma unroll
  for (int t = 0; t < 2; ++t) {
    if (t == 1) { SB(); m16_done(dr[1], di[1]); load_ws<WB + 17, WB + 19>(W, ws); }
#pragma unroll
    for (int i = (t == 0 ? 1 : 0); i < 4; ++i) {
      const int e = 4 * t + i;
      ur[e] = dr[t][i] * k.tc[e] - di[t][i] * k.ts[e]; ui[e] = di[t][i] * k.tc[e] + dr[t][i] * k.ts[e];
    }
    LAS unsigned char* d = L + M_C2 + (16 * w + r) * 72 + (16 * t + 4 * g) * 2;
    *(LAS v2u*)d = v2u{pk(ur[4 * t], ur[4 * t + 1]), pk(ur[4 * t + 2], ur[4 * t + 3])};
    *(LAS v2u*)(d + 4608) = v2u{pk(ui[4 * t], ui[4 * t + 1]), pk(ui[4 * t + 2], ui[4 * t + 3])};
  }
  load_ws<WB + 19, WB + 21>(W, ws);
  BAR();
  unsigned od[8];
  {
    v4u tr_[2][2], ti_[2][2];
#pragma unroll
    for (int t = 0; t < 2; ++t)
#pragma unroll
      for (int cs = 0; cs < 2; ++cs) {
        LAS unsigned char* s = L + M_C2 + (32 * cs + 8 * g + (r >> 2)) * 72 + (16 * t + 4 * (r & 3)) * 2;
        tr_[t][cs] = tr2(s, 4 * 72); ti_[t][cs] = tr2(s + 4608, 4 * 72);
      }
    load_ws<WB + 21, WB + 23>(W, ws);
    sfor<0, 16>([&](auto N) {
      constexpr int n = N.v, t = n >> 3, cs = (n >> 2) & 1, m = n & 3;
      if constexpr (m == 0) m16<cs == 0>(dr[t], k.C1[cs], tr_[t][cs]);
      if constexpr (m == 1) m16<cs == 0>(di[t], k.C1[cs], ti_[t][cs]);
      if constexpr (m == 2) m16<false>(dr[t], k.N1[cs], ti_[t][cs]);
      if constexpr (m == 3) m16<false>(di[t], k.S1[cs], tr_[t][cs]);
      if constexpr (m == 3) load_w<WB + 23 + (n >> 2)>(W, ws);
      if constexpr (n >= 9 && n < 13) {
        constexpr int e = n - 9;
        xr[e] += dr[0][e]; xi[e] = __builtin_fmaf(di[0][e], f0, xi[e]);
        pinf(xr[e]); pinf(xi[e]);
      }
      if constexpr (n == 14) { pk_tag(xr[0], xr[1], xr[2], xr[3], 1u, od[0], od[1]); pinu(od[0]); pinu(od[1]); }
    });
    pk_tag(xi[0], xi[1], xi[2], xi[3], 1u, od[4], od[5]);
    SB();
    m16_done(dr[1], di[1]);
#pragma unroll
    for (int e = 4; e < 8; ++e) { xr[e] += dr[1][e - 4]; xi[e] = __builtin_fmaf(di[1][e - 4], f0, xi[e]); }
    pk_tag(xr[4], xr[5], xr[6], xr[7], 1u, od[2], od[3]);
    pk_tag(xi[4], xi[5], xi[6], xi[7], 1u, od[6], od[7]);
  }
  const int vo = (w * 64 + lane) * 16;
  bstore_sc1(v4u{od[0], od[1], od[2], od[3]}, rs, vo, out_off + (2 * c + first) * ZS);
  bstore_sc1(v4u{od[4], od[5], od[6], od[7]}, rs, vo, out_off + (2 * c + 1 - first) * ZS);
  load_ws<WB + 27, WB + 30>(W, ws);
}

constexpr int NW_LATE = 8;
constexpr int NV_EARLY = 16;
constexpr int ZG[6] = {0, 2, 4, 8, 16, 32};
constexpr int ZR[5] = {-1, 1, 4, 10, 21};
constexpr int z_read_at(int ks) {
  for (int G = 1; G < 5; ++G) if (ks >= ZR[G] && 2 * (ks - ZR[G]) < ZG[G + 1] - ZG[G]) return ZG[G] + 2 * (ks - ZR[G]);
  return -1;
}
constexpr int z_first(int j) { for (int G = 0; G < 5; ++G) if (ZG[G] == j) return 1; return 0; }
constexpr int z_decide_at(int ks) { for (int G = 1; G < 5; ++G) if (ks == ZR[G] + (ZG[G + 1] - ZG[G]) / 2) return G; return -1; }
constexpr int z_requested(int ks) { return ks < 8 ? 9 + ks : ks < 16 ? 2 * ks + 1 : 32; }
constexpr int z_younger(int j, int ks) {
  int n = z_requested(ks) - j - 1;
  for (int k = 0; k < ks; ++k) if ((k < 8 && j < 16 && k >= j - 8) || k >= 16) ++n;
  return n;
}
struct ZSrc { v4u srd; int vo, so; unsigned lds; };
template <int J> DI void z_issue(const ZSrc& z, int so) {
  asm volatile("s_add_u32 m0, %0, %4\n\ts_nop 0\n\tbuffer_load_dwordx4 %1, %2, %3 offen sc1 lds"
               :: "s"(z.lds), "v"(z.vo), "s"(z.srd), "s"(so + (J & 15) * 32 * ZS + (J >> 4) * 128), "i"((J >> 4) * 65536 + (J & 15) * 4096)
               : "memory", "scc");
}
template <int J> DI v4u z_back(LAS unsigned char* zown) { return *(LAS v4u*)(zown + (J >> 4) * 65536 + (J & 15) * 4096); }
template <int G, int KS> DI void z_fix(unsigned long long bad, const ZSrc& z, LAS unsigned char* zown, LAS unsigned char* L, int layer, int w) {
  if (__builtin_expect(bad != 0ull, 0)) {
    unsigned spin = 0, m;
    vmwait<0>();
    do {
      RETRY(G == 0 ? 24 : 25);
      const int zo = opaque(z.so);
      sfor<ZG[G], z_requested(KS)>([&](auto I) { z_issue<I.v>(z, zo); });
      vmwait<0>();
      m = 1u;
      sfor<ZG[G], ZG[G + 1]>([&](auto I) { m = andacc(m, z_back<I.v>(zown)); });
    } while (bit0_clear(m) != 0ull && ++spin < SPIN_MAX);
  }
}

#define HA_START 14
#define HB_START 6
#define H_ROOM 16
constexpr int NSTR = 56 - NV_EARLY + 58;
struct Stream { int kind[NSTR], idx[NSTR], end[89]; };
constexpr Stream make_stream() {
  Stream s{};
  int kind[NSTR] = {}, idx[NSTR] = {}, rel[NSTR] = {}, need[NSTR] = {};
  bool done[NSTR] = {};
  int n = 0;
  for (int i = NV_EARLY; i < 56; ++i) { kind[n] = 0; idx[n] = i; rel[n] = (i - NV_EARLY) >> 1; need[n] = i >> 1; ++n; }
  for (int i = 0; i < 28; ++i) { kind[n] = 1; idx[n] = i; rel[n] = HA_START; need[n] = i; ++n; }
  for (int i = 0; i < 28; ++i) { kind[n] = 2; idx[n] = i; rel[n] = 33 + HB_START; need[n] = 28 + i; ++n; }
  for (int i = 0; i < 2; ++i) { kind[n] = 3; idx[n] = i; rel[n] = 33 + HB_START; need[n] = 60; ++n; }
  int e = 0, held = 0;
  for (int sl = 0; sl < 89; ++sl) {
    int cap = sl < 8 ? 1 : sl < 32 ? 2 - (sl % 4 == 3) : sl == 32 ? 6 : (sl - 33) % 7 == 6 ? 0 : 2;
    for (; cap > 0; --cap) {
      int best = -1;
      const int live = held - (sl < 33 ? 0 : sl - 32), room = sl < 32 ? H_ROOM : H_ROOM + 6;
      for (int i = 0; i < NSTR; ++i)
        if (!done[i] && rel[i] <= sl && (kind[i] == 0 || live < room) && (best < 0 || need[i] < need[best])) best = i;
      if (best < 0) break;
      if (kind[best] != 0) ++held;
      done[best] = true; s.kind[e] = kind[best]; s.idx[e] = idx[best]; ++e;
    }
    s.end[sl] = e;
  }
  return s;
}
constexpr Stream STR = make_stream();
static_assert(STR.end[88] == NSTR, "every load is issued");
constexpr int str_pos(int kind, int idx) { for (int i = 0; i < NSTR; ++i) if (STR.kind[i] == kind && STR.idx[i] == idx) return i; return -1; }
constexpr int str_count(int kind, int upto) { int n = 0; for (int i = 0; i < upto; ++i) if (STR.kind[i] == kind) ++n; return n; }
struct Down {
  int vbase, hbase, xvo, xso;
  DI int blk(int g) const { return g * 4096; }
};
template <int SLOT, int PART> DI void stream_issue(v4u (&W)[64], v4u (&hA)[28], v4u (&hB)[28], v4u (&x3)[2], const v4u& srd, int lane16, const Down& d) {
  constexpr int lo = SLOT == 0 ? 0 : STR.end[SLOT - 1], hi = STR.end[SLOT];
  sfor<(PART == 1 ? lo + 1 : lo), (PART == 0 && hi > lo ? lo + 1 : hi)>([&](auto I) {
    constexpr int k = STR.kind[I.v], i = STR.idx[I.v];
    if constexpr (k == 0) wload<((i >> 1) & 3) * 1024, true>(W[i - NV_EARLY], srd, lane16, d.vbase + (i & 1) * 112 * 1024 + d.blk(i >> 3));
    if constexpr (k == 1) ldx4<(i & 3) * 1024>(hA[i], srd, lane16, d.hbase + d.blk(i >> 2));
    if constexpr (k == 2) ldx4<(i & 3) * 1024>(hB[i], srd, lane16, d.hbase + 224 * 1024 + d.blk(i >> 2));
    if constexpr (k == 3) ldx4<0>(x3[i], srd, d.xvo, d.xso + i * 64 * XS);
  });
}
constexpr int dn_need(int g) {
  const int H = g / 28, i = g % 28;
  const int ph = str_pos(1 + H, i), pv = H == 0 && 2 * i >= NV_EARLY ? str_pos(0, 2 * i + 1) : 0;
  return ph > pv ? ph : pv;
}
template <int G, int ISSUED> DI void h_acc(v4u (&hA)[28], v4u (&hB)[28], unsigned& m, unsigned tag) {
  static_assert(dn_need(G) < ISSUED, "requested in time");
  vmwait<ISSUED - 1 - dn_need(G)>();
  v4u& x = G < 28 ? hA[G % 28] : hB[G % 28];
  pin(x);
  m = tagacc(m, x, tag);
  pinu(m);
}
template <int G, int ISSUED> DI void h_decide(v4u (&hA)[28], v4u (&hB)[28], unsigned long long bad, const v4u& srd, int lane16, const Down& d,
                                              LAS unsigned char* L, int layer, int w) {
  constexpr int H = G / 28, LO = G % 28;
  v4u (&hh)[28] = H == 0 ? hA : hB;
  if (__builtin_expect(bad != 0ull, 0)) {
    unsigned spin = 0, m;
    vmwait<0>();
    do {
      RETRY(26 + H);
      const int bo = opaque(d.hbase);
      sfor<LO, str_count(1 + H, ISSUED)>([&](auto I) { ldx4<(I.v & 3) * 1024>(hh[I.v], srd, lane16, bo + H * 224 * 1024 + d.blk(I.v >> 2)); });
      vmwait<0>();
      m = 0;
#pragma unroll
      for (int i = LO; i < LO + 7; ++i) { pin(hh[i]); m = tagacc(m, hh[i], layer); }
    } while (bit0_set(m) != 0ull && ++spin < SPIN_MAX);
  }
}

DI void sumsq1(float& ss, unsigned a) {
  const float x = blo(a), y = bhi(a);
  asm volatile("v_fmac_f32 %0, %1, %1\n\tv_fmac_f32 %0, %2, %2" : "+v"(ss) : "v"(x), "v"(y));
}
DI float mulf(float a, float b) { float r; asm volatile("v_mul_f32 %0, %1, %2" : "=v"(r) : "v"(a), "v"(b)); return r; }
DI void addf4(f32x4& a, const f32x4& b) {
  float x = a.x, y = a.y, z = a.z, t = a.w;
  asm volatile("v_add_f32 %0, %0, %4\n\tv_add_f32 %1, %1, %5\n\tv_add_f32 %2, %2, %6\n\tv_add_f32 %3, %3, %7"
               : "+v"(x), "+v"(y), "+v"(z), "+v"(t) : "v"(b.x), "v"(b.y), "v"(b.z), "v"(b.w));
  a = f32x4{x, y, z, t};
}
DI float relu(float a) { const int i = __builtin_bit_cast(int, a); return __builtin_bit_cast(float, i > 0 ? i : 0); }
DI void h_piece(const f32x16& a, unsigned tag, rsrc_t rs, int lane, int so) {
  unsigned d[4];
#pragma unroll
  for (int i = 0; i < 2; ++i)
    pk_tag(relu(a[4 * i]) * a[4 * i + 8], relu(a[4 * i + 1]) * a[4 * i + 9], relu(a[4 * i + 2]) * a[4 * i + 10], relu(a[4 * i + 3]) * a[4 * i + 11], tag,
           d[2 * i], d[2 * i + 1]);
  bstore_sc1(v4u{d[0], d[1], d[2], d[3]}, rs, lane * 16, so);
}
DI void x3_piece(const f32x16& a, rsrc_t rs, int lane, int so) {
  v4u o0, o1;
  widen(a, 1.f, o0, o1);
  bstore_sc1(nosent4(o0), rs, (lane & 31) * XS + (lane >> 5) * 16, so);
  bstore_sc1(nosent4(o1), rs, (lane & 31) * XS + (lane >> 5) * 16, so + 32);
}

DI void ffn(LAS unsigned char* L, rsrc_t rs, const WSrc& ws, const Params& p, v4u (&W)[64], int layer, int c, int rb, int fs, int pi, int nb,
            int w, int lane) {
  const v4u& srd = ws.srd;
  constexpr int hoff = OFF_H;
  const int h = lane >> 5, r = lane & 31, q = (lane & 15) >> 2, lane16 = lane * 16;
  ZSrc z;
  z.srd = srd;
  z.vo = (8 * w + (lane >> 3)) * ZS + (((lane & 7) ^ ((lane >> 2) & 4)) * 16);
  z.so = OFF_ZA + layer * (OFF_ZB - OFF_ZA) + rb * 256;
  z.lds = (unsigned)(size_t)L + M_Z + w * 1024;
  STAMP(4);
  sfor<0, 8>([&](auto I) { z_issue<I.v>(z, z.so); });
  SB();
  LAS unsigned char* zown = L + M_Z + w * 1024 + lane16;
  LAS unsigned char* zr[2];
#pragma unroll
  for (int s = 0; s < 2; ++s)
    zr[s] = L + M_Z + (4 * h + q) * 128 + (4 * (s ^ (q >> 1)) + 2 * ((lane >> 4) & 1) + ((lane & 3) >> 1)) * 16 + 8 * (lane & 1);
  const unsigned dcmask = (w == 0 && h == 0) ? 0xFFFF0000u : 0xFFFFFFFFu;
  const int pt = 4 * rb + pi;
  Down dn;
  dn.vbase = OFF_W2 + layer * (OFF_W3 - OFF_W2) + (nb * 2 * 112 + 28 * w) * 1024;
  dn.hbase = hoff + (pt * 112 + 28 * w) * 1024;
  dn.xvo = (lane & 31) * XS + h * 16;
  dn.xso = OFF_W1 + layer * (OFF_X3 - OFF_W1) + pt * 32 * XS + nb * 128 + w * 32;
  const int ks0 = fs * 7 + 2 * w;
  const bool two = w < 3;
  const bool idft = opaque(w + layer) == 4;
  const rsrc_t rs2 = __builtin_amdgcn_make_buffer_rsrc((void*)p.ws, 0, two ? WS_BYTES : 0, 0x00020000);
  v4u hA[28], hB[28], x3[2], res[2], Ve[NV_EARLY];
  unsigned s0, s1, s2, s3;
  asm volatile("v_mov_b32 %0, -1\n\tv_mov_b32 %1, -1\n\tv_mov_b32 %2, -1\n\tv_mov_b32 %3, -1" : "=v"(s0), "=v"(s1), "=v"(s2), "=v"(s3));
  const v4u sv = {s0, s1, s2, s3};
  const rsrc_t rsf = __builtin_amdgcn_make_buffer_rsrc((void*)p.ws, 0, layer == 0 ? WS_BYTES : 0, 0x00020000);
  const int fo_z = (w * 64 + lane) * 16 + OFF_ZB + 2 * c * ZS,
            fo_x = (w * 16 + (lane >> 2)) * XS + (lane & 3) * 16 + OFF_X3 + 128 * rb * XS + fs * 64;
  STAMP(5);
  vmwait<z_younger(1, -1)>();
  z_fix<0, -1>(bit0_clear(andacc(andacc(1u, z_back<0>(zown)), z_back<1>(zown))), z, zown, L, layer, w);
  STAMP(6);
  BAR();
  STAMP(7);
  CSTAMP(20);
  f32x16 acc[2][2][2];
  float ss[2] = {0.f, 0.f};
  sfor<0, 2>([&](auto HH) {
    constexpr int H = HH.v;
    v4u bf[2][2], sq[2], rbk[2];
    unsigned hq[4];
    float hp[4];
    unsigned zm;
    unsigned long long bad;
    LAS unsigned char* zb[2] = {zr[0] + H * 65536, zr[1] + H * 65536};
    asm volatile("" : "+v"(zb[0]), "+v"(zb[1]));
    LAS unsigned char* zq[2];
    if constexpr (H == 1) { zq[0] = (pi ? zr[1] : zr[0]) + w * 16384; zq[1] = zq[0] + 65536; asm volatile("" : "+v"(zq[0]), "+v"(zq[1])); }
#pragma unroll
    for (int s = 0; s < 2; ++s) bf[0][s] = tr2(zb[s], 1024);
    sfor<0, 32>([&](auto KS) {
      constexpr int ks = KS.v;
      constexpr bool edge = H == 0 && (ks == 3 || ks == 7 || ks == 15);
      constexpr int e = ks >> 1, es = e & 1, et = e >> 1;
      constexpr int sr = H == 1 && ks >= 8 && (ks - 8) % 3 < 2 ? 2 * ((ks - 8) / 3) + (ks - 8) % 3 : -1;
      constexpr int sn = H == 1 && ks >= 9 && (ks - 9) % 3 < 2 ? 2 * ((ks - 9) / 3) + (ks - 9) % 3 : -1;
      constexpr int rd = H == 0 ? z_read_at(ks) : -1, rp = H == 0 && ks > 0 ? z_read_at(ks - 1) : -1, dec = H == 0 ? z_decide_at(ks) : -1;
      mfma_w<ks == 0, true>(acc[H][0][0], W[2 * ks], bf[ks & 1][0]);
      SB();
      if constexpr (ks + 1 < 32 && !edge) bf[(ks + 1) & 1][0] = tr2(zb[0] + (ks + 1) * 2048, 1024);
      if constexpr (rp >= 0) { zm = andacc(z_first(rp) ? 1u : zm, rbk[0]); pinu(zm); }
      if constexpr (H == 1 && ks < 8) hp[0] = mulf(relu(acc[0][et][es][4 * (ks & 1)]), acc[0][et][es][4 * (ks & 1) + 8]);
      if constexpr (H == 1 && ks < 8) hp[1] = mulf(relu(acc[0][et][es][4 * (ks & 1) + 1]), acc[0][et][es][4 * (ks & 1) + 9]);
      if constexpr (sn >= 0) sumsq1(ss[sn & 1], sn < 2 ? sq[sn & 1].x & dcmask : sq[sn & 1].x);
      SB();
      mfma_w<ks == 0, true>(acc[H][1][0], W[2 * ks + 1], bf[ks & 1][0]);
      SB();
      if constexpr (H == 0 && ks < 16) z_issue<(ks < 8 ? 8 + ks : 2 * ks)>(z, z.so);
      if constexpr (H == 1 && ks < 8) hp[2] = mulf(relu(acc[0][et][es][4 * (ks & 1) + 2]), acc[0][et][es][4 * (ks & 1) + 10]);
      if constexpr (H == 1 && ks < 8) hp[3] = mulf(relu(acc[0][et][es][4 * (ks & 1) + 3]), acc[0][et][es][4 * (ks & 1) + 11]);
      if constexpr (sn >= 0) sumsq1(ss[sn & 1], sq[sn & 1].y);
      if constexpr (H == 1 && ks > 0) stream_issue<ks - 1, 1>(W, hA, hB, x3, srd, lane16, dn);
      if constexpr (H == 0 && ks >= 16 && (ks & 3) == 0) {
        constexpr int i = (ks - 16) / 4;
        if constexpr (i < 2) bstore_sc1(sv, rsf, fo_x, i * 64 * XS);
        else bstore_sc1(~sv, rsf, fo_z, (i - 2) * ZS);
      }
      SB();
      mfma_w<ks == 0, true>(acc[H][0][1], W[2 * ks], bf[ks & 1][1]);
      SB();
      if constexpr (ks + 1 < 32 && !edge) bf[(ks + 1) & 1][1] = tr2(zb[1] + (ks + 1) * 2048, 1024);
      if constexpr (rp >= 0) { zm = andacc(zm, rbk[1]); pinu(zm); }
      if constexpr (dec >= 0) { bad = bit0_clear(zm); pins(bad); }
      if constexpr (H == 1 && ks < 8) { pk_tag(hp[0], hp[1], hp[2], hp[3], layer, hq[2 * (ks & 1)], hq[2 * (ks & 1) + 1]); pinu(hq[2 * (ks & 1)]); pinu(hq[2 * (ks & 1) + 1]); }
      if constexpr (sn >= 0) sumsq1(ss[sn & 1], sq[sn & 1].z);
      SB();
      mfma_w<ks == 0, true>(acc[H][1][1], W[2 * ks + 1], bf[ks & 1][1]);
      SB();
      if constexpr (dec >= 0) { pins(bad); z_fix<dec, ks>(bad, z, zown, L, layer, w); }
      if constexpr (rd >= 0) { vmwait<z_younger(rd + 1, ks)>(); rbk[0] = z_back<rd>(zown); rbk[1] = z_back<rd + 1>(zown); }
      if constexpr (sn >= 0) sumsq1(ss[sn & 1], sq[sn & 1].w);
      if constexpr (sr >= 0) sq[sr & 1] = tr2(zq[sr & 1] + (sr >> 1) * 2048, 1024);
      if constexpr (H == 0 && ks < NW_LATE) load_w<64 - NW_LATE + ks>(W, ws);
      if constexpr (H == 0 && ks >= 8 && ks < 16) z_issue<2 * ks + 1>(z, z.so);
      if constexpr (H == 0 && ks >= 16) {
        constexpr int n = ks - 16;
        vload<((n >> 1) & 3) * 1024>(Ve[n], srd, lane16, dn.vbase + (n & 1) * 112 * 1024 + dn.blk(n >> 3));
      }
      if constexpr (H == 1) {
        if constexpr (ks < 8 && (ks & 1)) {
          bstore_sc1(v4u{hq[0], hq[1], hq[2], hq[3]}, et == 0 ? rs : rs2, lane16, hoff + ((4 * rb + es) * 112 + ks0 + et) * 1024);
          if constexpr (et == 1) { if (idft) x3_piece(acc[0][1][es], rs, lane, OFF_X3 + (128 * rb + 32 * es) * XS + fs * 64); }
        }
        stream_issue<ks, (ks == 31 ? 2 : 0)>(W, hA, hB, x3, srd, lane16, dn);
      }
      if constexpr (edge) {
        BAR();
#pragma unroll
        for (int s = 0; s < 2; ++s) bf[(ks + 1) & 1][s] = tr2(zb[s] + (ks + 1) * 2048, 1024);
      }
      SB();
    });
    STAMP(8 + 2 * H);
    if constexpr (H == 0) BAR();
    STAMP(9 + 2 * H);
  });
  mfma_done(acc[1][0][0], acc[1][0][1], acc[1][1][0], acc[1][1][1]);
#pragma unroll
  for (int s = 0; s < 2; ++s) h_piece(acc[1][0][s], layer, rs, lane, hoff + ((4 * rb + 2 + s) * 112 + ks0) * 1024);
  if (two) {
#pragma unroll
    for (int s = 0; s < 2; ++s) h_piece(acc[1][1][s], layer, rs, lane, hoff + ((4 * rb + 2 + s) * 112 + ks0 + 1) * 1024);
  } else if (idft) {
#pragma unroll
    for (int s = 0; s < 2; ++s) x3_piece(acc[1][1][s], rs, lane, OFF_X3 + (128 * rb + 64 + 32 * s) * XS + fs * 64);
  }
  SB();
  stream_issue<32, 2>(W, hA, hB, x3, srd, lane16, dn);
  {
    auto x = __builtin_amdgcn_permlane32_swap(asu(ss[0]), asu(ss[1]), false, false);
    *(LAS float*)(L + M_S1 + w * 256 + lane * 4) = asf(x[0]) + asf(x[1]);
  }
  const int row = 16 * w + (lane >> 2);
  {
    const int n = nb * 64 + row;
    LAS unsigned char* ra = L + M_Z + n * 128 + (((4 * pi + (lane & 3)) ^ ((n & 2) << 1)) * 16);
    res[0] = *(LAS v4u*)ra; res[1] = *(LAS v4u*)(ra + 65536);
  }
  STAMP(12);
  CSTAMP(21);
  unsigned hm = 0;
  sfor<0, 7>([&](auto G) { h_acc<G.v, STR.end[32]>(hA, hB, hm, layer); });
  h_decide<0, STR.end[32]>(hA, hB, bit0_set(hm), srd, lane16, dn, L, layer, w);
  hm = 0;
  BAR();
  STAMP(13);
  LAS unsigned char* rw = L + M_Z + w * 9216 + r * 144 + h * 16;
  LAS unsigned char* rr = L + M_Z + row * 144 + (lane & 3) * 32;
  f32x16 dac[2][2];
  f32x4 fa[2][2], tq[2];
  auto red_write = [&](auto HH, auto K) {
    constexpr int H = HH.v, t = K.v >> 2, g = K.v & 3;
    *(LAS f32x4*)(rw + H * M_RB + t * 32 * 144 + g * 32) = f32x4{dac[H][t][4 * g], dac[H][t][4 * g + 1], dac[H][t][4 * g + 2], dac[H][t][4 * g + 3]};
  };
  sfor<0, 56>([&](auto GG) {
    constexpr int g = GG.v, H = g / 28, i = g % 28;
    v4u& hx = H == 0 ? hA[i] : hB[i];
    unsigned long long bad = 0ull;
    if constexpr (2 * i < NV_EARLY) mfma_v<i == 0>(dac[H][0], Ve[2 * i], hx);
    else mfma_w<false, false>(dac[H][0], W[2 * i - NV_EARLY], hx);
    SB();
    if constexpr (g + 7 < 56) h_acc<g + 7, STR.end[32 + g]>(hA, hB, hm, layer);
    if constexpr (g + 7 < 56 && g % 7 == 6) { bad = bit0_set(hm); pins(bad); hm = 0; }
    if constexpr (g == 1) {
      LAS float* sp = (LAS float*)(L + M_S1 + lane * 4);
      *(LAS float*)(L + M_S1 + 1024 + w * 256 + lane * 4) =
          __builtin_amdgcn_rcpf(__builtin_fmaf((sp[0] + sp[64]) + (sp[128] + sp[192]), 1.f / 512.f, 1e-5f));
    }
    if constexpr (g >= 51 && g <= 53) addf4(fa[0][0], tq[0]);
    SB();
    if constexpr (2 * i < NV_EARLY) mfma_v<i == 0>(dac[H][1], Ve[2 * i + 1], hx);
    else mfma_w<false, false>(dac[H][1], W[2 * i + 1 - NV_EARLY], hx);
    SB();
    if constexpr (g + 7 < 56 && g % 7 == 6) { pins(bad); h_decide<g + 1, STR.end[32 + g]>(hA, hB, bad, srd, lane16, dn, L, layer, w); }
    stream_issue<33 + g, 2>(W, hA, hB, x3, srd, lane16, dn);
    if constexpr (g >= 30 && g <= 46 && g % 2 == 0 && g != 34) red_write(IC<0>{}, IC<(g < 34 ? (g - 30) / 2 : (g - 32) / 2)>{});
    if constexpr (g == 47) BAR();
    if constexpr (g == 49) { fa[0][0] = *(LAS f32x4*)rr; fa[0][1] = *(LAS f32x4*)(rr + 16); }
    if constexpr (g >= 51 && g <= 53) addf4(fa[0][1], tq[1]);
    if constexpr (g >= 50 && g <= 52) { tq[0] = *(LAS f32x4*)(rr + (g - 49) * 9216); tq[1] = *(LAS f32x4*)(rr + (g - 49) * 9216 + 16); }
    SB();
    if constexpr (g == 27) STAMP(14);
  });
  STAMP(15);
  asm volatile("s_nop 12" : "+v"(dac[1][0]), "+v"(dac[1][1]));
  sfor<0, 8>([&](auto K) { red_write(IC<1>{}, K); });
  vmwait<0>();
  pin(x3[0]); pin(x3[1]);
  if (__builtin_expect(layer == 1 && any_sent(mxacc(mxacc(0u, x3[0]), x3[1])), 0)) {
    unsigned spin = 0;
    do {
      RETRY(29);
      ldx4<0>(x3[0], srd, dn.xvo, dn.xso); ldx4<0>(x3[1], srd, dn.xvo, dn.xso + 64 * XS);
      vmwait<0>();
      pin(x3[0]); pin(x3[1]);
    } while (any_sent(mxacc(mxacc(0u, x3[0]), x3[1])) && ++spin < SPIN_MAX);
  }
  STAMP(16);
  BAR();
  STAMP(17);
  fa[1][0] = *(LAS f32x4*)(rr + M_RB); fa[1][1] = *(LAS f32x4*)(rr + M_RB + 16);
#pragma unroll
  for (int ww = 1; ww < 4; ++ww) { fa[1][0] += *(LAS f32x4*)(rr + M_RB + ww * 9216); fa[1][1] += *(LAS f32x4*)(rr + M_RB + ww * 9216 + 16); }
  f32x4 sc[2][2];
#pragma unroll
  for (int H = 0; H < 2; ++H)
#pragma unroll
    for (int x = 0; x < 2; ++x) sc[H][x] = *(LAS f32x4*)(L + M_S1 + 1024 + w * 256 + H * 128 + (lane & 3) * 32 + x * 16);
  if (layer == 0) {
    const bool dc = nb == 0 && row == 0;
#pragma unroll
    for (int H = 0; H < 2; ++H) {
      unsigned o[4];
      float sq[8];
#pragma unroll
      for (int x = 0; x < 2; ++x)
        pk_tag(__builtin_fmaf(fa[H][x][0], sc[H][x][0], blo(res[H][2 * x])), __builtin_fmaf(fa[H][x][1], sc[H][x][1], bhi(res[H][2 * x])),
               __builtin_fmaf(fa[H][x][2], sc[H][x][2], blo(res[H][2 * x + 1])), __builtin_fmaf(fa[H][x][3], sc[H][x][3], bhi(res[H][2 * x + 1])), 0u,
               o[2 * x], o[2 * x + 1]);
      bstore_sc1(v4u{o[0], o[1], o[2], o[3]}, rs, row * ZS + (lane & 3) * 16, OFF_ZA + nb * 64 * ZS + pt * 64 + H * 128);
#pragma unroll
      for (int j = 0; j < 4; ++j) {
        const unsigned u = dc ? 0u : o[j];
        sq[2 * j] = blo(u) * blo(u); sq[2 * j + 1] = bhi(u) * bhi(u);
      }
      float t4[4], t2[2];
#pragma unroll
      for (int j = 0; j < 4; ++j) {
        auto x = __builtin_amdgcn_permlane32_swap(asu(sq[j]), asu(sq[j + 4]), false, false);
        t4[j] = asf(x[0]) + asf(x[1]);
      }
#pragma unroll
      for (int j = 0; j < 2; ++j) {
        auto x = __builtin_amdgcn_permlane16_swap(asu(t4[j]), asu(t4[j + 2]), false, false);
        t2[j] = asf(x[0]) + asf(x[1]);
        t2[j] += dppf<0x124>(t2[j]);
        t2[j] += dppf<0x128>(t2[j]);
      }
      *(LAS f32x2*)(L + M_S2 + w * 256 + H * 128 + (8 * (lane & 3) + 4 * h + 2 * ((lane >> 4) & 1)) * 4) = f32x2{t2[0], t2[1]};
    }
    BAR();
    if (w == 0) {
      LAS float* sp = (LAS float*)(L + M_S2 + lane * 4);
      const float t = (sp[0] + sp[64]) + (sp[128] + sp[192]);
      __builtin_amdgcn_raw_buffer_store_b32(nosent(asu(t)), rs, stat_of(h * 64 + r), OFF_ST + nb * 8192 + pt * 64, SC1);
    }
  } else {
    LAS unsigned char* tb = L + M_TB + w * 2560;
#pragma unroll
    for (int H = 0; H < 2; ++H) {
      const f32x4 a = fa[H][0] * sc[H][0], bq = fa[H][1] * sc[H][1];
      *(LAS v4u*)(tb + H * 1280 + (lane >> 2) * 80 + (lane & 3) * 16) = v4u{pk(a.x, a.y), pk(a.z, a.w), pk(bq.x, bq.y), pk(bq.z, bq.w)};
    }
#pragma unroll
    for (int H = 0; H < 2; ++H) {
      const v4u y = tr2(tb + H * 1280 + (8 * h + q) * 80 + ((lane >> 4) & 1) * 32 + (lane & 3) * 8, 4 * 80);
      v4u o;
#pragma unroll
      for (int j = 0; j < 4; ++j) o[j] = pk(blo(x3[H][j]) + blo(y[j]), bhi(x3[H][j]) + bhi(y[j]));
      const int tok = token_of(32 * (pt + 2 * H) + r);
      *(v4u*)(p.out + (size_t)tok * 512 + nb * 64 + 16 * w + 8 * h) = o;
    }
  }
  SB();
}

#define XMAP 1
__global__ void __launch_bounds__(256, 1) k_main(Params p) {
  extern __shared__ __attribute__((aligned(16))) unsigned char smem[];
  LAS unsigned char* L = (LAS unsigned char*)smem;
  const rsrc_t rs = __builtin_amdgcn_make_buffer_rsrc((void*)p.ws, 0, WS_BYTES, 0x00020000);
  const v4u srd = {(unsigned)(size_t)p.ws, (unsigned)((size_t)p.ws >> 32) & 0xFFFFu, (unsigned)WS_BYTES, 0x00020000u};
#pragma unroll 1
  for (int layer = 0; layer < 2; ++layer) {
    int tid = threadIdx.x;
    asm volatile("" : "+v"(tid));
    const int c = opaque(blockIdx.x);
    const int lane = tid & 63, w = __builtin_amdgcn_readfirstlane(tid >> 6);
    const int rb = 4 * (c & 3) + (c >> 6), fs = 8 * ((c >> 2) & 1) + ((c >> 3) & 7), pi = fs >> 3, nb = fs & 7;
    WSrc ws;
    ws.srd = srd; ws.srd1 = srd;
    if (w == 3 && layer == 0) ws.srd1.z = 0u;
    ws.lane16 = lane * 16; ws.base = OFF_W1 + (fs * 8 + 2 * w) * 32768;
    v4u W[64];
    unsigned wf[8];
    const int wfvo = (16 * w + (lane & 15) + 64 * 4 * (lane >> 4)) * 4;
    STAMP(0);
    CSTAMP(22);
    v4u zr, zi, sa, sb;
    asm volatile("" : "=v"(zr), "=v"(zi), "=v"(sa), "=v"(sb));
    const int first = c == 0;
    if (layer == 0) {
      ldx4<0>(zr, srd, tid * 16, OFF_ZA + (2 * c + first) * ZS); ldx4<0>(zi, srd, tid * 16, OFF_ZA + (2 * c + 1 - first) * ZS);
      ldx4<0>(sa, srd, tid * 16, OFF_R0); ldx4<0>(sb, srd, tid * 16, OFF_R0 + 4096);
    }
    asm volatile("buffer_load_dword %0, %8, %9, %10 offen\n\tbuffer_load_dword %1, %8, %9, %10 offen offset:256\n\t"
                 "buffer_load_dword %2, %8, %9, %10 offen offset:512\n\tbuffer_load_dword %3, %8, %9, %10 offen offset:768\n\t"
                 "buffer_load_dword %4, %8, %9, %11 offen\n\tbuffer_load_dword %5, %8, %9, %11 offen offset:256\n\t"
                 "buffer_load_dword %6, %8, %9, %11 offen offset:512\n\tbuffer_load_dword %7, %8, %9, %11 offen offset:768"
                 : "=&v"(wf[0]), "=&v"(wf[1]), "=&v"(wf[2]), "=&v"(wf[3]), "=&v"(wf[4]), "=&v"(wf[5]), "=&v"(wf[6]), "=&v"(wf[7])
                 : "v"(wfvo), "s"(srd), "s"(OFF_WT + c * WS_), "s"(OFF_WT + c * WS_ + 4096));
    SB();
    ColConst k;
    col_consts(k, w, lane, W, ws);
    SB();
    vmwait<16>();
    asm volatile("" : "+v"(wf[0]), "+v"(wf[1]), "+v"(wf[2]), "+v"(wf[3]), "+v"(wf[4]), "+v"(wf[5]), "+v"(wf[6]), "+v"(wf[7]));
    pin(zr); pin(zi); pin(sa); pin(sb);
    float rstd[8], fr[8], fi[8];
#pragma unroll
    for (int e = 0; e < 8; ++e) { fr[e] = blo(wf[e]) * (1.f / 2048.f); fi[e] = bhi(wf[e]) * (1.f / 2048.f); pinf(fr[e]); pinf(fi[e]); }
    STAMP(1);
    if (layer == 1) {
      __builtin_amdgcn_s_sleep(DELAY_C);
      unsigned spin = 0;
      bool again;
      do {
        RETRY(28);
        v4u st[16];
        ldx4<0>(zr, srd, tid * 16, OFF_ZA + (2 * c + first) * ZS); ldx4<0>(zi, srd, tid * 16, OFF_ZA + (2 * c + 1 - first) * ZS);
        sfor<0, 16>([&](auto I) { ldx4<0>(st[I.v], srd, tid * 16, OFF_ST + I.v * 4096); });
        vmwait<0>();
        pin(zr); pin(zi);
#pragma unroll
        for (int i = 0; i < 16; ++i) pin(st[i]);
        f32x4 a = __builtin_bit_cast(f32x4, st[0]), bq = __builtin_bit_cast(f32x4, st[1]);
#pragma unroll
        for (int n = 1; n < 8; ++n) { a += __builtin_bit_cast(f32x4, st[2 * n]); bq += __builtin_bit_cast(f32x4, st[2 * n + 1]); }
        sa = __builtin_bit_cast(v4u, a); sb = __builtin_bit_cast(v4u, bq);
        const f32x4 t = a + bq;
        const float u = (t.x + t.y) + (t.z + t.w);
        unsigned m = 0u;
        if (__builtin_expect(__builtin_amdgcn_ballot_w64(u != u) != 0ull, 0)) {
#pragma unroll
          for (int i = 0; i < 16; ++i) m = mxacc(m, st[i]);
        }
        again = (__builtin_amdgcn_ballot_w64(m == SENT) | bit0_set(tagacc(tagacc(0u, zr, 0u), zi, 0u))) != 0ull;
      } while (__builtin_expect(again, 0) && ++spin < SPIN_MAX);
    }
    load_ws<16, 18>(W, ws);
#pragma unroll
    for (int e = 0; e < 4; ++e) {
      rstd[e] = __builtin_amdgcn_rsqf(__builtin_fmaf(asf(sa[e]), 1.f / 512.f, 1e-5f));
      rstd[4 + e] = __builtin_amdgcn_rsqf(__builtin_fmaf(asf(sb[e]), 1.f / 512.f, 1e-5f));
    }
    load_ws<18, 20>(W, ws);
    STAMP(2);
    col_compute<20>(L, rs, k, fr, fi, zr, zi, rstd, c, w, lane, OFF_ZA + layer * (OFF_ZB - OFF_ZA), W, ws);
    STAMP(3);
    CSTAMP(23);
    load_ws<50, 64 - NW_LATE>(W, ws);
    __builtin_amdgcn_s_sleep(DELAY_A);
    ffn(L, rs, ws, p, W, layer, c, rb, fs, pi, nb, w, lane);
    STAMP(18);
  }
}

extern "C" void kernel_launch(void* const* d_in, const int* in_sizes, int n_in, void* d_out, int out_size, void* d_ws, size_t ws_size,
                              hipStream_t stream) {
  Params p;
  p.x = (const u16*)d_in[0]; p.wr = (const u16*)d_in[1]; p.wi = (const u16*)d_in[2];
  p.wg = (const u16*)d_in[4]; p.wu = (const u16*)d_in[5]; p.wd = (const u16*)d_in[6];
  p.out = (u16*)d_out; p.ws = (unsigned char*)d_ws;
  (void)hipFuncSetAttribute((const void*)k_prep, hipFuncAttributeMaxDynamicSharedMemorySize, P_LDS);
  (void)hipFuncSetAttribute((const void*)k_main, hipFuncAttributeMaxDynamicSharedMemorySize, M_LDS);
  k_prep<<<256, 512, P_LDS, stream>>>(p);
  k_main<<<256, 256, M_LDS, stream>>>(p);
}
```

```cpp
#include <hip/hip_runtime.h>
#include <hip/hip_bf16.h>

#define LAS __attribute__((address_space(3)))
#define DI __device__ __forceinline__
typedef unsigned short u16;
typedef unsigned v4u __attribute__((ext_vector_type(4)));
typedef unsigned v2u __attribute__((ext_vector_type(2)));
typedef float f32x2 __attribute__((ext_vector_type(2)));
typedef float f32x4 __attribute__((ext_vector_type(4)));
typedef float f32x16 __attribute__((ext_vector_type(16)));
typedef short s16x4 __attribute__((ext_vector_type(4)));
typedef __bf16 bf16x2 __attribute__((ext_vector_type(2)));
typedef __bf16 bf16x8 __attribute__((ext_vector_type(8)));
typedef __amdgpu_buffer_rsrc_t rsrc_t;

constexpr int MB = 1 << 20;
constexpr int ZS = 4224, XS = 1152, WS_ = 8320;
constexpr int OFF_ZA = 0, OFF_ZB = 9 * MB / 4;
constexpr int OFF_WT = 9 * MB;
constexpr int OFF_W1 = 12 * MB;
constexpr int OFF_W2 = 16 * MB;
constexpr int OFF_W3 = 18 * MB;
constexpr int OFF_H = 20 * MB;
constexpr int OFF_X3 = 36 * MB;
constexpr int OFF_R0 = 39 * MB;
constexpr int OFF_ST = 39 * MB + 65536;
constexpr int WS_BYTES = 40 * MB;
constexpr int SC1 = 16;
constexpr unsigned SENT = 0xFFFFFFFFu;
constexpr unsigned SPIN_MAX = 1u << 16;

struct Params {
  const u16* x; const u16* wr; const u16* wi; const u16* wg; const u16* wu; const u16* wd;
  u16* out; unsigned char* ws;
};

DI unsigned pk(float a, float b) { f32x2 v = {a, b}; return __builtin_bit_cast(unsigned, __builtin_convertvector(v, bf16x2)); }
DI float asf(unsigned u) { return __builtin_bit_cast(float, u); }
DI unsigned asu(float f) { return __builtin_bit_cast(unsigned, f); }
DI bf16x2 asb2(unsigned u) { return __builtin_bit_cast(bf16x2, u); }
template <int V> struct IC { static constexpr int v = V; };
template <int I, int N, class Fn> DI void sfor(Fn&& fn) { if constexpr (I < N) { fn(IC<I>{}); sfor<I + 1, N>(fn); } }
DI int opaque(int x) { asm volatile("" : "+s"(x)); return x; }
DI float blo(unsigned u) { return __builtin_bit_cast(float, u << 16); }
DI float bhi(unsigned u) { return __builtin_bit_cast(float, u & 0xFFFF0000u); }
DI float rbf(float a) { return blo(pk(a, a)); }
DI void pk_tag(float a0, float b0, float a1, float b1, unsigned tag, unsigned& d0, unsigned& d1) {
  const unsigned c = pk(b0, b1), up = (tag ^ 1u) << 16;
  d0 = (__builtin_amdgcn_perm(c, asu(a0) + up, 0x05040302u) & ~1u) | tag;
  d1 = (__builtin_amdgcn_perm(c, asu(a1) + up, 0x07060302u) & ~1u) | tag;
}
DI unsigned nosent(unsigned u) { return u < 0xFFFFFFFEu ? u : 0xFFFFFFFEu; }
DI v4u nosent4(v4u v) { v.x = nosent(v.x); v.y = nosent(v.y); v.z = nosent(v.z); v.w = nosent(v.w); return v; }
DI unsigned umax(unsigned a, unsigned b) { return a > b ? a : b; }
DI unsigned mx4(v4u v) { return umax(umax(v.x, v.y), umax(v.z, v.w)); }
DI bool any_sent(unsigned m) { return __builtin_amdgcn_ballot_w64(m == SENT) != 0ull; }
DI f32x16 mfma32(v4u a, v4u b, f32x16 c) {
  return __builtin_amdgcn_mfma_f32_32x32x16_bf16(__builtin_bit_cast(bf16x8, a), __builtin_bit_cast(bf16x8, b), c, 0, 0, 0);
}
DI f32x4 mfma16(v4u a, v4u b, f32x4 c) {
  return __builtin_amdgcn_mfma_f32_16x16x32_bf16(__builtin_bit_cast(bf16x8, a), __builtin_bit_cast(bf16x8, b), c, 0, 0, 0);
}
template <bool FIRST, bool WA> DI void mfma_w(f32x16& acc, const v4u& wt, const v4u& x) {
  if constexpr (FIRST && WA) asm volatile("v_mfma_f32_32x32x16_bf16 %0, %1, %2, 0" : "=&v"(acc) : "a"(wt), "v"(x));
  if constexpr (FIRST && !WA) asm volatile("v_mfma_f32_32x32x16_bf16 %0, %2, %1, 0" : "=&v"(acc) : "a"(wt), "v"(x));
  if constexpr (!FIRST && WA) asm volatile("v_mfma_f32_32x32x16_bf16 %0, %1, %2, %0" : "+v"(acc) : "a"(wt), "v"(x));
  if constexpr (!FIRST && !WA) asm volatile("v_mfma_f32_32x32x16_bf16 %0, %2, %1, %0" : "+v"(acc) : "a"(wt), "v"(x));
}
template <bool FIRST> DI void mfma_v(f32x16& acc, const v4u& wt, const v4u& x) {
  if constexpr (FIRST) asm volatile("v_mfma_f32_32x32x16_bf16 %0, %2, %1, 0" : "=&v"(acc) : "v"(wt), "v"(x));
  else asm volatile("v_mfma_f32_32x32x16_bf16 %0, %2, %1, %0" : "+v"(acc) : "v"(wt), "v"(x));
}
template <bool FIRST> DI void mfma_vv(f32x16& acc, const v4u& a, const v4u& b) {
  if constexpr (FIRST) asm volatile("v_mfma_f32_32x32x16_bf16 %0, %1, %2, 0" : "=&v"(acc) : "v"(a), "v"(b));
  else asm volatile("v_mfma_f32_32x32x16_bf16 %0, %1, %2, %0" : "+v"(acc) : "v"(a), "v"(b));
}
template <int IMM> DI void vload(v4u& dst, const v4u& srd, int lane16, int so) {
  asm volatile("buffer_load_dwordx4 %0, %1, %2, %3 offen offset:%4" : "=v"(dst) : "v"(lane16), "s"(srd), "s"(so), "i"(IMM));
}
template <int IMM, bool REUSE> DI void wload(v4u& dst, const v4u& srd, int lane16, int so) {
  if constexpr (REUSE) asm volatile("buffer_load_dwordx4 %0, %1, %2, %3 offen offset:%4" : "+a"(dst) : "v"(lane16), "s"(srd), "s"(so), "i"(IMM));
  else asm volatile("buffer_load_dwordx4 %0, %1, %2, %3 offen offset:%4" : "=a"(dst) : "v"(lane16), "s"(srd), "s"(so), "i"(IMM));
}
DI void mfma_done(f32x16& a, f32x16& b, f32x16& c, f32x16& d) { asm volatile("s_nop 12" : "+v"(a), "+v"(b), "+v"(c), "+v"(d)); }
DI v2u trread(LAS unsigned char* p) { return __builtin_bit_cast(v2u, __builtin_amdgcn_ds_read_tr16_b64_v4i16((LAS s16x4*)p)); }
DI v4u tr2(LAS unsigned char* p, int d) { v2u a = trread(p), b = trread(p + d); return v4u{a.x, a.y, b.x, b.y}; }
DI v4u bload(rsrc_t rs, int voff, int soff) { return __builtin_amdgcn_raw_buffer_load_b128(rs, voff, soff, 0); }
DI v4u bload_sc1(rsrc_t rs, int voff, int soff) { return __builtin_amdgcn_raw_buffer_load_b128(rs, voff, soff, SC1); }
DI void bstore(v4u v, rsrc_t rs, int voff, int soff) { __builtin_amdgcn_raw_buffer_store_b128(v, rs, voff + soff, 0, 0); }
DI void bstore_sc1(v4u v, rsrc_t rs, int voff, int soff) { __builtin_amdgcn_raw_buffer_store_b128(v, rs, voff + soff, 0, SC1); }
template <int I> DI v4u fload(rsrc_t rs, int lane16, int base) { return bload(rs, lane16 + (I & 3) * 1024, base + (I >> 2) * 4096); }
template <int I> DI v4u fload_sc1(rsrc_t rs, int lane16, int base) { return bload_sc1(rs, lane16 + (I & 3) * 1024, base + (I >> 2) * 4096); }
#define SB() __builtin_amdgcn_sched_barrier(0)
#define MEMFENCE() asm volatile("" ::: "memory")
#define BAR() asm volatile("s_waitcnt lgkmcnt(0)\n\ts_barrier" ::: "memory")
template <int N> DI void vmwait() { asm volatile("s_waitcnt vmcnt(%0)" :: "i"(N) : "memory"); }
DI void pin(v4u& a) { asm volatile("" : "+v"(a)); }
DI f32x16 zero16() { f32x16 z; for (int i = 0; i < 16; ++i) z[i] = 0.f; return z; }
DI v4u pack8(const f32x16& a, int s, float sc) {
  return v4u{pk(a[8 * s] * sc, a[8 * s + 1] * sc), pk(a[8 * s + 2] * sc, a[8 * s + 3] * sc),
             pk(a[8 * s + 4] * sc, a[8 * s + 5] * sc), pk(a[8 * s + 6] * sc, a[8 * s + 7] * sc)};
}
DI void widen(const f32x16& a, float sc, v4u& o0, v4u& o1) {
  unsigned d[8];
#pragma unroll
  for (int i = 0; i < 8; ++i) d[i] = pk(a[2 * i] * sc, a[2 * i + 1] * sc);
  auto r0 = __builtin_amdgcn_permlane32_swap(d[0], d[2], false, false);
  auto r1 = __builtin_amdgcn_permlane32_swap(d[1], d[3], false, false);
  auto r2 = __builtin_amdgcn_permlane32_swap(d[4], d[6], false, false);
  auto r3 = __builtin_amdgcn_permlane32_swap(d[5], d[7], false, false);
  o0 = v4u{r0[0], r1[0], r0[1], r1[1]};
  o1 = v4u{r2[0], r3[0], r2[1], r3[1]};
}
template <int CTRL> DI float dppf(float v) {
  return __builtin_bit_cast(float, __builtin_amdgcn_update_dpp(0, __builtin_bit_cast(int, v), CTRL, 0xF, 0xF, true));
}
DI float sum32(float v) {
  v += dppf<0xB1>(v); v += dppf<0x4E>(v); v += dppf<0x141>(v); v += dppf<0x140>(v);
  unsigned u = __builtin_bit_cast(unsigned, v);
  auto r = __builtin_amdgcn_permlane16_swap(u, u, false, false);
  return __builtin_bit_cast(float, (unsigned)r[0]) + __builtin_bit_cast(float, (unsigned)r[1]);
}
DI int stat_of(int p) { return ((p >> 2) & 1) * 4096 + (p >> 3) * 16 + (p & 3) * 4; }
DI int token_of(int p) {
  const int l = (p >> 3) & 63, e = p & 7;
  return 32 * (16 * (p >> 9) + 4 * (l >> 4) + (e & 3)) + (l & 15) + 16 * (e >> 2);
}
#define TOUCH_POLICY " nt"
#define GLDS_POLICY " nt"
DI void glds16(const void* sbase, unsigned voff, unsigned lds) {
  unsigned keep;
  asm volatile("s_mov_b32 %0, m0\n\ts_mov_b32 m0, %3\n\ts_nop 0\n\tglobal_load_lds_dwordx4 %1, %2" GLDS_POLICY "\n\ts_mov_b32 m0, %0"
               : "=&s"(keep) : "v"(voff), "s"(sbase), "s"(lds) : "memory");
}
template <int IMM> DI void ld2u16(unsigned& a, unsigned& b, int voff, const v4u& sa, const v4u& sb, int soff) {
  asm volatile("buffer_load_ushort %0, %2, %3, %5 offen offset:%6\n\tbuffer_load_ushort %1, %2, %4, %5 offen offset:%6"
               : "=&v"(a), "=&v"(b) : "v"(voff), "s"(sa), "s"(sb), "s"(soff), "i"(IMM));
}
DI v4u cosfrag(float a0, float step, int off) {
  float v[8];
#pragma unroll
  for (int j = 0; j < 8; ++j) v[j] = __builtin_amdgcn_cosf(__builtin_fmaf(step, (float)(off + j), a0));
  return v4u{pk(v[0], v[1]), pk(v[2], v[3]), pk(v[4], v[5]), pk(v[6], v[7])};
}

constexpr int CODE_BYTES = 40 * 1024;
constexpr int P_XL = 0, P_GL = 33280, P_DL = P_GL + 65536, P_ZB = P_DL + 33280, P_WB = P_ZB + 4 * 2560,
              P_FL = P_WB + 64 * 144, P_LDS = P_FL + 64;

__global__ void __launch_bounds__(512, 2) k_prep(Params p) {
  extern __shared__ __attribute__((aligned(16))) unsigned char smem[];
  LAS unsigned char* L = (LAS unsigned char*)smem;
  const unsigned Lb = (unsigned)(size_t)L;
  const int tid = threadIdx.x, lane = tid & 63, w8 = __builtin_amdgcn_readfirstlane(tid >> 6), w = w8 & 3;
  const int b = blockIdx.x, xcd = b & 7, q = (b >> 3) & 3, tgi = b >> 5;
  const int t1 = xcd * 8 + tgi, tg = xcd * 7 + tgi;
  const bool heavy = tgi < 7;
  const int up = tg >= 28, cb = tg - 28 * up;
#define ROTQ q
  const int h = lane >> 5, r = lane & 31, ft = 4 * q + w;
  const rsrc_t rs = __builtin_amdgcn_make_buffer_rsrc((void*)p.ws, 0, WS_BYTES, 0x00020000);
#define PST(i)
  PST(0);
  if (b == 0 && tid == 256) *(unsigned long long*)(p.ws + 43 * MB) = __builtin_amdgcn_s_memrealtime();
  auto landed = [&](int stage) { *(volatile LAS unsigned*)(L + P_FL + stage * 16 + w * 4) = 1u; };
  auto await = [&](int stage) {
    v4u f;
    for (;;) {
      f = *(volatile LAS v4u*)(L + P_FL + stage * 16);
      if (__builtin_amdgcn_readfirstlane(f.x & f.y & f.z & f.w) != 0u) break;
      __builtin_amdgcn_s_sleep(1);
    }
    MEMFENCE();
  };
  if (__builtin_expect(w8 >= 4, 1)) {
    if (lane < 4) *(LAS unsigned*)(L + P_FL + lane * 16 + w * 4) = 0u;
    const unsigned vo = (lane >> 3) * 3584 + (((lane & 7) ^ ((lane >> 2) & 4)) * 16);
    const u16* gsrc = (up ? p.wu : p.wg) + 64 * cb;
    auto gu = [&](int i) {
      const int blk = 16 * (((i >> 1) + ROTQ) & 3) + 4 * w + 2 * (i & 1);
      glds16(gsrc + (size_t)blk * 8 * 1792, vo, Lb + P_GL + blk * 1024);
      glds16(gsrc + (size_t)(blk + 1) * 8 * 1792, vo, Lb + P_GL + (blk + 1) * 1024);
    };
#pragma unroll
    for (int i = 0; i < 8; ++i) {
      const int row = 8 * (((i >> 1) + ROTQ) & 3) + 2 * w + (i & 1);
      glds16(p.x + (size_t)token_of(32 * t1 + row) * 512, lane * 16, Lb + P_XL + row * 1040);
    }
    if (heavy) {
#pragma unroll
      for (int i = 0; i < 8; ++i) {
        const int row = 8 * (((i >> 1) + ROTQ) & 3) + 2 * w + (i & 1);
        glds16(p.wd + (size_t)(32 * tg + row) * 512, lane * 16, Lb + P_DL + row * 1040);
      }
      gu(0); gu(1);
    }
    BAR();
    if (heavy) vmwait<12>(); else vmwait<0>();
    PST(1);
    landed(0);
    if (heavy) { gu(2); gu(3); vmwait<8>(); }
    PST(2);
    landed(1);
    if (heavy) { gu(4); gu(5); gu(6); gu(7); }
    const int kk0 = 32 * t1 + 8 * w;
    unsigned fr[8], fi[8];
    {
      const v4u sr = {(unsigned)(size_t)p.wr, (unsigned)((size_t)p.wr >> 32) & 0xFFFFu, 2048u * 514u, 0x00020000u};
      const v4u si = {(unsigned)(size_t)p.wi, (unsigned)((size_t)p.wi >> 32) & 0xFFFFu, 2048u * 514u, 0x00020000u};
      const int wc = 64 * q + lane, src = (wc == 0 ? 256 : wc) * 2;
      sfor<0, 8>([&](auto I) { ld2u16<I.v * 514>(fr[I.v], fi[I.v], src, sr, si, kk0 * 514); });
    }
    PST(3);
    vmwait<16>();
    landed(2);
    PST(4);
    await(0);
    {
      const int row = 8 * q + 2 * w + h;
      const v4u u0 = *(LAS v4u*)(L + P_XL + row * 1040 + r * 32), u1 = *(LAS v4u*)(L + P_XL + row * 1040 + r * 32 + 16);
      float v[16];
#pragma unroll
      for (int i = 0; i < 4; ++i) { v[2 * i] = blo(u0[i]); v[2 * i + 1] = bhi(u0[i]); v[8 + 2 * i] = blo(u1[i]); v[9 + 2 * i] = bhi(u1[i]); }
      float s = 0.f;
#pragma unroll
      for (int i = 0; i < 16; ++i) s += v[i];
      const float m = sum32(s) * (1.f / 512.f);
      float ss = 0.f;
#pragma unroll
      for (int i = 0; i < 16; ++i) { const float d = v[i] - m; ss = __builtin_fmaf(d, d, ss); }
      ss = sum32(ss);
      if (r == 0) __builtin_amdgcn_raw_buffer_store_b32(asu(ss), rs, stat_of(32 * t1 + row), OFF_R0, 0);
    }
    {
      const v4u sv = {SENT, SENT, SENT, SENT};
      const int t16 = (w * 64 + lane) * 16;
#pragma unroll
      for (int i = 0; i < 7; ++i) bstore_sc1(sv, rs, t16, OFF_H + b * 28672 + i * 4096);
      if (t16 < 256) bstore_sc1(sv, rs, t16, OFF_ST + b * 256);
    }
    PST(5);
    asm volatile("s_waitcnt vmcnt(8)"
                 : "+v"(fr[0]), "+v"(fr[1]), "+v"(fr[2]), "+v"(fr[3]), "+v"(fr[4]), "+v"(fr[5]), "+v"(fr[6]), "+v"(fr[7]),
                   "+v"(fi[0]), "+v"(fi[1]), "+v"(fi[2]), "+v"(fi[3]), "+v"(fi[4]), "+v"(fi[5]), "+v"(fi[6]), "+v"(fi[7]) :: "memory");
    PST(6);
    {
      v4u o0, o1;
#pragma unroll
      for (int i = 0; i < 4; ++i) { o0[i] = fr[i] | (fi[i] << 16); o1[i] = fr[4 + i] | (fi[4 + i] << 16); }
      *(LAS v4u*)(L + P_WB + lane * 144 + w * 32) = o0;
      *(LAS v4u*)(L + P_WB + lane * 144 + w * 32 + 16) = o1;
    }
    asm volatile("s_waitcnt lgkmcnt(0)" ::: "memory");
    landed(3);
    await(3);
#pragma unroll
    for (int i = 0; i < 2; ++i) {
      const int col = 16 * w + 8 * i + (lane >> 3);
      bstore_sc1(*(LAS v4u*)(L + P_WB + col * 144 + (lane & 7) * 16), rs, (64 * q + col) * WS_ + (lane & 7) * 16, OFF_WT + t1 * 128);
    }
  } else {
    unsigned touch, touch2;
    {
      const char* base = (const char*)(up ? p.wu : p.wg) + (128 * q + 64 * (w & 1)) * 3584 + (heavy ? 128 * cb : 0);
      int off = lane * 3584;
      if (w >= 2) { base = (const char*)(w == 2 ? p.wr : p.wi) + (32 * t1 * 257 + 64 * q) * 2; off = r * 514; }
      asm volatile("global_load_ushort %0, %1, %2" TOUCH_POLICY : "=v"(touch) : "v"(off), "s"(base));
      unsigned lo, hi;
      asm volatile("s_getpc_b64 s[20:21]\n\ts_add_u32 %0, s20, _Z6k_prep6Params@rel32@lo+4\n\ts_addc_u32 %1, s21, _Z6k_prep6Params@rel32@hi+12"
                   : "=s"(lo), "=s"(hi) :: "s20", "s21", "scc");
      off = (w < 2 ? w : 2 + (2 * (b >> 3) + w) % 3) * 8192 + lane * 128;
      asm volatile("global_load_dword %0, %1, %2" : "=v"(touch2) : "v"(off < CODE_BYTES ? off : 0), "s"(((unsigned long long)hi << 32) | lo));
    }
    SB();
    const int feat = 32 * ft + r;
    const float step = (feat == 0 ? 0.f : feat == 1 ? 256.f : (float)(feat >> 1)) * (1.f / 512.f);
    const float ph = (feat > 1 && (feat & 1)) ? 0.25f : 0.f;
    const float a0 = ph + step * (float)(8 * h);
    const unsigned sgn = (feat > 1 && (feat & 2)) ? 0x80008000u : 0u;
    v4u F[32];
#pragma unroll
    for (int ks = 0; ks < 16; ++ks) { F[ks] = cosfrag(a0, step, 16 * ks); F[ks + 16] = F[ks] ^ sgn; }
    const float lsc = feat < 2 ? 0.04419417382415922f : 0.0625f;
#pragma unroll
    for (int ks = 0; ks < 32; ks += 8)
      asm volatile("" : "+v"(F[ks]), "+v"(F[ks + 1]), "+v"(F[ks + 2]), "+v"(F[ks + 3]), "+v"(F[ks + 4]), "+v"(F[ks + 5]), "+v"(F[ks + 6]), "+v"(F[ks + 7]));
    v4u I[2];
#pragma unroll
    for (int s = 0; s < 2; ++s) {
      const int e = r - 16 * s - 8 * h;
      const unsigned one = (e & 1) ? 0x3F800000u : 0x00003F80u;
      I[s] = v4u{(e >> 1) == 0 ? one : 0u, (e >> 1) == 1 ? one : 0u, (e >> 1) == 2 ? one : 0u, (e >> 1) == 3 ? one : 0u};
    }
    pin(I[0]); pin(I[1]);
    PST(1);
    BAR();
    await(0);
    PST(2);
    f32x16 aZ;
    {
      LAS unsigned char* xa = L + P_XL + r * 1040 + h * 16;
      v4u a[4];
#pragma unroll
      for (int i = 0; i < 4; ++i) a[i] = *(LAS v4u*)(xa + i * 32);
      sfor<0, 32>([&](auto KS) {
        constexpr int ks = KS.v;
        mfma_vv<ks == 0>(aZ, a[ks & 3], F[ks]);
        SB();
        if constexpr (ks + 4 < 32) a[ks & 3] = *(LAS v4u*)(xa + (ks + 4) * 32);
        SB();
      });
    }
    unsigned zd[8];
    auto z_out = [&](auto STEP, v4u (&o)[2]) {
      constexpr int n = STEP.v;
      LAS unsigned char* zb = L + P_ZB + w * 2560;
      if constexpr (n < 4) {
        pk_tag(aZ[4 * n] * lsc, aZ[4 * n + 1] * lsc, aZ[4 * n + 2] * lsc, aZ[4 * n + 3] * lsc, 0u, zd[2 * n], zd[2 * n + 1]);
        asm volatile("" : "+v"(zd[2 * n]), "+v"(zd[2 * n + 1]));
      }
      if constexpr (n == 4 || n == 5) {
        auto r0 = __builtin_amdgcn_permlane32_swap(zd[4 * (n - 4)], zd[4 * (n - 4) + 2], false, false);
        auto r1 = __builtin_amdgcn_permlane32_swap(zd[4 * (n - 4) + 1], zd[4 * (n - 4) + 3], false, false);
        *(LAS v4u*)(zb + r * 80 + h * 16 + 32 * (n - 4)) = v4u{r0[0], r1[0], r0[1], r1[1]};
      }
      if constexpr (n == 6) { o[0] = *(LAS v4u*)(zb + (lane >> 2) * 80 + (lane & 3) * 16); o[1] = *(LAS v4u*)(zb + (16 + (lane >> 2)) * 80 + (lane & 3) * 16); }
      if constexpr (n == 7 || n == 8) bstore_sc1(o[n - 7], rs, (32 * ft + 16 * (n - 7) + (lane >> 2)) * ZS + (lane & 3) * 16, OFF_ZA + t1 * 64);
    };
    auto f_out = [&](auto N, const f32x16& a, float sc, v4u& o, int vo, int so) {
      constexpr int k = 8 * (N.v >> 1) + 4 * (N.v & 1);
      o[2 * (N.v & 1)] = pk(a[k] * sc, a[k + 1] * sc); o[2 * (N.v & 1) + 1] = pk(a[k + 2] * sc, a[k + 3] * sc);
      if constexpr (N.v & 1) bstore_sc1(o, rs, vo, so + (N.v >> 1) * 1024); else pin(o);
    };
    v4u zo[2];
    asm volatile("s_nop 12\n\ts_waitcnt vmcnt(0)" : "+v"(aZ) : "v"(touch), "v"(touch2));
    PST(3);
    await(1);
    PST(4);
    if (heavy) {
      f32x16 aD, aT;
      v4u fo = {0u, 0u, 0u, 0u};
      {
        LAS unsigned char* da = L + P_DL + r * 1040 + h * 16;
        const v4u t0 = *(LAS v4u*)(da + ft * 64), t1_ = *(LAS v4u*)(da + ft * 64 + 32);
        v4u a[4];
#pragma unroll
        for (int i = 0; i < 4; ++i) a[i] = *(LAS v4u*)(da + i * 32);
        mfma_vv<true>(aT, t0, I[0]);
        mfma_vv<false>(aT, t1_, I[1]);
        sfor<0, 32>([&](auto KS) {
          constexpr int ks = KS.v;
          mfma_vv<ks == 0>(aD, a[ks & 3], F[ks]);
          SB();
          if constexpr (ks + 4 < 32) a[ks & 3] = *(LAS v4u*)(da + (ks + 4) * 32);
          if constexpr (ks < 18 && (ks & 1)) z_out(IC<(ks >> 1)>{}, zo);
          if constexpr (ks >= 20 && ks < 28 && (ks & 1)) f_out(IC<((ks - 20) >> 1)>{}, aT, 1.f, fo, lane * 16, OFF_W3 + (ft * 112 + 2 * tg) * 1024);
          SB();
        });
      }
      PST(5);
      await(2);
      PST(6);
      f32x16 aG0, aG1;
      const int q4 = (lane & 15) >> 2;
      LAS unsigned char* ba = L + P_GL + (8 * h + q4) * 128 + (4 * (q4 >> 1) + 2 * ((lane >> 4) & 1) + ((lane & 3) >> 1)) * 16 + 8 * (lane & 1);
      LAS unsigned char* bb = L + P_GL + (8 * h + q4) * 128 + (4 * (1 - (q4 >> 1)) + 2 * ((lane >> 4) & 1) + ((lane & 3) >> 1)) * 16 + 8 * (lane & 1);
      v4u b0[2], b1[2];
#pragma unroll
      for (int i = 0; i < 2; ++i) { b0[i] = tr2(ba + i * 2048, 512); b1[i] = tr2(bb + i * 2048, 512); }
      sfor<0, 32>([&](auto KS) {
        constexpr int ks = KS.v;
        mfma_vv<ks == 0>(aG0, F[ks], b0[ks & 1]);
        SB();
        if constexpr (ks + 2 < 32) b0[ks & 1] = tr2(ba + (ks + 2) * 2048, 512);
        if constexpr (ks >= 2 && ks < 6) f_out(IC<ks - 2>{}, aD, lsc, fo, lane * 16, OFF_W2 + (ft * 112 + 2 * tg) * 1024);
        SB();
        mfma_vv<ks == 0>(aG1, F[ks], b1[ks & 1]);
        SB();
        if constexpr (ks + 2 < 32) b1[ks & 1] = tr2(bb + (ks + 2) * 2048, 512);
        SB();
      });
      PST(7);
      asm volatile("s_nop 12" : "+v"(aG0), "+v"(aG1));
      if (ft == 0 && h == 0) { aG0[0] = 0.f; aG1[0] = 0.f; aG0[1] *= 0.70710678118654752f; aG1[1] *= 0.70710678118654752f; }
      const int g0 = 4 * cb + (r >> 4), g1 = g0 + 2, slot = ((r & 15) + 16 * up + 32 * h) * 16;
      const int o0 = ((g0 / 7) * 8 + g0 % 7) * 32768 + slot, o1 = ((g1 / 7) * 8 + g1 % 7) * 32768 + slot;
#pragma unroll
      for (int s = 0; s < 2; ++s) {
        bstore_sc1(pack8(aG0, s, 0.0625f), rs, o0, OFF_W1 + (2 * ft + s) * 1024);
        bstore_sc1(pack8(aG1, s, 0.0625f), rs, o1, OFF_W1 + (2 * ft + s) * 1024);
      }
    } else {
      sfor<0, 9>([&](auto N) { z_out(N, zo); });
#pragma unroll
      for (int i = 0; i < 2; ++i) {
        const int dt = 2 * xcd + i;
        const float b0 = __builtin_amdgcn_fractf(__builtin_fmaf(step, (float)(32 * dt), a0));
        f32x16 aI = mfma32(cosfrag(b0, step, 0), I[0], zero16());
        aI = mfma32(cosfrag(b0, step, 16), I[1], aI);
        if (ft == 0 && h == 0) { aI[0] *= 0.70710678118654752f; aI[1] *= 0.70710678118654752f; }
#pragma unroll
        for (int s = 0; s < 2; ++s) bstore_sc1(pack8(aI, s, 0.0625f), rs, lane * 16, OFF_W1 + (dt * 8 + 7) * 32768 + (2 * ft + s) * 1024);
      }
    }
  }
}

constexpr int M_Z = 0;
constexpr int M_RB = 40960;
constexpr int M_TB = 81920;
constexpr int M_S1 = 131072;
constexpr int M_S2 = M_S1 + 2048;
constexpr int M_C1 = M_S2 + 1024;
constexpr int M_C2 = M_C1 + 2 * 4608;
constexpr int M_LDS = M_C2 + 2 * 4608;
#define STAMP(i)
#define KSTAMP(i)
#define CSTAMP(i)
#define RETRY(i)
#define DELAY_A 6
#define DELAY_C 2

template <int IMM> DI void ldx4(v4u& dst, const v4u& srd, int voff, int soff) {
  asm volatile("buffer_load_dwordx4 %0, %1, %2, %3 offen offset:%4 sc1" : "=v"(dst) : "v"(voff), "s"(srd), "s"(soff), "i"(IMM));
}
struct WSrc { v4u srd, srd1; int lane16, base; };
template <int N> DI void load_w(v4u (&W)[64], const WSrc& s) {
  wload<((N >> 1) & 3) * 1024, false>(W[N], (N & 1) ? s.srd1 : s.srd, s.lane16, s.base + (N & 1) * 32768 + (N >> 3) * 4096);
}
template <int LO, int HI> DI void load_ws(v4u (&W)[64], const WSrc& s) { SB(); sfor<LO, HI>([&](auto J) { load_w<J.v>(W, s); }); SB(); }
DI unsigned mxacc(unsigned m, const v4u& v) { return umax(umax(umax(umax(m, v.x), v.y), v.z), v.w); }
DI unsigned tagacc(unsigned m, const v4u& v, unsigned tag) { return m | ((v.x ^ tag) | (v.y ^ tag)) | ((v.z ^ tag) | (v.w ^ tag)); }
DI unsigned andacc(unsigned m, const v4u& v) { return (m & v.x & v.y) & (v.z & v.w); }
DI unsigned long long bit0_set(unsigned m) { return __builtin_amdgcn_ballot_w64((m & 1u) != 0u); }
DI unsigned long long bit0_clear(unsigned m) { return __builtin_amdgcn_ballot_w64((m & 1u) == 0u); }
DI void pins(unsigned long long& k) { asm volatile("" : "+s"(k)); }
DI void pinf(float& a) { asm volatile("" : "+v"(a)); }
DI void pinu(unsigned& a) { asm volatile("" : "+v"(a)); }

struct ColConst { v4u C1[2], S1[2], N1[2], C2[2], S2[2], N2[2]; float tc[8], ts[8]; };

DI void col_consts(ColConst& k, int w, int lane, v4u (&W)[64], const WSrc& ws) {
  const int g = lane >> 4, r = lane & 15;
  const float k1 = (float)(16 * w + r);
  const unsigned neg = 0x80008000u;
  sfor<0, 2>([&](auto CS) {
    constexpr int cs = CS.v;
    const float st = k1 * (1.f / 64.f), a0 = st * (float)(8 * g);
    load_ws<3 * cs, 3 * cs + 2>(W, ws);
    k.C1[cs] = cosfrag(a0, st, 32 * cs);
    pin(k.C1[cs]);
    load_ws<3 * cs + 2, 3 * cs + 3>(W, ws);
    k.S1[cs] = cosfrag(a0 - 0.25f, st, 32 * cs);
    k.N1[cs] = k.S1[cs] ^ neg;
    pin(k.S1[cs]); pin(k.N1[cs]);
  });
  sfor<0, 2>([&](auto T) {
    constexpr int t = T.v;
    const float st = (float)(16 * t + r) * (1.f / 32.f), a0 = st * (float)(4 * g);
    float c[8], s[8];
    load_ws<6 + 3 * t, 8 + 3 * t>(W, ws);
#pragma unroll
    for (int j = 0; j < 8; ++j) c[j] = __builtin_amdgcn_cosf(__builtin_fmaf(st, (float)(16 * (j >> 2) + (j & 3)), a0));
    k.C2[t] = v4u{pk(c[0], c[1]), pk(c[2], c[3]), pk(c[4], c[5]), pk(c[6], c[7])};
    pin(k.C2[t]);
    load_ws<8 + 3 * t, 9 + 3 * t>(W, ws);
#pragma unroll
    for (int j = 0; j < 8; ++j) s[j] = __builtin_amdgcn_sinf(__builtin_fmaf(st, (float)(16 * (j >> 2) + (j & 3)), a0));
    k.S2[t] = v4u{pk(s[0], s[1]), pk(s[2], s[3]), pk(s[4], s[5]), pk(s[6], s[7])};
    k.N2[t] = k.S2[t] ^ neg;
    pin(k.S2[t]); pin(k.N2[t]);
  });
  sfor<0, 2>([&](auto T) {
    constexpr int t = T.v;
    load_ws<12 + 2 * t, 14 + 2 * t>(W, ws);
#pragma unroll
    for (int e = 4 * t; e < 4 * t + 4; ++e) {
      const float a = k1 * (1.f / 2048.f) * (float)(16 * (e >> 2) + 4 * g + (e & 3));
      k.tc[e] = __builtin_amdgcn_cosf(a); k.ts[e] = __builtin_amdgcn_sinf(a);
      pinf(k.tc[e]); pinf(k.ts[e]);
    }
  });
}

template <bool FIRST> DI void m16(f32x4& acc, const v4u& a, const v4u& b) {
  SB();
  if constexpr (FIRST) asm volatile("v_mfma_f32_16x16x32_bf16 %0, %1, %2, 0" : "=&v"(acc) : "v"(a), "v"(b));
  else asm volatile("v_mfma_f32_16x16x32_bf16 %0, %1, %2, %0" : "+v"(acc) : "v"(a), "v"(b));
  SB();
}
DI void m16_done(f32x4& a, f32x4& b) { asm volatile("s_nop 7" : "+v"(a), "+v"(b)); }
DI void m16_ready(v4u& a, v4u& b) { asm volatile("s_nop 1" : "+v"(a), "+v"(b)); }
template <int PART> DI float cmul(float a, float b, float c, float s) {
  float r = PART == 0 ? __builtin_fmaf(a, c, -(b * s)) : __builtin_fmaf(a, s, b * c);
  pinf(r);
  return r;
}

template <int WB>
DI void col_compute(LAS unsigned char* L, rsrc_t rs, const ColConst& k, const float (&fr)[8], const float (&fi)[8], v4u zr, v4u zi,
                    const float (&rstd)[8], int c, int w, int lane, int out_off, v4u (&W)[64], const WSrc& ws) {
  const int g = lane >> 4, r = lane & 15;
  const int first = c == 0;
  const float f0 = first ? 0.f : 1.f;
  float xr[8], xi[8];
#pragma unroll
  for (int i = 0; i < 4; ++i) { xr[2 * i] = blo(zr[i]); xr[2 * i + 1] = bhi(zr[i]); xi[2 * i] = blo(zi[i]); xi[2 * i + 1] = bhi(zi[i]); }
#pragma unroll
  for (int t = 0; t < 2; ++t) {
    const f32x4 rv = {rstd[4 * t], rstd[4 * t + 1], rstd[4 * t + 2], rstd[4 * t + 3]};
    const f32x4 a = f32x4{xr[4 * t], xr[4 * t + 1], xr[4 * t + 2], xr[4 * t + 3]} * rv;
    const f32x4 bq = f32x4{xi[4 * t], xi[4 * t + 1], xi[4 * t + 2], xi[4 * t + 3]} * (rv * f0);
    LAS unsigned char* d = L + M_C1 + (r + 16 * t) * 144 + (16 * w + 4 * g) * 2;
    *(LAS v2u*)d = v2u{pk(a[0], a[1]), pk(a[2], a[3])};
    *(LAS v2u*)(d + 4608) = v2u{pk(bq[0], bq[1]), pk(bq[2], bq[3])};
  }
  load_ws<WB, WB + 3>(W, ws);
  BAR();
  f32x4 dr[2], di[2];
  float ur[8], ui[8];
  {
    v4u ar[2][2], ai[2][2];
#pragma unroll
    for (int t = 0; t < 2; ++t)
#pragma unroll
      for (int cs = 0; cs < 2; ++cs) {
        LAS unsigned char* s = L + M_C1 + (r + 16 * t) * 144 + (32 * cs + 8 * g) * 2;
        ar[t][cs] = *(LAS v4u*)s; ai[t][cs] = *(LAS v4u*)(s + 4608);
      }
    load_ws<WB + 3, WB + 5>(W, ws);
    sfor<0, 16>([&](auto N) {
      constexpr int n = N.v, t = n >> 3, cs = (n >> 2) & 1, m = n & 3;
      if constexpr (m == 0) m16<cs == 0>(dr[t], ar[t][cs], k.C1[cs]);
      if constexpr (m == 1) m16<cs == 0>(di[t], ai[t][cs], k.C1[cs]);
      if constexpr (m == 2) m16<false>(dr[t], ai[t][cs], k.S1[cs]);
      if constexpr (m == 3) m16<false>(di[t], ar[t][cs], k.N1[cs]);
      if constexpr (m == 3) load_w<WB + 5 + (n >> 2)>(W, ws);
      if constexpr (n >= 9) {
        constexpr int e = (n - 9) >> 1;
        if constexpr (n & 1) ur[e] = cmul<0>(dr[0][e], di[0][e], k.tc[e], -k.ts[e]);
        else ui[e] = cmul<1>(dr[0][e], di[0][e], k.tc[e], -k.ts[e]);
      }
    });
    ui[3] = cmul<1>(dr[0][3], di[0][3], k.tc[3], -k.ts[3]);
  }
  v4u br, bi;
  br.x = pk(ur[0], ur[1]); br.y = pk(ur[2], ur[3]); bi.x = pk(ui[0], ui[1]); bi.y = pk(ui[2], ui[3]);
  SB();
  m16_done(dr[1], di[1]);
  load_ws<WB + 9, WB + 11>(W, ws);
#pragma unroll
  for (int e = 4; e < 8; ++e) {
    ur[e] = dr[1][e - 4] * k.tc[e] + di[1][e - 4] * k.ts[e]; ui[e] = di[1][e - 4] * k.tc[e] - dr[1][e - 4] * k.ts[e];
  }
  br.z = pk(ur[4], ur[5]); br.w = pk(ur[6], ur[7]); bi.z = pk(ui[4], ui[5]); bi.w = pk(ui[6], ui[7]);
  m16_ready(br, bi);
  sfor<0, 8>([&](auto N) {
    constexpr int n = N.v, t = n >> 2, m = n & 3;
    if constexpr (m == 0) m16<true>(dr[t], k.C2[t], br);
    if constexpr (m == 1) m16<true>(di[t], k.C2[t], bi);
    if constexpr (m == 2) m16<false>(dr[t], k.S2[t], bi);
    if constexpr (m == 3) m16<false>(di[t], k.N2[t], br);
    if constexpr (m == 3) load_w<WB + 11 + t>(W, ws);
    if constexpr (n == 5) ur[0] = cmul<0>(dr[0][0], di[0][0], fr[0], fi[0]);
    if constexpr (n == 6) ui[0] = cmul<1>(dr[0][0], di[0][0], fr[0], fi[0]);
  });
#pragma unroll
  for (int e = 1; e < 4; ++e) { ur[e] = dr[0][e] * fr[e] - di[0][e] * fi[e]; ui[e] = dr[0][e] * fi[e] + di[0][e] * fr[e]; }
  br.x = pk(ur[0], ur[1]); br.y = pk(ur[2], ur[3]); bi.x = pk(ui[0], ui[1]); bi.y = pk(ui[2], ui[3]);
  SB();
  m16_done(dr[1], di[1]);
  load_ws<WB + 13, WB + 15>(W, ws);
#pragma unroll
  for (int e = 4; e < 8; ++e) { ur[e] = dr[1][e - 4] * fr[e] - di[1][e - 4] * fi[e]; ui[e] = dr[1][e - 4] * fi[e] + di[1][e - 4] * fr[e]; }
  br.z = pk(ur[4], ur[5]); br.w = pk(ur[6], ur[7]); bi.z = pk(ui[4], ui[5]); bi.w = pk(ui[6], ui[7]);
  m16_ready(br, bi);
  sfor<0, 8>([&](auto N) {
    constexpr int n = N.v, t = n >> 2, m = n & 3;
    if constexpr (m == 0) m16<true>(dr[t], k.C2[t], br);
    if constexpr (m == 1) m16<true>(di[t], k.C2[t], bi);
    if constexpr (m == 2) m16<false>(dr[t], k.N2[t], bi);
    if constexpr (m == 3) m16<false>(di[t], k.S2[t], br);
    if constexpr (m == 3) load_w<WB + 15 + t>(W, ws);
    if constexpr (n == 5) ur[0] = cmul<0>(dr[0][0], di[0][0], k.tc[0], k.ts[0]);
    if constexpr (n == 6) ui[0] = cmul<1>(dr[0][0], di[0][0], k.tc[0], k.ts[0]);
  });
#pragma unroll
  for (int t = 0; t < 2; ++t) {
    if (t == 1) { SB(); m16_done(dr[1], di[1]); load_ws<WB + 17, WB + 19>(W, ws); }
#pragma unroll
    for (int i = (t == 0 ? 1 : 0); i < 4; ++i) {
      const int e = 4 * t + i;
      ur[e] = dr[t][i] * k.tc[e] - di[t][i] * k.ts[e]; ui[e] = di[t][i] * k.tc[e] + dr[t][i] * k.ts[e];
    }
    LAS unsigned char* d = L + M_C2 + (16 * w + r) * 72 + (16 * t + 4 * g) * 2;
    *(LAS v2u*)d = v2u{pk(ur[4 * t], ur[4 * t + 1]), pk(ur[4 * t + 2], ur[4 * t + 3])};
    *(LAS v2u*)(d + 4608) = v2u{pk(ui[4 * t], ui[4 * t + 1]), pk(ui[4 * t + 2], ui[4 * t + 3])};
  }
  load_ws<WB + 19, WB + 21>(W, ws);
  BAR();
  unsigned od[8];
  {
    v4u tr_[2][2], ti_[2][2];
#pragma unroll
    for (int t = 0; t < 2; ++t)
#pragma unroll
      for (int cs = 0; cs < 2; ++cs) {
        LAS unsigned char* s = L + M_C2 + (32 * cs + 8 * g + (r >> 2)) * 72 + (16 * t + 4 * (r & 3)) * 2;
        tr_[t][cs] = tr2(s, 4 * 72); ti_[t][cs] = tr2(s + 4608, 4 * 72);
      }
    load_ws<WB + 21, WB + 23>(W, ws);
    sfor<0, 16>([&](auto N) {
      constexpr int n = N.v, t = n >> 3, cs = (n >> 2) & 1, m = n & 3;
      if constexpr (m == 0) m16<cs == 0>(dr[t], k.C1[cs], tr_[t][cs]);
      if constexpr (m == 1) m16<cs == 0>(di[t], k.C1[cs], ti_[t][cs]);
      if constexpr (m == 2) m16<false>(dr[t], k.N1[cs], ti_[t][cs]);
      if constexpr (m == 3) m16<false>(di[t], k.S1[cs], tr_[t][cs]);
      if constexpr (m == 3) load_w<WB + 23 + (n >> 2)>(W, ws);
      if constexpr (n >= 9 && n < 13) {
        constexpr int e = n - 9;
        xr[e] += dr[0][e]; xi[e] = __builtin_fmaf(di[0][e], f0, xi[e]);
        pinf(xr[e]); pinf(xi[e]);
      }
      if constexpr (n == 14) { pk_tag(xr[0], xr[1], xr[2], xr[3], 1u, od[0], od[1]); pinu(od[0]); pinu(od[1]); }
    });
    pk_tag(xi[0], xi[1], xi[2], xi[3], 1u, od[4], od[5]);
    SB();
    m16_done(dr[1], di[1]);
#pragma unroll
    for (int e = 4; e < 8; ++e) { xr[e] += dr[1][e - 4]; xi[e] = __builtin_fmaf(di[1][e - 4], f0, xi[e]); }
    pk_tag(xr[4], xr[5], xr[6], xr[7], 1u, od[2], od[3]);
    pk_tag(xi[4], xi[5], xi[6], xi[7], 1u, od[6], od[7]);
  }
  const int vo = (w * 64 + lane) * 16;
  bstore_sc1(v4u{od[0], od[1], od[2], od[3]}, rs, vo, out_off + (2 * c + first) * ZS);
  bstore_sc1(v4u{od[4], od[5], od[6], od[7]}, rs, vo, out_off + (2 * c + 1 - first) * ZS);
  load_ws<WB + 27, WB + 30>(W, ws);
}

constexpr int NW_LATE = 8;
constexpr int NV_EARLY = 16;
constexpr int ZG[6] = {0, 2, 4, 8, 16, 32};
constexpr int ZR[5] = {-1, 1, 4, 10, 21};
constexpr int z_read_at(int ks) {
  for (int G = 1; G < 5; ++G) if (ks >= ZR[G] && 2 * (ks - ZR[G]) < ZG[G + 1] - ZG[G]) return ZG[G] + 2 * (ks - ZR[G]);
  return -1;
}
constexpr int z_first(int j) { for (int G = 0; G < 5; ++G) if (ZG[G] == j) return 1; return 0; }
constexpr int z_decide_at(int ks) { for (int G = 1; G < 5; ++G) if (ks == ZR[G] + (ZG[G + 1] - ZG[G]) / 2) return G; return -1; }
constexpr int z_requested(int ks) { return ks < 8 ? 9 + ks : ks < 16 ? 2 * ks + 1 : 32; }
constexpr int z_younger(int j, int ks) {
  int n = z_requested(ks) - j - 1;
  for (int k = 0; k < ks; ++k) if ((k < 8 && j < 16 && k >= j - 8) || k >= 16) ++n;
  return n;
}
struct ZSrc { v4u srd; int vo, so; unsigned lds; };
template <int J> DI void z_issue(const ZSrc& z, int so) {
  asm volatile("s_add_u32 m0, %0, %4\n\ts_nop 0\n\tbuffer_load_dwordx4 %1, %2, %3 offen sc1 lds"
               :: "s"(z.lds), "v"(z.vo), "s"(z.srd), "s"(so + (J & 15) * 32 * ZS + (J >> 4) * 128), "i"((J >> 4) * 65536 + (J & 15) * 4096)
               : "memory", "scc");
}
template <int J> DI v4u z_back(LAS unsigned char* zown) { return *(LAS v4u*)(zown + (J >> 4) * 65536 + (J & 15) * 4096); }
template <int G, int KS> DI void z_fix(unsigned long long bad, const ZSrc& z, LAS unsigned char* zown, LAS unsigned char* L, int layer, int w) {
  if (__builtin_expect(bad != 0ull, 0)) {
    unsigned spin = 0, m;
    vmwait<0>();
    do {
      RETRY(G == 0 ? 24 : 25);
      const int zo = opaque(z.so);
      sfor<ZG[G], z_requested(KS)>([&](auto I) { z_issue<I.v>(z, zo); });
      vmwait<0>();
      m = 1u;
      sfor<ZG[G], ZG[G + 1]>([&](auto I) { m = andacc(m, z_back<I.v>(zown)); });
    } while (bit0_clear(m) != 0ull && ++spin < SPIN_MAX);
  }
}

#define HA_START 14
#define HB_START 6
#define H_ROOM 16
constexpr int NSTR = 56 - NV_EARLY + 58;
struct Stream { int kind[NSTR], idx[NSTR], end[89]; };
constexpr Stream make_stream() {
  Stream s{};
  int kind[NSTR] = {}, idx[NSTR] = {}, rel[NSTR] = {}, need[NSTR] = {};
  bool done[NSTR] = {};
  int n = 0;
  for (int i = NV_EARLY; i < 56; ++i) { kind[n] = 0; idx[n] = i; rel[n] = (i - NV_EARLY) >> 1; need[n] = i >> 1; ++n; }
  for (int i = 0; i < 28; ++i) { kind[n] = 1; idx[n] = i; rel[n] = HA_START; need[n] = i; ++n; }
  for (int i = 0; i < 28; ++i) { kind[n] = 2; idx[n] = i; rel[n] = 33 + HB_START; need[n] = 28 + i; ++n; }
  for (int i = 0; i < 2; ++i) { kind[n] = 3; idx[n] = i; rel[n] = 33 + HB_START; need[n] = 60; ++n; }
  int e = 0, held = 0;
  for (int sl = 0; sl < 89; ++sl) {
    int cap = sl < 8 ? 1 : sl < 32 ? 2 - (sl % 4 == 3) : sl == 32 ? 6 : (sl - 33) % 7 == 6 ? 0 : 2;
    for (; cap > 0; --cap) {
      int best = -1;
      const int live = held - (sl < 33 ? 0 : sl - 32), room = sl < 32 ? H_ROOM : H_ROOM + 6;
      for (int i = 0; i < NSTR; ++i)
        if (!done[i] && rel[i] <= sl && (kind[i] == 0 || live < room) && (best < 0 || need[i] < need[best])) best = i;
      if (best < 0) break;
      if (kind[best] != 0) ++held;
      done[best] = true; s.kind[e] = kind[best]; s.idx[e] = idx[best]; ++e;
    }
    s.end[sl] = e;
  }
  return s;
}
constexpr Stream STR = make_stream();
static_assert(STR.end[88] == NSTR, "every load is issued");
constexpr int str_pos(int kind, int idx) { for (int i = 0; i < NSTR; ++i) if (STR.kind[i] == kind && STR.idx[i] == idx) return i; return -1; }
constexpr int str_count(int kind, int upto) { int n = 0; for (int i = 0; i < upto; ++i) if (STR.kind[i] == kind) ++n; return n; }
struct Down {
  int vbase, hbase, xvo, xso;
  DI int blk(int g) const { return g * 4096; }
};
template <int SLOT, int PART> DI void stream_issue(v4u (&W)[64], v4u (&hA)[28], v4u (&hB)[28], v4u (&x3)[2], const v4u& srd, int lane16, const Down& d) {
  constexpr int lo = SLOT == 0 ? 0 : STR.end[SLOT - 1], hi = STR.end[SLOT];
  sfor<(PART == 1 ? lo + 1 : lo), (PART == 0 && hi > lo ? lo + 1 : hi)>([&](auto I) {
    constexpr int k = STR.kind[I.v], i = STR.idx[I.v];
    if constexpr (k == 0) wload<((i >> 1) & 3) * 1024, true>(W[i - NV_EARLY], srd, lane16, d.vbase + (i & 1) * 112 * 1024 + d.blk(i >> 3));
    if constexpr (k == 1) ldx4<(i & 3) * 1024>(hA[i], srd, lane16, d.hbase + d.blk(i >> 2));
    if constexpr (k == 2) ldx4<(i & 3) * 1024>(hB[i], srd, lane16, d.hbase + 224 * 1024 + d.blk(i >> 2));
    if constexpr (k == 3) ldx4<0>(x3[i], srd, d.xvo, d.xso + i * 64 * XS);
  });
}
constexpr int dn_need(int g) {
  const int H = g / 28, i = g % 28;
  const int ph = str_pos(1 + H, i), pv = H == 0 && 2 * i >= NV_EARLY ? str_pos(0, 2 * i + 1) : 0;
  return ph > pv ? ph : pv;
}
template <int G, int ISSUED> DI void h_acc(v4u (&hA)[28], v4u (&hB)[28], unsigned& m, unsigned tag) {
  static_assert(dn_need(G) < ISSUED, "requested in time");
  vmwait<ISSUED - 1 - dn_need(G)>();
  v4u& x = G < 28 ? hA[G % 28] : hB[G % 28];
  pin(x);
  m = tagacc(m, x, tag);
  pinu(m);
}
template <int G, int ISSUED> DI void h_decide(v4u (&hA)[28], v4u (&hB)[28], unsigned long long bad, const v4u& srd, int lane16, const Down& d,
                                              LAS unsigned char* L, int layer, int w) {
  constexpr int H = G / 28, LO = G % 28;
  v4u (&hh)[28] = H == 0 ? hA : hB;
  if (__builtin_expect(bad != 0ull, 0)) {
    unsigned spin = 0, m;
    vmwait<0>();
    do {
      RETRY(26 + H);
      const int bo = opaque(d.hbase);
      sfor<LO, str_count(1 + H, ISSUED)>([&](auto I) { ldx4<(I.v & 3) * 1024>(hh[I.v], srd, lane16, bo + H * 224 * 1024 + d.blk(I.v >> 2)); });
      vmwait<0>();
      m = 0;
#pragma unroll
      for (int i = LO; i < LO + 7; ++i) { pin(hh[i]); m = tagacc(m, hh[i], layer); }
    } while (bit0_set(m) != 0ull && ++spin < SPIN_MAX);
  }
}

DI void sumsq1(float& ss, unsigned a) {
  const float x = blo(a), y = bhi(a);
  asm volatile("v_fmac_f32 %0, %1, %1\n\tv_fmac_f32 %0, %2, %2" : "+v"(ss) : "v"(x), "v"(y));
}
DI float mulf(float a, float b) { float r; asm volatile("v_mul_f32 %0, %1, %2" : "=v"(r) : "v"(a), "v"(b)); return r; }
DI void addf4(f32x4& a, const f32x4& b) {
  float x = a.x, y = a.y, z = a.z, t = a.w;
  asm volatile("v_add_f32 %0, %0, %4\n\tv_add_f32 %1, %1, %5\n\tv_add_f32 %2, %2, %6\n\tv_add_f32 %3, %3, %7"
               : "+v"(x), "+v"(y), "+v"(z), "+v"(t) : "v"(b.x), "v"(b.y), "v"(b.z), "v"(b.w));
  a = f32x4{x, y, z, t};
}
DI float relu(float a) { const int i = __builtin_bit_cast(int, a); return __builtin_bit_cast(float, i > 0 ? i : 0); }
DI void h_piece(const f32x16& a, unsigned tag, rsrc_t rs, int lane, int so) {
  unsigned d[4];
#pragma unroll
  for (int i = 0; i < 2; ++i)
    pk_tag(relu(a[4 * i]) * a[4 * i + 8], relu(a[4 * i + 1]) * a[4 * i + 9], relu(a[4 * i + 2]) * a[4 * i + 10], relu(a[4 * i + 3]) * a[4 * i + 11], tag,
           d[2 * i], d[2 * i + 1]);
  bstore_sc1(v4u{d[0], d[1], d[2], d[3]}, rs, lane * 16, so);
}
DI void x3_piece(const f32x16& a, rsrc_t rs, int lane, int so) {
  v4u o0, o1;
  widen(a, 1.f, o0, o1);
  bstore_sc1(nosent4(o0), rs, (lane & 31) * XS + (lane >> 5) * 16, so);
  bstore_sc1(nosent4(o1), rs, (lane & 31) * XS + (lane >> 5) * 16, so + 32);
}

DI void ffn(LAS unsigned char* L, rsrc_t rs, const WSrc& ws, const Params& p, v4u (&W)[64], int layer, int c, int rb, int fs, int pi, int nb,
            int w, int lane) {
  const v4u& srd = ws.srd;
  constexpr int hoff = OFF_H;
  const int h = lane >> 5, r = lane & 31, q = (lane & 15) >> 2, lane16 = lane * 16;
  ZSrc z;
  z.srd = srd;
  z.vo = (8 * w + (lane >> 3)) * ZS + (((lane & 7) ^ ((lane >> 2) & 4)) * 16);
  z.so = OFF_ZA + layer * (OFF_ZB - OFF_ZA) + rb * 256;
  z.lds = (unsigned)(size_t)L + M_Z + w * 1024;
  STAMP(4);
  sfor<0, 8>([&](auto I) { z_issue<I.v>(z, z.so); });
  SB();
  LAS unsigned char* zown = L + M_Z + w * 1024 + lane16;
  LAS unsigned char* zr[2];
#pragma unroll
  for (int s = 0; s < 2; ++s)
    zr[s] = L + M_Z + (4 * h + q) * 128 + (4 * (s ^ (q >> 1)) + 2 * ((lane >> 4) & 1) + ((lane & 3) >> 1)) * 16 + 8 * (lane & 1);
  const unsigned dcmask = (w == 0 && h == 0) ? 0xFFFF0000u : 0xFFFFFFFFu;
  const int pt = 4 * rb + pi;
  Down dn;
  dn.vbase = OFF_W2 + layer * (OFF_W3 - OFF_W2) + (nb * 2 * 112 + 28 * w) * 1024;
  dn.hbase = hoff + (pt * 112 + 28 * w) * 1024;
  dn.xvo = (lane & 31) * XS + h * 16;
  dn.xso = OFF_W1 + layer * (OFF_X3 - OFF_W1) + pt * 32 * XS + nb * 128 + w * 32;
  const int ks0 = fs * 7 + 2 * w;
  const bool two = w < 3;
  const bool idft = opaque(w + layer) == 4;
  const rsrc_t rs2 = __builtin_amdgcn_make_buffer_rsrc((void*)p.ws, 0, two ? WS_BYTES : 0, 0x00020000);
  v4u hA[28], hB[28], x3[2], res[2], Ve[NV_EARLY];
  unsigned s0, s1, s2, s3;
  asm volatile("v_mov_b32 %0, -1\n\tv_mov_b32 %1, -1\n\tv_mov_b32 %2, -1\n\tv_mov_b32 %3, -1" : "=v"(s0), "=v"(s1), "=v"(s2), "=v"(s3));
  const v4u sv = {s0, s1, s2, s3};
  const rsrc_t rsf = __builtin_amdgcn_make_buffer_rsrc((void*)p.ws, 0, layer == 0 ? WS_BYTES : 0, 0x00020000);
  const int fo_z = (w * 64 + lane) * 16 + OFF_ZB + 2 * c * ZS,
            fo_x = (w * 16 + (lane >> 2)) * XS + (lane & 3) * 16 + OFF_X3 + 128 * rb * XS + fs * 64;
  STAMP(5);
  vmwait<z_younger(1, -1)>();
  z_fix<0, -1>(bit0_clear(andacc(andacc(1u, z_back<0>(zown)), z_back<1>(zown))), z, zown, L, layer, w);
  STAMP(6);
  BAR();
  STAMP(7);
  CSTAMP(20);
  f32x16 acc[2][2][2];
  float ss[2] = {0.f, 0.f};
  sfor<0, 2>([&](auto HH) {
    constexpr int H = HH.v;
    v4u bf[2][2], sq[2], rbk[2];
    unsigned hq[4];
    float hp[4];
    unsigned zm;
    unsigned long long bad;
    LAS unsigned char* zb[2] = {zr[0] + H * 65536, zr[1] + H * 65536};
    asm volatile("" : "+v"(zb[0]), "+v"(zb[1]));
    LAS unsigned char* zq[2];
    if constexpr (H == 1) { zq[0] = (pi ? zr[1] : zr[0]) + w * 16384; zq[1] = zq[0] + 65536; asm volatile("" : "+v"(zq[0]), "+v"(zq[1])); }
#pragma unroll
    for (int s = 0; s < 2; ++s) bf[0][s] = tr2(zb[s], 1024);
    sfor<0, 32>([&](auto KS) {
      constexpr int ks = KS.v;
      constexpr bool edge = H == 0 && (ks == 3 || ks == 7 || ks == 15);
      constexpr int e = ks >> 1, es = e & 1, et = e >> 1;
      constexpr int sr = H == 1 && ks >= 8 && (ks - 8) % 3 < 2 ? 2 * ((ks - 8) / 3) + (ks - 8) % 3 : -1;
      constexpr int sn = H == 1 && ks >= 9 && (ks - 9) % 3 < 2 ? 2 * ((ks - 9) / 3) + (ks - 9) % 3 : -1;
      constexpr int rd = H == 0 ? z_read_at(ks) : -1, rp = H == 0 && ks > 0 ? z_read_at(ks - 1) : -1, dec = H == 0 ? z_decide_at(ks) : -1;
      mfma_w<ks == 0, true>(acc[H][0][0], W[2 * ks], bf[ks & 1][0]);
      SB();
      if constexpr (ks + 1 < 32 && !edge) bf[(ks + 1) & 1][0] = tr2(zb[0] + (ks + 1) * 2048, 1024);
      if constexpr (rp >= 0) { zm = andacc(z_first(rp) ? 1u : zm, rbk[0]); pinu(zm); }
      if constexpr (H == 1 && ks < 8) hp[0] = mulf(relu(acc[0][et][es][4 * (ks & 1)]), acc[0][et][es][4 * (ks & 1) + 8]);
      if constexpr (H == 1 && ks < 8) hp[1] = mulf(relu(acc[0][et][es][4 * (ks & 1) + 1]), acc[0][et][es][4 * (ks & 1) + 9]);
      if constexpr (sn >= 0) sumsq1(ss[sn & 1], sn < 2 ? sq[sn & 1].x & dcmask : sq[sn & 1].x);
      SB();
      mfma_w<ks == 0, true>(acc[H][1][0], W[2 * ks + 1], bf[ks & 1][0]);
      SB();
      if constexpr (H == 0 && ks < 16) z_issue<(ks < 8 ? 8 + ks : 2 * ks)>(z, z.so);
      if constexpr (H == 1 && ks < 8) hp[2] = mulf(relu(acc[0][et][es][4 * (ks & 1) + 2]), acc[0][et][es][4 * (ks & 1) + 10]);
      if constexpr (H == 1 && ks < 8) hp[3] = mulf(relu(acc[0][et][es][4 * (ks & 1) + 3]), acc[0][et][es][4 * (ks & 1) + 11]);
      if constexpr (sn >= 0) sumsq1(ss[sn & 1], sq[sn & 1].y);
      if constexpr (H == 1 && ks > 0) stream_issue<ks - 1, 1>(W, hA, hB, x3, srd, lane16, dn);
      if constexpr (H == 0 && ks >= 16 && (ks & 3) == 0) {
        constexpr int i = (ks - 16) / 4;
        if constexpr (i < 2) bstore_sc1(sv, rsf, fo_x, i * 64 * XS);
        else bstore_sc1(~sv, rsf, fo_z, (i - 2) * ZS);
      }
      SB();
      mfma_w<ks == 0, true>(acc[H][0][1], W[2 * ks], bf[ks & 1][1]);
      SB();
      if constexpr (ks + 1 < 32 && !edge) bf[(ks + 1) & 1][1] = tr2(zb[1] + (ks + 1) * 2048, 1024);
      if constexpr (rp >= 0) { zm = andacc(zm, rbk[1]); pinu(zm); }
      if constexpr (dec >= 0) { bad = bit0_clear(zm); pins(bad); }
      if constexpr (H == 1 && ks < 8) { pk_tag(hp[0], hp[1], hp[2], hp[3], layer, hq[2 * (ks & 1)], hq[2 * (ks & 1) + 1]); pinu(hq[2 * (ks & 1)]); pinu(hq[2 * (ks & 1) + 1]); }
      if constexpr (sn >= 0) sumsq1(ss[sn & 1], sq[sn & 1].z);
      SB();
      mfma_w<ks == 0, true>(acc[H][1][1], W[2 * ks + 1], bf[ks & 1][1]);
      SB();
      if constexpr (dec >= 0) { pins(bad); z_fix<dec, ks>(bad, z, zown, L, layer, w); }
      if constexpr (rd >= 0) { vmwait<z_younger(rd + 1, ks)>(); rbk[0] = z_back<rd>(zown); rbk[1] = z_back<rd + 1>(zown); }
      if constexpr (sn >= 0) sumsq1(ss[sn & 1], sq[sn & 1].w);
      if constexpr (sr >= 0) sq[sr & 1] = tr2(zq[sr & 1] + (sr >> 1) * 2048, 1024);
      if constexpr (H == 0 && ks < NW_LATE) load_w<64 - NW_LATE + ks>(W, ws);
      if constexpr (H == 0 && ks >= 8 && ks < 16) z_issue<2 * ks + 1>(z, z.so);
      if constexpr (H == 0 && ks >= 16) {
        constexpr int n = ks - 16;
        vload<((n >> 1) & 3) * 1024>(Ve[n], srd, lane16, dn.vbase + (n & 1) * 112 * 1024 + dn.blk(n >> 3));
      }
      if constexpr (H == 1) {
        if constexpr (ks < 8 && (ks & 1)) {
          bstore_sc1(v4u{hq[0], hq[1], hq[2], hq[3]}, et == 0 ? rs : rs2, lane16, hoff + ((4 * rb + es) * 112 + ks0 + et) * 1024);
          if constexpr (et == 1) { if (idft) x3_piece(acc[0][1][es], rs, lane, OFF_X3 + (128 * rb + 32 * es) * XS + fs * 64); }
        }
        stream_issue<ks, (ks == 31 ? 2 : 0)>(W, hA, hB, x3, srd, lane16, dn);
      }
      if constexpr (edge) {
        BAR();
#pragma unroll
        for (int s = 0; s < 2; ++s) bf[(ks + 1) & 1][s] = tr2(zb[s] + (ks + 1) * 2048, 1024);
      }
      SB();
    });
    STAMP(8 + 2 * H);
    if constexpr (H == 0) BAR();
    STAMP(9 + 2 * H);
  });
  mfma_done(acc[1][0][0], acc[1][0][1], acc[1][1][0], acc[1][1][1]);
#pragma unroll
  for (int s = 0; s < 2; ++s) h_piece(acc[1][0][s], layer, rs, lane, hoff + ((4 * rb + 2 + s) * 112 + ks0) * 1024);
  if (two) {
#pragma unroll
    for (int s = 0; s < 2; ++s) h_piece(acc[1][1][s], layer, rs, lane, hoff + ((4 * rb + 2 + s) * 112 + ks0 + 1) * 1024);
  } else if (idft) {
#pragma unroll
    for (int s = 0; s < 2; ++s) x3_piece(acc[1][1][s], rs, lane, OFF_X3 + (128 * rb + 64 + 32 * s) * XS + fs * 64);
  }
  SB();
  stream_issue<32, 2>(W, hA, hB, x3, srd, lane16, dn);
  {
    auto x = __builtin_amdgcn_permlane32_swap(asu(ss[0]), asu(ss[1]), false, false);
    *(LAS float*)(L + M_S1 + w * 256 + lane * 4) = asf(x[0]) + asf(x[1]);
  }
  const int row = 16 * w + (lane >> 2);
  {
    const int n = nb * 64 + row;
    LAS unsigned char* ra = L + M_Z + n * 128 + (((4 * pi + (lane & 3)) ^ ((n & 2) << 1)) * 16);
    res[0] = *(LAS v4u*)ra; res[1] = *(LAS v4u*)(ra + 65536);
  }
  STAMP(12);
  CSTAMP(21);
  unsigned hm = 0;
  sfor<0, 7>([&](auto G) { h_acc<G.v, STR.end[32]>(hA, hB, hm, layer); });
  h_decide<0, STR.end[32]>(hA, hB, bit0_set(hm), srd, lane16, dn, L, layer, w);
  hm = 0;
  BAR();
  STAMP(13);
  LAS unsigned char* rw = L + M_Z + w * 9216 + r * 144 + h * 16;
  LAS unsigned char* rr = L + M_Z + row * 144 + (lane & 3) * 32;
  f32x16 dac[2][2];
  f32x4 fa[2][2], tq[2];
  auto red_write = [&](auto HH, auto K) {
    constexpr int H = HH.v, t = K.v >> 2, g = K.v & 3;
    *(LAS f32x4*)(rw + H * M_RB + t * 32 * 144 + g * 32) = f32x4{dac[H][t][4 * g], dac[H][t][4 * g + 1], dac[H][t][4 * g + 2], dac[H][t][4 * g + 3]};
  };
  sfor<0, 56>([&](auto GG) {
    constexpr int g = GG.v, H = g / 28, i = g % 28;
    v4u& hx = H == 0 ? hA[i] : hB[i];
    unsigned long long bad = 0ull;
    if constexpr (2 * i < NV_EARLY) mfma_v<i == 0>(dac[H][0], Ve[2 * i], hx);
    else mfma_w<false, false>(dac[H][0], W[2 * i - NV_EARLY], hx);
    SB();
    if constexpr (g + 7 < 56) h_acc<g + 7, STR.end[32 + g]>(hA, hB, hm, layer);
    if constexpr (g + 7 < 56 && g % 7 == 6) { bad = bit0_set(hm); pins(bad); hm = 0; }
    if constexpr (g == 1) {
      LAS float* sp = (LAS float*)(L + M_S1 + lane * 4);
      *(LAS float*)(L + M_S1 + 1024 + w * 256 + lane * 4) =
          __builtin_amdgcn_rcpf(__builtin_fmaf((sp[0] + sp[64]) + (sp[128] + sp[192]), 1.f / 512.f, 1e-5f));
    }
    if constexpr (g >= 51 && g <= 53) addf4(fa[0][0], tq[0]);
    SB();
    if constexpr (2 * i < NV_EARLY) mfma_v<i == 0>(dac[H][1], Ve[2 * i + 1], hx);
    else mfma_w<false, false>(dac[H][1], W[2 * i + 1 - NV_EARLY], hx);
    SB();
    if constexpr (g + 7 < 56 && g % 7 == 6) { pins(bad); h_decide<g + 1, STR.end[32 + g]>(hA, hB, bad, srd, lane16, dn, L, layer, w); }
    stream_issue<33 + g, 2>(W, hA, hB, x3, srd, lane16, dn);
    if constexpr (g >= 30 && g <= 46 && g % 2 == 0 && g != 34) red_write(IC<0>{}, IC<(g < 34 ? (g - 30) / 2 : (g - 32) / 2)>{});
    if constexpr (g == 47) BAR();
    if constexpr (g == 49) { fa[0][0] = *(LAS f32x4*)rr; fa[0][1] = *(LAS f32x4*)(rr + 16); }
    if constexpr (g >= 51 && g <= 53) addf4(fa[0][1], tq[1]);
    if constexpr (g >= 50 && g <= 52) { tq[0] = *(LAS f32x4*)(rr + (g - 49) * 9216); tq[1] = *(LAS f32x4*)(rr + (g - 49) * 9216 + 16); }
    SB();
    if constexpr (g == 27) STAMP(14);
  });
  STAMP(15);
  asm volatile("s_nop 12" : "+v"(dac[1][0]), "+v"(dac[1][1]));
  sfor<0, 8>([&](auto K) { red_write(IC<1>{}, K); });
  vmwait<0>();
  pin(x3[0]); pin(x3[1]);
  if (__builtin_expect(layer == 1 && any_sent(mxacc(mxacc(0u, x3[0]), x3[1])), 0)) {
    unsigned spin = 0;
    do {
      RETRY(29);
      ldx4<0>(x3[0], srd, dn.xvo, dn.xso); ldx4<0>(x3[1], srd, dn.xvo, dn.xso + 64 * XS);
      vmwait<0>();
      pin(x3[0]); pin(x3[1]);
    } while (any_sent(mxacc(mxacc(0u, x3[0]), x3[1])) && ++spin < SPIN_MAX);
  }
  STAMP(16);
  BAR();
  STAMP(17);
  fa[1][0] = *(LAS f32x4*)(rr + M_RB); fa[1][1] = *(LAS f32x4*)(rr + M_RB + 16);
#pragma unroll
  for (int ww = 1; ww < 4; ++ww) { fa[1][0] += *(LAS f32x4*)(rr + M_RB + ww * 9216); fa[1][1] += *(LAS f32x4*)(rr + M_RB + ww * 9216 + 16); }
  f32x4 sc[2][2];
#pragma unroll
  for (int H = 0; H < 2; ++H)
#pragma unroll
    for (int x = 0; x < 2; ++x) sc[H][x] = *(LAS f32x4*)(L + M_S1 + 1024 + w * 256 + H * 128 + (lane & 3) * 32 + x * 16);
  if (layer == 0) {
    const bool dc = nb == 0 && row == 0;
#pragma unroll
    for (int H = 0; H < 2; ++H) {
      unsigned o[4];
      float sq[8];
#pragma unroll
      for (int x = 0; x < 2; ++x)
        pk_tag(__builtin_fmaf(fa[H][x][0], sc[H][x][0], blo(res[H][2 * x])), __builtin_fmaf(fa[H][x][1], sc[H][x][1], bhi(res[H][2 * x])),
               __builtin_fmaf(fa[H][x][2], sc[H][x][2], blo(res[H][2 * x + 1])), __builtin_fmaf(fa[H][x][3], sc[H][x][3], bhi(res[H][2 * x + 1])), 0u,
               o[2 * x], o[2 * x + 1]);
      bstore_sc1(v4u{o[0], o[1], o[2], o[3]}, rs, row * ZS + (lane & 3) * 16, OFF_ZA + nb * 64 * ZS + pt * 64 + H * 128);
#pragma unroll
      for (int j = 0; j < 4; ++j) {
        const unsigned u = dc ? 0u : o[j];
        sq[2 * j] = blo(u) * blo(u); sq[2 * j + 1] = bhi(u) * bhi(u);
      }
      float t4[4], t2[2];
#pragma unroll
      for (int j = 0; j < 4; ++j) {
        auto x = __builtin_amdgcn_permlane32_swap(asu(sq[j]), asu(sq[j + 4]), false, false);
        t4[j] = asf(x[0]) + asf(x[1]);
      }
#pragma unroll
      for (int j = 0; j < 2; ++j) {
        auto x = __builtin_amdgcn_permlane16_swap(asu(t4[j]), asu(t4[j + 2]), false, false);
        t2[j] = asf(x[0]) + asf(x[1]);
        t2[j] += dppf<0x124>(t2[j]);
        t2[j] += dppf<0x128>(t2[j]);
      }
      *(LAS f32x2*)(L + M_S2 + w * 256 + H * 128 + (8 * (lane & 3) + 4 * h + 2 * ((lane >> 4) & 1)) * 4) = f32x2{t2[0], t2[1]};
    }
    BAR();
    if (w == 0) {
      LAS float* sp = (LAS float*)(L + M_S2 + lane * 4);
      const float t = (sp[0] + sp[64]) + (sp[128] + sp[192]);
      __builtin_amdgcn_raw_buffer_store_b32(nosent(asu(t)), rs, stat_of(h * 64 + r), OFF_ST + nb * 8192 + pt * 64, SC1);
    }
  } else {
    LAS unsigned char* tb = L + M_TB + w * 2560;
#pragma unroll
    for (int H = 0; H < 2; ++H) {
      const f32x4 a = fa[H][0] * sc[H][0], bq = fa[H][1] * sc[H][1];
      *(LAS v4u*)(tb + H * 1280 + (lane >> 2) * 80 + (lane & 3) * 16) = v4u{pk(a.x, a.y), pk(a.z, a.w), pk(bq.x, bq.y), pk(bq.z, bq.w)};
    }
#pragma unroll
    for (int H = 0; H < 2; ++H) {
      const v4u y = tr2(tb + H * 1280 + (8 * h + q) * 80 + ((lane >> 4) & 1) * 32 + (lane & 3) * 8, 4 * 80);
      v4u o;
#pragma unroll
      for (int j = 0; j < 4; ++j) o[j] = pk(blo(x3[H][j]) + blo(y[j]), bhi(x3[H][j]) + bhi(y[j]));
      const int tok = token_of(32 * (pt + 2 * H) + r);
      *(v4u*)(p.out + (size_t)tok * 512 + nb * 64 + 16 * w + 8 * h) = o;
    }
  }
  SB();
}

#define XMAP 1
__global__ void __launch_bounds__(256, 1) k_main(Params p) {
  extern __shared__ __attribute__((aligned(16))) unsigned char smem[];
  LAS unsigned char* L = (LAS unsigned char*)smem;
  const rsrc_t rs = __builtin_amdgcn_make_buffer_rsrc((void*)p.ws, 0, WS_BYTES, 0x00020000);
  const v4u srd = {(unsigned)(size_t)p.ws, (unsigned)((size_t)p.ws >> 32) & 0xFFFFu, (unsigned)WS_BYTES, 0x00020000u};
  unsigned long long T0 = __builtin_amdgcn_s_memrealtime(), T1 = 0;
#pragma unroll 1
  for (int layer = 0; layer < 2; ++layer) {
    if (layer == 1) T1 = __builtin_amdgcn_s_memrealtime();
    int tid = threadIdx.x;
    asm volatile("" : "+v"(tid));
    const int c = opaque(blockIdx.x);
    const int lane = tid & 63, w = __builtin_amdgcn_readfirstlane(tid >> 6);
    const int rb = 4 * (c & 3) + (c >> 6), fs = 8 * ((c >> 2) & 1) + ((c >> 3) & 7), pi = fs >> 3, nb = fs & 7;
    WSrc ws;
    ws.srd = srd; ws.srd1 = srd;
    if (w == 3 && layer == 0) ws.srd1.z = 0u;
    ws.lane16 = lane * 16; ws.base = OFF_W1 + (fs * 8 + 2 * w) * 32768;
    v4u W[64];
    unsigned wf[8];
    const int wfvo = (16 * w + (lane & 15) + 64 * 4 * (lane >> 4)) * 4;
    STAMP(0);
    CSTAMP(22);
    v4u zr, zi, sa, sb;
    asm volatile("" : "=v"(zr), "=v"(zi), "=v"(sa), "=v"(sb));
    const int first = c == 0;
    if (layer == 0) {
      ldx4<0>(zr, srd, tid * 16, OFF_ZA + (2 * c + first) * ZS); ldx4<0>(zi, srd, tid * 16, OFF_ZA + (2 * c + 1 - first) * ZS);
      ldx4<0>(sa, srd, tid * 16, OFF_R0); ldx4<0>(sb, srd, tid * 16, OFF_R0 + 4096);
    }
    asm volatile("buffer_load_dword %0, %8, %9, %10 offen\n\tbuffer_load_dword %1, %8, %9, %10 offen offset:256\n\t"
                 "buffer_load_dword %2, %8, %9, %10 offen offset:512\n\tbuffer_load_dword %3, %8, %9, %10 offen offset:768\n\t"
                 "buffer_load_dword %4, %8, %9, %11 offen\n\tbuffer_load_dword %5, %8, %9, %11 offen offset:256\n\t"
                 "buffer_load_dword %6, %8, %9, %11 offen offset:512\n\tbuffer_load_dword %7, %8, %9, %11 offen offset:768"
                 : "=&v"(wf[0]), "=&v"(wf[1]), "=&v"(wf[2]), "=&v"(wf[3]), "=&v"(wf[4]), "=&v"(wf[5]), "=&v"(wf[6]), "=&v"(wf[7])
                 : "v"(wfvo), "s"(srd), "s"(OFF_WT + c * WS_), "s"(OFF_WT + c * WS_ + 4096));
    SB();
    ColConst k;
    col_consts(k, w, lane, W, ws);
    SB();
    vmwait<16>();
    asm volatile("" : "+v"(wf[0]), "+v"(wf[1]), "+v"(wf[2]), "+v"(wf[3]), "+v"(wf[4]), "+v"(wf[5]), "+v"(wf[6]), "+v"(wf[7]));
    pin(zr); pin(zi); pin(sa); pin(sb);
    float rstd[8], fr[8], fi[8];
#pragma unroll
    for (int e = 0; e < 8; ++e) { fr[e] = blo(wf[e]) * (1.f / 2048.f); fi[e] = bhi(wf[e]) * (1.f / 2048.f); pinf(fr[e]); pinf(fi[e]); }
    STAMP(1);
    if (layer == 1) {
      __builtin_amdgcn_s_sleep(DELAY_C);
      unsigned spin = 0;
      bool again;
      do {
        RETRY(28);
        v4u st[16];
        ldx4<0>(zr, srd, tid * 16, OFF_ZA + (2 * c + first) * ZS); ldx4<0>(zi, srd, tid * 16, OFF_ZA + (2 * c + 1 - first) * ZS);
        sfor<0, 16>([&](auto I) { ldx4<0>(st[I.v], srd, tid * 16, OFF_ST + I.v * 4096); });
        vmwait<0>();
        pin(zr); pin(zi);
#pragma unroll
        for (int i = 0; i < 16; ++i) pin(st[i]);
        f32x4 a = __builtin_bit_cast(f32x4, st[0]), bq = __builtin_bit_cast(f32x4, st[1]);
#pragma unroll
        for (int n = 1; n < 8; ++n) { a += __builtin_bit_cast(f32x4, st[2 * n]); bq += __builtin_bit_cast(f32x4, st[2 * n + 1]); }
        sa = __builtin_bit_cast(v4u, a); sb = __builtin_bit_cast(v4u, bq);
        const f32x4 t = a + bq;
        const float u = (t.x + t.y) + (t.z + t.w);
        unsigned m = 0u;
        if (__builtin_expect(__builtin_amdgcn_ballot_w64(u != u) != 0ull, 0)) {
#pragma unroll
          for (int i = 0; i < 16; ++i) m = mxacc(m, st[i]);
        }
        again = (__builtin_amdgcn_ballot_w64(m == SENT) | bit0_set(tagacc(tagacc(0u, zr, 0u), zi, 0u))) != 0ull;
      } while (__builtin_expect(again, 0) && ++spin < SPIN_MAX);
    }
    load_ws<16, 18>(W, ws);
#pragma unroll
    for (int e = 0; e < 4; ++e) {
      rstd[e] = __builtin_amdgcn_rsqf(__builtin_fmaf(asf(sa[e]), 1.f / 512.f, 1e-5f));
      rstd[4 + e] = __builtin_amdgcn_rsqf(__builtin_fmaf(asf(sb[e]), 1.f / 512.f, 1e-5f));
    }
    load_ws<18, 20>(W, ws);
    STAMP(2);
    col_compute<20>(L, rs, k, fr, fi, zr, zi, rstd, c, w, lane, OFF_ZA + layer * (OFF_ZB - OFF_ZA), W, ws);
    STAMP(3);
    CSTAMP(23);
    load_ws<50, 64 - NW_LATE>(W, ws);
    __builtin_amdgcn_s_sleep(DELAY_A);
    ffn(L, rs, ws, p, W, layer, c, rb, fs, pi, nb, w, lane);
    STAMP(18);
  }
  if (blockIdx.x == 0) {
    const unsigned long long T2 = __builtin_amdgcn_s_memrealtime(), TP = *(volatile unsigned long long*)(p.ws + 43 * MB);
    const unsigned long long d = T0 - TP;
    while (__builtin_amdgcn_s_memrealtime() - T2 < 10 * (d > 5000 ? 5000 : d)) __builtin_amdgcn_s_sleep(4);
  }
}

extern "C" void kernel_launch(void* const* d_in, const int* in_sizes, int n_in, void* d_out, int out_size, void* d_ws, size_t ws_size,
                              hipStream_t stream) {
  Params p;
  p.x = (const u16*)d_in[0]; p.wr = (const u16*)d_in[1]; p.wi = (const u16*)d_in[2];
  p.wg = (const u16*)d_in[4]; p.wu = (const u16*)d_in[5]; p.wd = (const u16*)d_in[6];
  p.out = (u16*)d_out; p.ws = (unsigned char*)d_ws;
  (void)hipFuncSetAttribute((const void*)k_prep, hipFuncAttributeMaxDynamicSharedMemorySize, P_LDS);
  (void)hipFuncSetAttribute((const void*)k_main, hipFuncAttributeMaxDynamicSharedMemorySize, M_LDS);
  k_prep<<<256, 512, P_LDS, stream>>>(p);
  k_main<<<256, 256, M_LDS, stream>>>(p);
}
```

```cpp
#include <hip/hip_runtime.h>
#include <hip/hip_bf16.h>

#define LAS __attribute__((address_space(3)))
#define DI __device__ __forceinline__
typedef unsigned short u16;
typedef unsigned v4u __attribute__((ext_vector_type(4)));
typedef unsigned v2u __attribute__((ext_vector_type(2)));
typedef float f32x2 __attribute__((ext_vector_type(2)));
typedef float f32x4 __attribute__((ext_vector_type(4)));
typedef float f32x16 __attribute__((ext_vector_type(16)));
typedef short s16x4 __attribute__((ext_vector_type(4)));
typedef __bf16 bf16x2 __attribute__((ext_vector_type(2)));
typedef __bf16 bf16x8 __attribute__((ext_vector_type(8)));
typedef __amdgpu_buffer_rsrc_t rsrc_t;

constexpr int MB = 1 << 20;
constexpr int ZS = 4224, XS = 1152, WS_ = 8320;
constexpr int OFF_ZA = 0, OFF_ZB = 9 * MB / 4;
constexpr int OFF_WT = 9 * MB;
constexpr int OFF_W1 = 12 * MB;
constexpr int OFF_W2 = 16 * MB;
constexpr int OFF_W3 = 18 * MB;
constexpr int OFF_H = 20 * MB;
constexpr int OFF_X3 = 36 * MB;
constexpr int OFF_R0 = 39 * MB;
constexpr int OFF_ST = 39 * MB + 65536;
constexpr int WS_BYTES = 40 * MB;
constexpr int SC1 = 16;
constexpr unsigned SENT = 0xFFFFFFFFu;
constexpr unsigned SPIN_MAX = 1u << 16;

struct Params {
  const u16* x; const u16* wr; const u16* wi; const u16* wg; const u16* wu; const u16* wd;
  u16* out; unsigned char* ws;
};

DI unsigned pk(float a, float b) { f32x2 v = {a, b}; return __builtin_bit_cast(unsigned, __builtin_convertvector(v, bf16x2)); }
DI float asf(unsigned u) { return __builtin_bit_cast(float, u); }
DI unsigned asu(float f) { return __builtin_bit_cast(unsigned, f); }
DI bf16x2 asb2(unsigned u) { return __builtin_bit_cast(bf16x2, u); }
template <int V> struct IC { static constexpr int v = V; };
template <int I, int N, class Fn> DI void sfor(Fn&& fn) { if constexpr (I < N) { fn(IC<I>{}); sfor<I + 1, N>(fn); } }
DI int opaque(int x) { asm volatile("" : "+s"(x)); return x; }
DI float blo(unsigned u) { return __builtin_bit_cast(float, u << 16); }
DI float bhi(unsigned u) { return __builtin_bit_cast(float, u & 0xFFFF0000u); }
DI float rbf(float a) { return blo(pk(a, a)); }
DI void pk_tag(float a0, float b0, float a1, float b1, unsigned tag, unsigned& d0, unsigned& d1) {
  const unsigned c = pk(b0, b1), up = (tag ^ 1u) << 16;
  d0 = (__builtin_amdgcn_perm(c, asu(a0) + up, 0x05040302u) & ~1u) | tag;
  d1 = (__builtin_amdgcn_perm(c, asu(a1) + up, 0x07060302u) & ~1u) | tag;
}
DI unsigned nosent(unsigned u) { return u < 0xFFFFFFFEu ? u : 0xFFFFFFFEu; }
DI v4u nosent4(v4u v) { v.x = nosent(v.x); v.y = nosent(v.y); v.z = nosent(v.z); v.w = nosent(v.w); return v; }
DI unsigned umax(unsigned a, unsigned b) { return a > b ? a : b; }
DI unsigned mx4(v4u v) { return umax(umax(v.x, v.y), umax(v.z, v.w)); }
DI bool any_sent(unsigned m) { return __builtin_amdgcn_ballot_w64(m == SENT) != 0ull; }
DI f32x16 mfma32(v4u a, v4u b, f32x16 c) {
  return __builtin_amdgcn_mfma_f32_32x32x16_bf16(__builtin_bit_cast(bf16x8, a), __builtin_bit_cast(bf16x8, b), c, 0, 0, 0);
}
DI f32x4 mfma16(v4u a, v4u b, f32x4 c) {
  return __builtin_amdgcn_mfma_f32_16x16x32_bf16(__builtin_bit_cast(bf16x8, a), __builtin_bit_cast(bf16x8, b), c, 0, 0, 0);
}
template <bool FIRST, bool WA> DI void mfma_w(f32x16& acc, const v4u& wt, const v4u& x) {
  if constexpr (FIRST && WA) asm volatile("v_mfma_f32_32x32x16_bf16 %0, %1, %2, 0" : "=&v"(acc) : "a"(wt), "v"(x));
  if constexpr (FIRST && !WA) asm volatile("v_mfma_f32_32x32x16_bf16 %0, %2, %1, 0" : "=&v"(acc) : "a"(wt), "v"(x));
  if constexpr (!FIRST && WA) asm volatile("v_mfma_f32_32x32x16_bf16 %0, %1, %2, %0" : "+v"(acc) : "a"(wt), "v"(x));
  if constexpr (!FIRST && !WA) asm volatile("v_mfma_f32_32x32x16_bf16 %0, %2, %1, %0" : "+v"(acc) : "a"(wt), "v"(x));
}
template <bool FIRST> DI void mfma_v(f32x16& acc, const v4u& wt, const v4u& x) {
  if constexpr (FIRST) asm volatile("v_mfma_f32_32x32x16_bf16 %0, %2, %1, 0" : "=&v"(acc) : "v"(wt), "v"(x));
  else asm volatile("v_mfma_f32_32x32x16_bf16 %0, %2, %1, %0" : "+v"(acc) : "v"(wt), "v"(x));
}
template <bool FIRST> DI void mfma_vv(f32x16& acc, const v4u& a, const v4u& b) {
  if constexpr (FIRST) asm volatile("v_mfma_f32_32x32x16_bf16 %0, %1, %2, 0" : "=&v"(acc) : "v"(a), "v"(b));
  else asm volatile("v_mfma_f32_32x32x16_bf16 %0, %1, %2, %0" : "+v"(acc) : "v"(a), "v"(b));
}
template <int IMM> DI void vload(v4u& dst, const v4u& srd, int lane16, int so) {
  asm volatile("buffer_load_dwordx4 %0, %1, %2, %3 offen offset:%4" : "=v"(dst) : "v"(lane16), "s"(srd), "s"(so), "i"(IMM));
}
template <int IMM, bool REUSE> DI void wload(v4u& dst, const v4u& srd, int lane16, int so) {
  if constexpr (REUSE) asm volatile("buffer_load_dwordx4 %0, %1, %2, %3 offen offset:%4" : "+a"(dst) : "v"(lane16), "s"(srd), "s"(so), "i"(IMM));
  else asm volatile("buffer_load_dwordx4 %0, %1, %2, %3 offen offset:%4" : "=a"(dst) : "v"(lane16), "s"(srd), "s"(so), "i"(IMM));
}
DI void mfma_done(f32x16& a, f32x16& b, f32x16& c, f32x16& d) { asm volatile("s_nop 12" : "+v"(a), "+v"(b), "+v"(c), "+v"(d)); }
DI v2u trread(LAS unsigned char* p) { return __builtin_bit_cast(v2u, __builtin_amdgcn_ds_read_tr16_b64_v4i16((LAS s16x4*)p)); }
DI v4u tr2(LAS unsigned char* p, int d) { v2u a = trread(p), b = trread(p + d); return v4u{a.x, a.y, b.x, b.y}; }
DI v4u bload(rsrc_t rs, int voff, int soff) { return __builtin_amdgcn_raw_buffer_load_b128(rs, voff, soff, 0); }
DI v4u bload_sc1(rsrc_t rs, int voff, int soff) { return __builtin_amdgcn_raw_buffer_load_b128(rs, voff, soff, SC1); }
DI void bstore(v4u v, rsrc_t rs, int voff, int soff) { __builtin_amdgcn_raw_buffer_store_b128(v, rs, voff + soff, 0, 0); }
DI void bstore_sc1(v4u v, rsrc_t rs, int voff, int soff) { __builtin_amdgcn_raw_buffer_store_b128(v, rs, voff + soff, 0, SC1); }
template <int I> DI v4u fload(rsrc_t rs, int lane16, int base) { return bload(rs, lane16 + (I & 3) * 1024, base + (I >> 2) * 4096); }
template <int I> DI v4u fload_sc1(rsrc_t rs, int lane16, int base) { return bload_sc1(rs, lane16 + (I & 3) * 1024, base + (I >> 2) * 4096); }
#define SB() __builtin_amdgcn_sched_barrier(0)
#define MEMFENCE() asm volatile("" ::: "memory")
#define BAR() asm volatile("s_waitcnt lgkmcnt(0)\n\ts_barrier" ::: "memory")
template <int N> DI void vmwait() { asm volatile("s_waitcnt vmcnt(%0)" :: "i"(N) : "memory"); }
DI void pin(v4u& a) { asm volatile("" : "+v"(a)); }
DI f32x16 zero16() { f32x16 z; for (int i = 0; i < 16; ++i) z[i] = 0.f; return z; }
DI v4u pack8(const f32x16& a, int s, float sc) {
  return v4u{pk(a[8 * s] * sc, a[8 * s + 1] * sc), pk(a[8 * s + 2] * sc, a[8 * s + 3] * sc),
             pk(a[8 * s + 4] * sc, a[8 * s + 5] * sc), pk(a[8 * s + 6] * sc, a[8 * s + 7] * sc)};
}
DI void widen(const f32x16& a, float sc, v4u& o0, v4u& o1) {
  unsigned d[8];
#pragma unroll
  for (int i = 0; i < 8; ++i) d[i] = pk(a[2 * i] * sc, a[2 * i + 1] * sc);
  auto r0 = __builtin_amdgcn_permlane32_swap(d[0], d[2], false, false);
  auto r1 = __builtin_amdgcn_permlane32_swap(d[1], d[3], false, false);
  auto r2 = __builtin_amdgcn_permlane32_swap(d[4], d[6], false, false);
  auto r3 = __builtin_amdgcn_permlane32_swap(d[5], d[7], false, false);
  o0 = v4u{r0[0], r1[0], r0[1], r1[1]};
  o1 = v4u{r2[0], r3[0], r2[1], r3[1]};
}
template <int CTRL> DI float dppf(float v) {
  return __builtin_bit_cast(float, __builtin_amdgcn_update_dpp(0, __builtin_bit_cast(int, v), CTRL, 0xF, 0xF, true));
}
DI float sum32(float v) {
  v += dppf<0xB1>(v); v += dppf<0x4E>(v); v += dppf<0x141>(v); v += dppf<0x140>(v);
  unsigned u = __builtin_bit_cast(unsigned, v);
  auto r = __builtin_amdgcn_permlane16_swap(u, u, false, false);
  return __builtin_bit_cast(float, (unsigned)r[0]) + __builtin_bit_cast(float, (unsigned)r[1]);
}
DI int stat_of(int p) { return ((p >> 2) & 1) * 4096 + (p >> 3) * 16 + (p & 3) * 4; }
DI int token_of(int p) {
  const int l = (p >> 3) & 63, e = p & 7;
  return 32 * (16 * (p >> 9) + 4 * (l >> 4) + (e & 3)) + (l & 15) + 16 * (e >> 2);
}
#define TOUCH_POLICY " nt"
#define GLDS_POLICY " nt"
DI void glds16(const void* sbase, unsigned voff, unsigned lds) {
  unsigned keep;
  asm volatile("s_mov_b32 %0, m0\n\ts_mov_b32 m0, %3\n\ts_nop 0\n\tglobal_load_lds_dwordx4 %1, %2" GLDS_POLICY "\n\ts_mov_b32 m0, %0"
               : "=&s"(keep) : "v"(voff), "s"(sbase), "s"(lds) : "memory");
}
template <int IMM> DI void ld2u16(unsigned& a, unsigned& b, int voff, const v4u& sa, const v4u& sb, int soff) {
  asm volatile("buffer_load_ushort %0, %2, %3, %5 offen offset:%6\n\tbuffer_load_ushort %1, %2, %4, %5 offen offset:%6"
               : "=&v"(a), "=&v"(b) : "v"(voff), "s"(sa), "s"(sb), "s"(soff), "i"(IMM));
}
DI v4u cosfrag(float a0, float step, int off) {
  float v[8];
#pragma unroll
  for (int j = 0; j < 8; ++j) v[j] = __builtin_amdgcn_cosf(__builtin_fmaf(step, (float)(off + j), a0));
  return v4u{pk(v[0], v[1]), pk(v[2], v[3]), pk(v[4], v[5]), pk(v[6], v[7])};
}

constexpr int CODE_BYTES = 40 * 1024;
constexpr int P_XL = 0, P_GL = 33280, P_DL = P_GL + 65536, P_ZB = P_DL + 33280, P_WB = P_ZB + 4 * 2560,
              P_FL = P_WB + 64 * 144, P_LDS = P_FL + 64;

__global__ void __launch_bounds__(512, 2) k_prep(Params p) {
  extern __shared__ __attribute__((aligned(16))) unsigned char smem[];
  LAS unsigned char* L = (LAS unsigned char*)smem;
  const unsigned Lb = (unsigned)(size_t)L;
  const int tid = threadIdx.x, lane = tid & 63, w8 = __builtin_amdgcn_readfirstlane(tid >> 6), w = w8 & 3;
  const int b = blockIdx.x, xcd = b & 7, q = (b >> 3) & 3, tgi = b >> 5;
  const int t1 = xcd * 8 + tgi, tg = xcd * 7 + tgi;
  const bool heavy = tgi < 7;
  const int up = tg >= 28, cb = tg - 28 * up;
#define ROTQ q
  const int h = lane >> 5, r = lane & 31, ft = 4 * q + w;
  const rsrc_t rs = __builtin_amdgcn_make_buffer_rsrc((void*)p.ws, 0, WS_BYTES, 0x00020000);
#define PST(i)
  PST(0);
  auto landed = [&](int stage) { *(volatile LAS unsigned*)(L + P_FL + stage * 16 + w * 4) = 1u; };
  auto await = [&](int stage) {
    v4u f;
    for (;;) {
      f = *(volatile LAS v4u*)(L + P_FL + stage * 16);
      if (__builtin_amdgcn_readfirstlane(f.x & f.y & f.z & f.w) != 0u) break;
      __builtin_amdgcn_s_sleep(1);
    }
    MEMFENCE();
  };
  if (__builtin_expect(w8 >= 4, 1)) {
    if (lane < 4) *(LAS unsigned*)(L + P_FL + lane * 16 + w * 4) = 0u;
    const unsigned vo = (lane >> 3) * 3584 + (((lane & 7) ^ ((lane >> 2) & 4)) * 16);
    const u16* gsrc = (up ? p.wu : p.wg) + 64 * cb;
    auto gu = [&](int i) {
      const int blk = 16 * (((i >> 1) + ROTQ) & 3) + 4 * w + 2 * (i & 1);
      glds16(gsrc + (size_t)blk * 8 * 1792, vo, Lb + P_GL + blk * 1024);
      glds16(gsrc + (size_t)(blk + 1) * 8 * 1792, vo, Lb + P_GL + (blk + 1) * 1024);
    };
#pragma unroll
    for (int i = 0; i < 8; ++i) {
      const int row = 8 * (((i >> 1) + ROTQ) & 3) + 2 * w + (i & 1);
      glds16(p.x + (size_t)token_of(32 * t1 + row) * 512, lane * 16, Lb + P_XL + row * 1040);
    }
    if (heavy) {
#pragma unroll
      for (int i = 0; i < 8; ++i) {
        const int row = 8 * (((i >> 1) + ROTQ) & 3) + 2 * w + (i & 1);
        glds16(p.wd + (size_t)(32 * tg + row) * 512, lane * 16, Lb + P_DL + row * 1040);
      }
      gu(0); gu(1);
    }
    BAR();
    if (heavy) vmwait<12>(); else vmwait<0>();
    PST(1);
    landed(0);
    if (heavy) { gu(2); gu(3); vmwait<8>(); }
    PST(2);
    landed(1);
    if (heavy) { gu(4); gu(5); gu(6); gu(7); }
    const int kk0 = 32 * t1 + 8 * w;
    unsigned fr[8], fi[8];
    {
      const v4u sr = {(unsigned)(size_t)p.wr, (unsigned)((size_t)p.wr >> 32) & 0xFFFFu, 2048u * 514u, 0x00020000u};
      const v4u si = {(unsigned)(size_t)p.wi, (unsigned)((size_t)p.wi >> 32) & 0xFFFFu, 2048u * 514u, 0x00020000u};
      const int wc = 64 * q + lane, src = (wc == 0 ? 256 : wc) * 2;
      sfor<0, 8>([&](auto I) { ld2u16<I.v * 514>(fr[I.v], fi[I.v], src, sr, si, kk0 * 514); });
    }
    PST(3);
    vmwait<16>();
    landed(2);
    PST(4);
    await(0);
    {
      const int row = 8 * q + 2 * w + h;
      const v4u u0 = *(LAS v4u*)(L + P_XL + row * 1040 + r * 32), u1 = *(LAS v4u*)(L + P_XL + row * 1040 + r * 32 + 16);
      float v[16];
#pragma unroll
      for (int i = 0; i < 4; ++i) { v[2 * i] = blo(u0[i]); v[2 * i + 1] = bhi(u0[i]); v[8 + 2 * i] = blo(u1[i]); v[9 + 2 * i] = bhi(u1[i]); }
      float s = 0.f;
#pragma unroll
      for (int i = 0; i < 16; ++i) s += v[i];
      const float m = sum32(s) * (1.f / 512.f);
      float ss = 0.f;
#pragma unroll
      for (int i = 0; i < 16; ++i) { const float d = v[i] - m; ss = __builtin_fmaf(d, d, ss); }
      ss = sum32(ss);
      if (r == 0) __builtin_amdgcn_raw_buffer_store_b32(asu(ss), rs, stat_of(32 * t1 + row), OFF_R0, 0);
    }
    {
      const v4u sv = {SENT, SENT, SENT, SENT};
      const int t16 = (w * 64 + lane) * 16;
#pragma unroll
      for (int i = 0; i < 7; ++i) bstore_sc1(sv, rs, t16, OFF_H + b * 28672 + i * 4096);
      if (t16 < 256) bstore_sc1(sv, rs, t16, OFF_ST + b * 256);
    }
    PST(5);
    asm volatile("s_waitcnt vmcnt(8)"
                 : "+v"(fr[0]), "+v"(fr[1]), "+v"(fr[2]), "+v"(fr[3]), "+v"(fr[4]), "+v"(fr[5]), "+v"(fr[6]), "+v"(fr[7]),
                   "+v"(fi[0]), "+v"(fi[1]), "+v"(fi[2]), "+v"(fi[3]), "+v"(fi[4]), "+v"(fi[5]), "+v"(fi[6]), "+v"(fi[7]) :: "memory");
    PST(6);
    {
      v4u o0, o1;
#pragma unroll
      for (int i = 0; i < 4; ++i) { o0[i] = fr[i] | (fi[i] << 16); o1[i] = fr[4 + i] | (fi[4 + i] << 16); }
      *(LAS v4u*)(L + P_WB + lane * 144 + w * 32) = o0;
      *(LAS v4u*)(L + P_WB + lane * 144 + w * 32 + 16) = o1;
    }
    asm volatile("s_waitcnt lgkmcnt(0)" ::: "memory");
    landed(3);
    await(3);
#pragma unroll
    for (int i = 0; i < 2; ++i) {
      const int col = 16 * w + 8 * i + (lane >> 3);
      bstore_sc1(*(LAS v4u*)(L + P_WB + col * 144 + (lane & 7) * 16), rs, (64 * q + col) * WS_ + (lane & 7) * 16, OFF_WT + t1 * 128);
    }
  } else {
    unsigned touch, touch2;
    {
      const char* base = (const char*)(up ? p.wu : p.wg) + (128 * q + 64 * (w & 1)) * 3584 + (heavy ? 128 * cb : 0);
      int off = lane * 3584;
      if (w >= 2) { base = (const char*)(w == 2 ? p.wr : p.wi) + (32 * t1 * 257 + 64 * q) * 2; off = r * 514; }
      asm volatile("global_load_ushort %0, %1, %2" TOUCH_POLICY : "=v"(touch) : "v"(off), "s"(base));
      unsigned lo, hi;
      asm volatile("s_getpc_b64 s[20:21]\n\ts_add_u32 %0, s20, _Z6k_prep6Params@rel32@lo+4\n\ts_addc_u32 %1, s21, _Z6k_prep6Params@rel32@hi+12"
                   : "=s"(lo), "=s"(hi) :: "s20", "s21", "scc");
      off = (w < 2 ? w : 2 + (2 * (b >> 3) + w) % 3) * 8192 + lane * 128;
      asm volatile("global_load_dword %0, %1, %2" : "=v"(touch2) : "v"(off < CODE_BYTES ? off : 0), "s"(((unsigned long long)hi << 32) | lo));
    }
    SB();
    const int feat = 32 * ft + r;
    const float step = (feat == 0 ? 0.f : feat == 1 ? 256.f : (float)(feat >> 1)) * (1.f / 512.f);
    const float ph = (feat > 1 && (feat & 1)) ? 0.25f : 0.f;
    const float a0 = ph + step * (float)(8 * h);
    const unsigned sgn = (feat > 1 && (feat & 2)) ? 0x80008000u : 0u;
    v4u F[32];
#pragma unroll
    for (int ks = 0; ks < 16; ++ks) { F[ks] = cosfrag(a0, step, 16 * ks); F[ks + 16] = F[ks] ^ sgn; }
    const float lsc = feat < 2 ? 0.04419417382415922f : 0.0625f;
#pragma unroll
    for (int ks = 0; ks < 32; ks += 8)
      asm volatile("" : "+v"(F[ks]), "+v"(F[ks + 1]), "+v"(F[ks + 2]), "+v"(F[ks + 3]), "+v"(F[ks + 4]), "+v"(F[ks + 5]), "+v"(F[ks + 6]), "+v"(F[ks + 7]));
    v4u I[2];
#pragma unroll
    for (int s = 0; s < 2; ++s) {
      const int e = r - 16 * s - 8 * h;
      const unsigned one = (e & 1) ? 0x3F800000u : 0x00003F80u;
      I[s] = v4u{(e >> 1) == 0 ? one : 0u, (e >> 1) == 1 ? one : 0u, (e >> 1) == 2 ? one : 0u, (e >> 1) == 3 ? one : 0u};
    }
    pin(I[0]); pin(I[1]);
    PST(1);
    BAR();
    await(0);
    PST(2);
    f32x16 aZ;
    {
      LAS unsigned char* xa = L + P_XL + r * 1040 + h * 16;
      v4u a[4];
#pragma unroll
      for (int i = 0; i < 4; ++i) a[i] = *(LAS v4u*)(xa + i * 32);
      sfor<0, 32>([&](auto KS) {
        constexpr int ks = KS.v;
        mfma_vv<ks == 0>(aZ, a[ks & 3], F[ks]);
        SB();
        if constexpr (ks + 4 < 32) a[ks & 3] = *(LAS v4u*)(xa + (ks + 4) * 32);
        SB();
      });
    }
    unsigned zd[8];
    auto z_out = [&](auto STEP, v4u (&o)[2]) {
      constexpr int n = STEP.v;
      LAS unsigned char* zb = L + P_ZB + w * 2560;
      if constexpr (n < 4) {
        pk_tag(aZ[4 * n] * lsc, aZ[4 * n + 1] * lsc, aZ[4 * n + 2] * lsc, aZ[4 * n + 3] * lsc, 0u, zd[2 * n], zd[2 * n + 1]);
        asm volatile("" : "+v"(zd[2 * n]), "+v"(zd[2 * n + 1]));
      }
      if constexpr (n == 4 || n == 5) {
        auto r0 = __builtin_amdgcn_permlane32_swap(zd[4 * (n - 4)], zd[4 * (n - 4) + 2], false, false);
        auto r1 = __builtin_amdgcn_permlane32_swap(zd[4 * (n - 4) + 1], zd[4 * (n - 4) + 3], false, false);
        *(LAS v4u*)(zb + r * 80 + h * 16 + 32 * (n - 4)) = v4u{r0[0], r1[0], r0[1], r1[1]};
      }
      if constexpr (n == 6) { o[0] = *(LAS v4u*)(zb + (lane >> 2) * 80 + (lane & 3) * 16); o[1] = *(LAS v4u*)(zb + (16 + (lane >> 2)) * 80 + (lane & 3) * 16); }
      if constexpr (n == 7 || n == 8) bstore_sc1(o[n - 7], rs, (32 * ft + 16 * (n - 7) + (lane >> 2)) * ZS + (lane & 3) * 16, OFF_ZA + t1 * 64);
    };
    auto f_out = [&](auto N, const f32x16& a, float sc, v4u& o, int vo, int so) {
      constexpr int k = 8 * (N.v >> 1) + 4 * (N.v & 1);
      o[2 * (N.v & 1)] = pk(a[k] * sc, a[k + 1] * sc); o[2 * (N.v & 1) + 1] = pk(a[k + 2] * sc, a[k + 3] * sc);
      if constexpr (N.v & 1) bstore_sc1(o, rs, vo, so + (N.v >> 1) * 1024); else pin(o);
    };
    v4u zo[2];
    asm volatile("s_nop 12\n\ts_waitcnt vmcnt(0)" : "+v"(aZ) : "v"(touch), "v"(touch2));
    PST(3);
    await(1);
    PST(4);
    if (heavy) {
      f32x16 aD, aT;
      v4u fo = {0u, 0u, 0u, 0u};
      {
        LAS unsigned char* da = L + P_DL + r * 1040 + h * 16;
        const v4u t0 = *(LAS v4u*)(da + ft * 64), t1_ = *(LAS v4u*)(da + ft * 64 + 32);
        v4u a[4];
#pragma unroll
        for (int i = 0; i < 4; ++i) a[i] = *(LAS v4u*)(da + i * 32);
        mfma_vv<true>(aT, t0, I[0]);
        mfma_vv<false>(aT, t1_, I[1]);
        sfor<0, 32>([&](auto KS) {
          constexpr int ks = KS.v;
          mfma_vv<ks == 0>(aD, a[ks & 3], F[ks]);
          SB();
          if constexpr (ks + 4 < 32) a[ks & 3] = *(LAS v4u*)(da + (ks + 4) * 32);
          if constexpr (ks < 18 && (ks & 1)) z_out(IC<(ks >> 1)>{}, zo);
          if constexpr (ks >= 20 && ks < 28 && (ks & 1)) f_out(IC<((ks - 20) >> 1)>{}, aT, 1.f, fo, lane * 16, OFF_W3 + (ft * 112 + 2 * tg) * 1024);
          SB();
        });
      }
      PST(5);
      await(2);
      PST(6);
      f32x16 aG0, aG1;
      const int q4 = (lane & 15) >> 2;
      LAS unsigned char* ba = L + P_GL + (8 * h + q4) * 128 + (4 * (q4 >> 1) + 2 * ((lane >> 4) & 1) + ((lane & 3) >> 1)) * 16 + 8 * (lane & 1);
      LAS unsigned char* bb = L + P_GL + (8 * h + q4) * 128 + (4 * (1 - (q4 >> 1)) + 2 * ((lane >> 4) & 1) + ((lane & 3) >> 1)) * 16 + 8 * (lane & 1);
      v4u b0[2], b1[2];
#pragma unroll
      for (int i = 0; i < 2; ++i) { b0[i] = tr2(ba + i * 2048, 512); b1[i] = tr2(bb + i * 2048, 512); }
      sfor<0, 32>([&](auto KS) {
        constexpr int ks = KS.v;
        mfma_vv<ks == 0>(aG0, F[ks], b0[ks & 1]);
        SB();
        if constexpr (ks + 2 < 32) b0[ks & 1] = tr2(ba + (ks + 2) * 2048, 512);
        if constexpr (ks >= 2 && ks < 6) f_out(IC<ks - 2>{}, aD, lsc, fo, lane * 16, OFF_W2 + (ft * 112 + 2 * tg) * 1024);
        SB();
        mfma_vv<ks == 0>(aG1, F[ks], b1[ks & 1]);
        SB();
        if constexpr (ks + 2 < 32) b1[ks & 1] = tr2(bb + (ks + 2) * 2048, 512);
        SB();
      });
      PST(7);
      asm volatile("s_nop 12" : "+v"(aG0), "+v"(aG1));
      if (ft == 0 && h == 0) { aG0[0] = 0.f; aG1[0] = 0.f; aG0[1] *= 0.70710678118654752f; aG1[1] *= 0.70710678118654752f; }
      const int g0 = 4 * cb + (r >> 4), g1 = g0 + 2, slot = ((r & 15) + 16 * up + 32 * h) * 16;
      const int o0 = ((g0 / 7) * 8 + g0 % 7) * 32768 + slot, o1 = ((g1 / 7) * 8 + g1 % 7) * 32768 + slot;
#pragma unroll
      for (int s = 0; s < 2; ++s) {
        bstore_sc1(pack8(aG0, s, 0.0625f), rs, o0, OFF_W1 + (2 * ft + s) * 1024);
        bstore_sc1(pack8(aG1, s, 0.0625f), rs, o1, OFF_W1 + (2 * ft + s) * 1024);
      }
    } else {
      sfor<0, 9>([&](auto N) { z_out(N, zo); });
#pragma unroll
      for (int i = 0; i < 2; ++i) {
        const int dt = 2 * xcd + i;
        const float b0 = __builtin_amdgcn_fractf(__builtin_fmaf(step, (float)(32 * dt), a0));
        f32x16 aI = mfma32(cosfrag(b0, step, 0), I[0], zero16());
        aI = mfma32(cosfrag(b0, step, 16), I[1], aI);
        if (ft == 0 && h == 0) { aI[0] *= 0.70710678118654752f; aI[1] *= 0.70710678118654752f; }
#pragma unroll
        for (int s = 0; s < 2; ++s) bstore_sc1(pack8(aI, s, 0.0625f), rs, lane * 16, OFF_W1 + (dt * 8 + 7) * 32768 + (2 * ft + s) * 1024);
      }
    }
  }
}

constexpr int M_Z = 0;
constexpr int M_RB = 40960;
constexpr int M_TB = 81920;
constexpr int M_S1 = 131072;
constexpr int M_S2 = M_S1 + 2048;
constexpr int M_C1 = M_S2 + 1024;
constexpr int M_C2 = M_C1 + 2 * 4608;
constexpr int M_LDS = M_C2 + 2 * 4608;
#define STAMP(i)
#define KSTAMP(i)
#define CSTAMP(i)
#define RETRY(i)
#define DELAY_A 6
#define DELAY_C 2

template <int IMM> DI void ldx4(v4u& dst, const v4u& srd, int voff, int soff) {
  asm volatile("buffer_load_dwordx4 %0, %1, %2, %3 offen offset:%4 sc1" : "=v"(dst) : "v"(voff), "s"(srd), "s"(soff), "i"(IMM));
}
struct WSrc { v4u srd, srd1; int lane16, base; };
template <int N> DI void load_w(v4u (&W)[64], const WSrc& s) {
  wload<((N >> 1) & 3) * 1024, false>(W[N], (N & 1) ? s.srd1 : s.srd, s.lane16, s.base + (N & 1) * 32768 + (N >> 3) * 4096);
}
template <int LO, int HI> DI void load_ws(v4u (&W)[64], const WSrc& s) { SB(); sfor<LO, HI>([&](auto J) { load_w<J.v>(W, s); }); SB(); }
DI unsigned mxacc(unsigned m, const v4u& v) { return umax(umax(umax(umax(m, v.x), v.y), v.z), v.w); }
DI unsigned tagacc(unsigned m, const v4u& v, unsigned tag) { return m | ((v.x ^ tag) | (v.y ^ tag)) | ((v.z ^ tag) | (v.w ^ tag)); }
DI unsigned andacc(unsigned m, const v4u& v) { return (m & v.x & v.y) & (v.z & v.w); }
DI unsigned long long bit0_set(unsigned m) { return __builtin_amdgcn_ballot_w64((m & 1u) != 0u); }
DI unsigned long long bit0_clear(unsigned m) { return __builtin_amdgcn_ballot_w64((m & 1u) == 0u); }
DI void pins(unsigned long long& k) { asm volatile("" : "+s"(k)); }
DI void pinf(float& a) { asm volatile("" : "+v"(a)); }
DI void pinu(unsigned& a) { asm volatile("" : "+v"(a)); }

struct ColConst { v4u C1[2], S1[2], N1[2], C2[2], S2[2], N2[2]; float tc[8], ts[8]; };

DI void col_consts(ColConst& k, int w, int lane, v4u (&W)[64], const WSrc& ws) {
  const int g = lane >> 4, r = lane & 15;
  const float k1 = (float)(16 * w + r);
  const unsigned neg = 0x80008000u;
  sfor<0, 2>([&](auto CS) {
    constexpr int cs = CS.v;
    const float st = k1 * (1.f / 64.f), a0 = st * (float)(8 * g);
    load_ws<3 * cs, 3 * cs + 2>(W, ws);
    k.C1[cs] = cosfrag(a0, st, 32 * cs);
    pin(k.C1[cs]);
    load_ws<3 * cs + 2, 3 * cs + 3>(W, ws);
    k.S1[cs] = cosfrag(a0 - 0.25f, st, 32 * cs);
    k.N1[cs] = k.S1[cs] ^ neg;
    pin(k.S1[cs]); pin(k.N1[cs]);
  });
  sfor<0, 2>([&](auto T) {
    constexpr int t = T.v;
    const float st = (float)(16 * t + r) * (1.f / 32.f), a0 = st * (float)(4 * g);
    float c[8], s[8];
    load_ws<6 + 3 * t, 8 + 3 * t>(W, ws);
#pragma unroll
    for (int j = 0; j < 8; ++j) c[j] = __builtin_amdgcn_cosf(__builtin_fmaf(st, (float)(16 * (j >> 2) + (j & 3)), a0));
    k.C2[t] = v4u{pk(c[0], c[1]), pk(c[2], c[3]), pk(c[4], c[5]), pk(c[6], c[7])};
    pin(k.C2[t]);
    load_ws<8 + 3 * t, 9 + 3 * t>(W, ws);
#pragma unroll
    for (int j = 0; j < 8; ++j) s[j] = __builtin_amdgcn_sinf(__builtin_fmaf(st, (float)(16 * (j >> 2) + (j & 3)), a0));
    k.S2[t] = v4u{pk(s[0], s[1]), pk(s[2], s[3]), pk(s[4], s[5]), pk(s[6], s[7])};
    k.N2[t] = k.S2[t] ^ neg;
    pin(k.S2[t]); pin(k.N2[t]);
  });
  sfor<0, 2>([&](auto T) {
    constexpr int t = T.v;
    load_ws<12 + 2 * t, 14 + 2 * t>(W, ws);
#pragma unroll
    for (int e = 4 * t; e < 4 * t + 4; ++e) {
      const float a = k1 * (1.f / 2048.f) * (float)(16 * (e >> 2) + 4 * g + (e & 3));
      k.tc[e] = __builtin_amdgcn_cosf(a); k.ts[e] = __builtin_amdgcn_sinf(a);
      pinf(k.tc[e]); pinf(k.ts[e]);
    }
  });
}

template <bool FIRST> DI void m16(f32x4& acc, const v4u& a, const v4u& b) {
  SB();
  if constexpr (FIRST) asm volatile("v_mfma_f32_16x16x32_bf16 %0, %1, %2, 0" : "=&v"(acc) : "v"(a), "v"(b));
  else asm volatile("v_mfma_f32_16x16x32_bf16 %0, %1, %2, %0" : "+v"(acc) : "v"(a), "v"(b));
  SB();
}
DI void m16_done(f32x4& a, f32x4& b) { asm volatile("s_nop 7" : "+v"(a), "+v"(b)); }
DI void m16_ready(v4u& a, v4u& b) { asm volatile("s_nop 1" : "+v"(a), "+v"(b)); }
template <int PART> DI float cmul(float a, float b, float c, float s) {
  float r = PART == 0 ? __builtin_fmaf(a, c, -(b * s)) : __builtin_fmaf(a, s, b * c);
  pinf(r);
  return r;
}

template <int WB>
DI void col_compute(LAS unsigned char* L, rsrc_t rs, const ColConst& k, const float (&fr)[8], const float (&fi)[8], v4u zr, v4u zi,
                    const float (&rstd)[8], int c, int w, int lane, int out_off, v4u (&W)[64], const WSrc& ws) {
  const int g = lane >> 4, r = lane & 15;
  const int first = c == 0;
  const float f0 = first ? 0.f : 1.f;
  float xr[8], xi[8];
#pragma unroll
  for (int i = 0; i < 4; ++i) { xr[2 * i] = blo(zr[i]); xr[2 * i + 1] = bhi(zr[i]); xi[2 * i] = blo(zi[i]); xi[2 * i + 1] = bhi(zi[i]); }
#pragma unroll
  for (int t = 0; t < 2; ++t) {
    const f32x4 rv = {rstd[4 * t], rstd[4 * t + 1], rstd[4 * t + 2], rstd[4 * t + 3]};
    const f32x4 a = f32x4{xr[4 * t], xr[4 * t + 1], xr[4 * t + 2], xr[4 * t + 3]} * rv;
    const f32x4 bq = f32x4{xi[4 * t], xi[4 * t + 1], xi[4 * t + 2], xi[4 * t + 3]} * (rv * f0);
    LAS unsigned char* d = L + M_C1 + (r + 16 * t) * 144 + (16 * w + 4 * g) * 2;
    *(LAS v2u*)d = v2u{pk(a[0], a[1]), pk(a[2], a[3])};
    *(LAS v2u*)(d + 4608) = v2u{pk(bq[0], bq[1]), pk(bq[2], bq[3])};
  }
  load_ws<WB, WB + 3>(W, ws);
  BAR();
  f32x4 dr[2], di[2];
  float ur[8], ui[8];
  {
    v4u ar[2][2], ai[2][2];
#pragma unroll
    for (int t = 0; t < 2; ++t)
#pragma unroll
      for (int cs = 0; cs < 2; ++cs) {
        LAS unsigned char* s = L + M_C1 + (r + 16 * t) * 144 + (32 * cs + 8 * g) * 2;
        ar[t][cs] = *(LAS v4u*)s; ai[t][cs] = *(LAS v4u*)(s + 4608);
      }
    load_ws<WB + 3, WB + 5>(W, ws);
    sfor<0, 16>([&](auto N) {
      constexpr int n = N.v, t = n >> 3, cs = (n >> 2) & 1, m = n & 3;
      if constexpr (m == 0) m16<cs == 0>(dr[t], ar[t][cs], k.C1[cs]);
      if constexpr (m == 1) m16<cs == 0>(di[t], ai[t][cs], k.C1[cs]);
      if constexpr (m == 2) m16<false>(dr[t], ai[t][cs], k.S1[cs]);
      if constexpr (m == 3) m16<false>(di[t], ar[t][cs], k.N1[cs]);
      if constexpr (m == 3) load_w<WB + 5 + (n >> 2)>(W, ws);
      if constexpr (n >= 9) {
        constexpr int e = (n - 9) >> 1;
        if constexpr (n & 1) ur[e] = cmul<0>(dr[0][e], di[0][e], k.tc[e], -k.ts[e]);
        else ui[e] = cmul<1>(dr[0][e], di[0][e], k.tc[e], -k.ts[e]);
      }
    });
    ui[3] = cmul<1>(dr[0][3], di[0][3], k.tc[3], -k.ts[3]);
  }
  v4u br, bi;
  br.x = pk(ur[0], ur[1]); br.y = pk(ur[2], ur[3]); bi.x = pk(ui[0], ui[1]); bi.y = pk(ui[2], ui[3]);
  SB();
  m16_done(dr[1], di[1]);
  load_ws<WB + 9, WB + 11>(W, ws);
#pragma unroll
  for (int e = 4; e < 8; ++e) {
    ur[e] = dr[1][e - 4] * k.tc[e] + di[1][e - 4] * k.ts[e]; ui[e] = di[1][e - 4] * k.tc[e] - dr[1][e - 4] * k.ts[e];
  }
  br.z = pk(ur[4], ur[5]); br.w = pk(ur[6], ur[7]); bi.z = pk(ui[4], ui[5]); bi.w = pk(ui[6], ui[7]);
  m16_ready(br, bi);
  sfor<0, 8>([&](auto N) {
    constexpr int n = N.v, t = n >> 2, m = n & 3;
    if constexpr (m == 0) m16<true>(dr[t], k.C2[t], br);
    if constexpr (m == 1) m16<true>(di[t], k.C2[t], bi);
    if constexpr (m == 2) m16<false>(dr[t], k.S2[t], bi);
    if constexpr (m == 3) m16<false>(di[t], k.N2[t], br);
    if constexpr (m == 3) load_w<WB + 11 + t>(W, ws);
    if constexpr (n == 5) ur[0] = cmul<0>(dr[0][0], di[0][0], fr[0], fi[0]);
    if constexpr (n == 6) ui[0] = cmul<1>(dr[0][0], di[0][0], fr[0], fi[0]);
  });
#pragma unroll
  for (int e = 1; e < 4; ++e) { ur[e] = dr[0][e] * fr[e] - di[0][e] * fi[e]; ui[e] = dr[0][e] * fi[e] + di[0][e] * fr[e]; }
  br.x = pk(ur[0], ur[1]); br.y = pk(ur[2], ur[3]); bi.x = pk(ui[0], ui[1]); bi.y = pk(ui[2], ui[3]);
  SB();
  m16_done(dr[1], di[1]);
  load_ws<WB + 13, WB + 15>(W, ws);
#pragma unroll
  for (int e = 4; e < 8; ++e) { ur[e] = dr[1][e - 4] * fr[e] - di[1][e - 4] * fi[e]; ui[e] = dr[1][e - 4] * fi[e] + di[1][e - 4] * fr[e]; }
  br.z = pk(ur[4], ur[5]); br.w = pk(ur[6], ur[7]); bi.z = pk(ui[4], ui[5]); bi.w = pk(ui[6], ui[7]);
  m16_ready(br, bi);
  sfor<0, 8>([&](auto N) {
    constexpr int n = N.v, t = n >> 2, m = n & 3;
    if constexpr (m == 0) m16<true>(dr[t], k.C2[t], br);
    if constexpr (m == 1) m16<true>(di[t], k.C2[t], bi);
    if constexpr (m == 2) m16<false>(dr[t], k.N2[t], bi);
    if constexpr (m == 3) m16<false>(di[t], k.S2[t], br);
    if constexpr (m == 3) load_w<WB + 15 + t>(W, ws);
    if constexpr (n == 5) ur[0] = cmul<0>(dr[0][0], di[0][0], k.tc[0], k.ts[0]);
    if constexpr (n == 6) ui[0] = cmul<1>(dr[0][0], di[0][0], k.tc[0], k.ts[0]);
  });
#pragma unroll
  for (int t = 0; t < 2; ++t) {
    if (t == 1) { SB(); m16_done(dr[1], di[1]); load_ws<WB + 17, WB + 19>(W, ws); }
#pragma unroll
    for (int i = (t == 0 ? 1 : 0); i < 4; ++i) {
      const int e = 4 * t + i;
      ur[e] = dr[t][i] * k.tc[e] - di[t][i] * k.ts[e]; ui[e] = di[t][i] * k.tc[e] + dr[t][i] * k.ts[e];
    }
    LAS unsigned char* d = L + M_C2 + (16 * w + r) * 72 + (16 * t + 4 * g) * 2;
    *(LAS v2u*)d = v2u{pk(ur[4 * t], ur[4 * t + 1]), pk(ur[4 * t + 2], ur[4 * t + 3])};
    *(LAS v2u*)(d + 4608) = v2u{pk(ui[4 * t], ui[4 * t + 1]), pk(ui[4 * t + 2], ui[4 * t + 3])};
  }
  load_ws<WB + 19, WB + 21>(W, ws);
  BAR();
  unsigned od[8];
  {
    v4u tr_[2][2], ti_[2][2];
#pragma unroll
    for (int t = 0; t < 2; ++t)
#pragma unroll
      for (int cs = 0; cs < 2; ++cs) {
        LAS unsigned char* s = L + M_C2 + (32 * cs + 8 * g + (r >> 2)) * 72 + (16 * t + 4 * (r & 3)) * 2;
        tr_[t][cs] = tr2(s, 4 * 72); ti_[t][cs] = tr2(s + 4608, 4 * 72);
      }
    load_ws<WB + 21, WB + 23>(W, ws);
    sfor<0, 16>([&](auto N) {
      constexpr int n = N.v, t = n >> 3, cs = (n >> 2) & 1, m = n & 3;
      if constexpr (m == 0) m16<cs == 0>(dr[t], k.C1[cs], tr_[t][cs]);
      if constexpr (m == 1) m16<cs == 0>(di[t], k.C1[cs], ti_[t][cs]);
      if constexpr (m == 2) m16<false>(dr[t], k.N1[cs], ti_[t][cs]);
      if constexpr (m == 3) m16<false>(di[t], k.S1[cs], tr_[t][cs]);
      if constexpr (m == 3) load_w<WB + 23 + (n >> 2)>(W, ws);
      if constexpr (n >= 9 && n < 13) {
        constexpr int e = n - 9;
        xr[e] += dr[0][e]; xi[e] = __builtin_fmaf(di[0][e], f0, xi[e]);
        pinf(xr[e]); pinf(xi[e]);
      }
      if constexpr (n == 14) { pk_tag(xr[0], xr[1], xr[2], xr[3], 1u, od[0], od[1]); pinu(od[0]); pinu(od[1]); }
    });
    pk_tag(xi[0], xi[1], xi[2], xi[3], 1u, od[4], od[5]);
    SB();
    m16_done(dr[1], di[1]);
#pragma unroll
    for (int e = 4; e < 8; ++e) { xr[e] += dr[1][e - 4]; xi[e] = __builtin_fmaf(di[1][e - 4], f0, xi[e]); }
    pk_tag(xr[4], xr[5], xr[6], xr[7], 1u, od[2], od[3]);
    pk_tag(xi[4], xi[5], xi[6], xi[7], 1u, od[6], od[7]);
  }
  const int vo = (w * 64 + lane) * 16;
  bstore_sc1(v4u{od[0], od[1], od[2], od[3]}, rs, vo, out_off + (2 * c + first) * ZS);
  bstore_sc1(v4u{od[4], od[5], od[6], od[7]}, rs, vo, out_off + (2 * c + 1 - first) * ZS);
  load_ws<WB + 27, WB + 30>(W, ws);
}

constexpr int NW_LATE = 8;
constexpr int NV_EARLY = 16;
constexpr int ZG[6] = {0, 2, 4, 8, 16, 32};
constexpr int ZR[5] = {-1, 1, 4, 10, 21};
constexpr int z_read_at(int ks) {
  for (int G = 1; G < 5; ++G) if (ks >= ZR[G] && 2 * (ks - ZR[G]) < ZG[G + 1] - ZG[G]) return ZG[G] + 2 * (ks - ZR[G]);
  return -1;
}
constexpr int z_first(int j) { for (int G = 0; G < 5; ++G) if (ZG[G] == j) return 1; return 0; }
constexpr int z_decide_at(int ks) { for (int G = 1; G < 5; ++G) if (ks == ZR[G] + (ZG[G + 1] - ZG[G]) / 2) return G; return -1; }
constexpr int z_requested(int ks) { return ks < 8 ? 9 + ks : ks < 16 ? 2 * ks + 1 : 32; }
constexpr int z_younger(int j, int ks) {
  int n = z_requested(ks) - j - 1;
  for (int k = 0; k < ks; ++k) if ((k < 8 && j < 16 && k >= j - 8) || k >= 16) ++n;
  return n;
}
struct ZSrc { v4u srd; int vo, so; unsigned lds; };
template <int J> DI void z_issue(const ZSrc& z, int so) {
  asm volatile("s_add_u32 m0, %0, %4\n\ts_nop 0\n\tbuffer_load_dwordx4 %1, %2, %3 offen sc1 lds"
               :: "s"(z.lds), "v"(z.vo), "s"(z.srd), "s"(so + (J & 15) * 32 * ZS + (J >> 4) * 128), "i"((J >> 4) * 65536 + (J & 15) * 4096)
               : "memory", "scc");
}
template <int J> DI v4u z_back(LAS unsigned char* zown) { return *(LAS v4u*)(zown + (J >> 4) * 65536 + (J & 15) * 4096); }
template <int G, int KS> DI void z_fix(unsigned long long bad, const ZSrc& z, LAS unsigned char* zown, LAS unsigned char* L, int layer, int w) {
  if (__builtin_expect(bad != 0ull, 0)) {
    unsigned spin = 0, m;
    vmwait<0>();
    do {
      RETRY(G == 0 ? 24 : 25);
      const int zo = opaque(z.so);
      sfor<ZG[G], z_requested(KS)>([&](auto I) { z_issue<I.v>(z, zo); });
      vmwait<0>();
      m = 1u;
      sfor<ZG[G], ZG[G + 1]>([&](auto I) { m = andacc(m, z_back<I.v>(zown)); });
    } while (bit0_clear(m) != 0ull && ++spin < SPIN_MAX);
  }
}

#define HA_START 14
#define HB_START 6
#define H_ROOM 16
constexpr int NSTR = 56 - NV_EARLY + 58;
struct Stream { int kind[NSTR], idx[NSTR], end[89]; };
constexpr Stream make_stream() {
  Stream s{};
  int kind[NSTR] = {}, idx[NSTR] = {}, rel[NSTR] = {}, need[NSTR] = {};
  bool done[NSTR] = {};
  int n = 0;
  for (int i = NV_EARLY; i < 56; ++i) { kind[n] = 0; idx[n] = i; rel[n] = (i - NV_EARLY) >> 1; need[n] = i >> 1; ++n; }
  for (int i = 0; i < 28; ++i) { kind[n] = 1; idx[n] = i; rel[n] = HA_START; need[n] = i; ++n; }
  for (int i = 0; i < 28; ++i) { kind[n] = 2; idx[n] = i; rel[n] = 33 + HB_START; need[n] = 28 + i; ++n; }
  for (int i = 0; i < 2; ++i) { kind[n] = 3; idx[n] = i; rel[n] = 33 + HB_START; need[n] = 60; ++n; }
  int e = 0, held = 0;
  for (int sl = 0; sl < 89; ++sl) {
    int cap = sl < 8 ? 1 : sl < 32 ? 2 - (sl % 4 == 3) : sl == 32 ? 6 : (sl - 33) % 7 == 6 ? 0 : 2;
    for (; cap > 0; --cap) {
      int best = -1;
      const int live = held - (sl < 33 ? 0 : sl - 32), room = sl < 32 ? H_ROOM : H_ROOM + 6;
      for (int i = 0; i < NSTR; ++i)
        if (!done[i] && rel[i] <= sl && (kind[i] == 0 || live < room) && (best < 0 || need[i] < need[best])) best = i;
      if (best < 0) break;
      if (kind[best] != 0) ++held;
      done[best] = true; s.kind[e] = kind[best]; s.idx[e] = idx[best]; ++e;
    }
    s.end[sl] = e;
  }
  return s;
}
constexpr Stream STR = make_stream();
static_assert(STR.end[88] == NSTR, "every load is issued");
constexpr int str_pos(int kind, int idx) { for (int i = 0; i < NSTR; ++i) if (STR.kind[i] == kind && STR.idx[i] == idx) return i; return -1; }
constexpr int str_count(int kind, int upto) { int n = 0; for (int i = 0; i < upto; ++i) if (STR.kind[i] == kind) ++n; return n; }
struct Down {
  int vbase, hbase, xvo, xso;
  DI int blk(int g) const { return g * 4096; }
};
template <int SLOT, int PART> DI void stream_issue(v4u (&W)[64], v4u (&hA)[28], v4u (&hB)[28], v4u (&x3)[2], const v4u& srd, int lane16, const Down& d) {
  constexpr int lo = SLOT == 0 ? 0 : STR.end[SLOT - 1], hi = STR.end[SLOT];
  sfor<(PART == 1 ? lo + 1 : lo), (PART == 0 && hi > lo ? lo + 1 : hi)>([&](auto I) {
    constexpr int k = STR.kind[I.v], i = STR.idx[I.v];
    if constexpr (k == 0) wload<((i >> 1) & 3) * 1024, true>(W[i - NV_EARLY], srd, lane16, d.vbase + (i & 1) * 112 * 1024 + d.blk(i >> 3));
    if constexpr (k == 1) ldx4<(i & 3) * 1024>(hA[i], srd, lane16, d.hbase + d.blk(i >> 2));
    if constexpr (k == 2) ldx4<(i & 3) * 1024>(hB[i], srd, lane16, d.hbase + 224 * 1024 + d.blk(i >> 2));
    if constexpr (k == 3) ldx4<0>(x3[i], srd, d.xvo, d.xso + i * 64 * XS);
  });
}
constexpr int dn_need(int g) {
  const int H = g / 28, i = g % 28;
  const int ph = str_pos(1 + H, i), pv = H == 0 && 2 * i >= NV_EARLY ? str_pos(0, 2 * i + 1) : 0;
  return ph > pv ? ph : pv;
}
template <int G, int ISSUED> DI void h_acc(v4u (&hA)[28], v4u (&hB)[28], unsigned& m, unsigned tag) {
  static_assert(dn_need(G) < ISSUED, "requested in time");
  vmwait<ISSUED - 1 - dn_need(G)>();
  v4u& x = G < 28 ? hA[G % 28] : hB[G % 28];
  pin(x);
  m = tagacc(m, x, tag);
  pinu(m);
}
template <int G, int ISSUED> DI void h_decide(v4u (&hA)[28], v4u (&hB)[28], unsigned long long bad, const v4u& srd, int lane16, const Down& d,
                                              LAS unsigned char* L, int layer, int w) {
  constexpr int H = G / 28, LO = G % 28;
  v4u (&hh)[28] = H == 0 ? hA : hB;
  if (__builtin_expect(bad != 0ull, 0)) {
    unsigned spin = 0, m;
    vmwait<0>();
    do {
      RETRY(26 + H);
      const int bo = opaque(d.hbase);
      sfor<LO, str_count(1 + H, ISSUED)>([&](auto I) { ldx4<(I.v & 3) * 1024>(hh[I.v], srd, lane16, bo + H * 224 * 1024 + d.blk(I.v >> 2)); });
      vmwait<0>();
      m = 0;
#pragma unroll
      for (int i = LO; i < LO + 7; ++i) { pin(hh[i]); m = tagacc(m, hh[i], layer); }
    } while (bit0_set(m) != 0ull && ++spin < SPIN_MAX);
  }
}

DI void sumsq1(float& ss, unsigned a) {
  const float x = blo(a), y = bhi(a);
  asm volatile("v_fmac_f32 %0, %1, %1\n\tv_fmac_f32 %0, %2, %2" : "+v"(ss) : "v"(x), "v"(y));
}
DI float mulf(float a, float b) { float r; asm volatile("v_mul_f32 %0, %1, %2" : "=v"(r) : "v"(a), "v"(b)); return r; }
DI void addf4(f32x4& a, const f32x4& b) {
  float x = a.x, y = a.y, z = a.z, t = a.w;
  asm volatile("v_add_f32 %0, %0, %4\n\tv_add_f32 %1, %1, %5\n\tv_add_f32 %2, %2, %6\n\tv_add_f32 %3, %3, %7"
               : "+v"(x), "+v"(y), "+v"(z), "+v"(t) : "v"(b.x), "v"(b.y), "v"(b.z), "v"(b.w));
  a = f32x4{x, y, z, t};
}
DI float relu(float a) { const int i = __builtin_bit_cast(int, a); return __builtin_bit_cast(float, i > 0 ? i : 0); }
DI void h_piece(const f32x16& a, unsigned tag, rsrc_t rs, int lane, int so) {
  unsigned d[4];
#pragma unroll
  for (int i = 0; i < 2; ++i)
    pk_tag(relu(a[4 * i]) * a[4 * i + 8], relu(a[4 * i + 1]) * a[4 * i + 9], relu(a[4 * i + 2]) * a[4 * i + 10], relu(a[4 * i + 3]) * a[4 * i + 11], tag,
           d[2 * i], d[2 * i + 1]);
  bstore_sc1(v4u{d[0], d[1], d[2], d[3]}, rs, lane * 16, so);
}
DI void x3_piece(const f32x16& a, rsrc_t rs, int lane, int so) {
  v4u o0, o1;
  widen(a, 1.f, o0, o1);
  bstore_sc1(nosent4(o0), rs, (lane & 31) * XS + (lane >> 5) * 16, so);
  bstore_sc1(nosent4(o1), rs, (lane & 31) * XS + (lane >> 5) * 16, so + 32);
}

DI void ffn(LAS unsigned char* L, rsrc_t rs, const WSrc& ws, const Params& p, v4u (&W)[64], int layer, int c, int rb, int fs, int pi, int nb,
            int w, int lane) {
  const v4u& srd = ws.srd;
  constexpr int hoff = OFF_H;
  const int h = lane >> 5, r = lane & 31, q = (lane & 15) >> 2, lane16 = lane * 16;
  ZSrc z;
  z.srd = srd;
  z.vo = (8 * w + (lane >> 3)) * ZS + (((lane & 7) ^ ((lane >> 2) & 4)) * 16);
  z.so = OFF_ZA + layer * (OFF_ZB - OFF_ZA) + rb * 256;
  z.lds = (unsigned)(size_t)L + M_Z + w * 1024;
  STAMP(4);
  sfor<0, 8>([&](auto I) { z_issue<I.v>(z, z.so); });
  SB();
  LAS unsigned char* zown = L + M_Z + w * 1024 + lane16;
  LAS unsigned char* zr[2];
#pragma unroll
  for (int s = 0; s < 2; ++s)
    zr[s] = L + M_Z + (4 * h + q) * 128 + (4 * (s ^ (q >> 1)) + 2 * ((lane >> 4) & 1) + ((lane & 3) >> 1)) * 16 + 8 * (lane & 1);
  const unsigned dcmask = (w == 0 && h == 0) ? 0xFFFF0000u : 0xFFFFFFFFu;
  const int pt = 4 * rb + pi;
  Down dn;
  dn.vbase = OFF_W2 + layer * (OFF_W3 - OFF_W2) + (nb * 2 * 112 + 28 * w) * 1024;
  dn.hbase = hoff + (pt * 112 + 28 * w) * 1024;
  dn.xvo = (lane & 31) * XS + h * 16;
  dn.xso = OFF_W1 + layer * (OFF_X3 - OFF_W1) + pt * 32 * XS + nb * 128 + w * 32;
  const int ks0 = fs * 7 + 2 * w;
  const bool two = w < 3;
  const bool idft = opaque(w + layer) == 4;
  const rsrc_t rs2 = __builtin_amdgcn_make_buffer_rsrc((void*)p.ws, 0, two ? WS_BYTES : 0, 0x00020000);
  v4u hA[28], hB[28], x3[2], res[2], Ve[NV_EARLY];
  unsigned s0, s1, s2, s3;
  asm volatile("v_mov_b32 %0, -1\n\tv_mov_b32 %1, -1\n\tv_mov_b32 %2, -1\n\tv_mov_b32 %3, -1" : "=v"(s0), "=v"(s1), "=v"(s2), "=v"(s3));
  const v4u sv = {s0, s1, s2, s3};
  const rsrc_t rsf = __builtin_amdgcn_make_buffer_rsrc((void*)p.ws, 0, layer == 0 ? WS_BYTES : 0, 0x00020000);
  const int fo_z = (w * 64 + lane) * 16 + OFF_ZB + 2 * c * ZS,
            fo_x = (w * 16 + (lane >> 2)) * XS + (lane & 3) * 16 + OFF_X3 + 128 * rb * XS + fs * 64;
  STAMP(5);
  vmwait<z_younger(1, -1)>();
  z_fix<0, -1>(bit0_clear(andacc(andacc(1u, z_back<0>(zown)), z_back<1>(zown))), z, zown, L, layer, w);
  STAMP(6);
  BAR();
  STAMP(7);
  CSTAMP(20);
  f32x16 acc[2][2][2];
  float ss[2] = {0.f, 0.f};
  sfor<0, 2>([&](auto HH) {
    constexpr int H = HH.v;
    v4u bf[2][2], sq[2], rbk[2];
    unsigned hq[4];
    float hp[4];
    unsigned zm;
    unsigned long long bad;
    LAS unsigned char* zb[2] = {zr[0] + H * 65536, zr[1] + H * 65536};
    asm volatile("" : "+v"(zb[0]), "+v"(zb[1]));
    LAS unsigned char* zq[2];
    if constexpr (H == 1) { zq[0] = (pi ? zr[1] : zr[0]) + w * 16384; zq[1] = zq[0] + 65536; asm volatile("" : "+v"(zq[0]), "+v"(zq[1])); }
#pragma unroll
    for (int s = 0; s < 2; ++s) bf[0][s] = tr2(zb[s], 1024);
    sfor<0, 32>([&](auto KS) {
      constexpr int ks = KS.v;
      constexpr bool edge = H == 0 && (ks == 3 || ks == 7 || ks == 15);
      constexpr int e = ks >> 1, es = e & 1, et = e >> 1;
      constexpr int sr = H == 1 && ks >= 8 && (ks - 8) % 3 < 2 ? 2 * ((ks - 8) / 3) + (ks - 8) % 3 : -1;
      constexpr int sn = H == 1 && ks >= 9 && (ks - 9) % 3 < 2 ? 2 * ((ks - 9) / 3) + (ks - 9) % 3 : -1;
      constexpr int rd = H == 0 ? z_read_at(ks) : -1, rp = H == 0 && ks > 0 ? z_read_at(ks - 1) : -1, dec = H == 0 ? z_decide_at(ks) : -1;
      mfma_w<ks == 0, true>(acc[H][0][0], W[2 * ks], bf[ks & 1][0]);
      SB();
      if constexpr (ks + 1 < 32 && !edge) bf[(ks + 1) & 1][0] = tr2(zb[0] + (ks + 1) * 2048, 1024);
      if constexpr (rp >= 0) { zm = andacc(z_first(rp) ? 1u : zm, rbk[0]); pinu(zm); }
      if constexpr (H == 1 && ks < 8) hp[0] = mulf(relu(acc[0][et][es][4 * (ks & 1)]), acc[0][et][es][4 * (ks & 1) + 8]);
      if constexpr (H == 1 && ks < 8) hp[1] = mulf(relu(acc[0][et][es][4 * (ks & 1) + 1]), acc[0][et][es][4 * (ks & 1) + 9]);
      if constexpr (sn >= 0) sumsq1(ss[sn & 1], sn < 2 ? sq[sn & 1].x & dcmask : sq[sn & 1].x);
      SB();
      mfma_w<ks == 0, true>(acc[H][1][0], W[2 * ks + 1], bf[ks & 1][0]);
      SB();
      if constexpr (H == 0 && ks < 16) z_issue<(ks < 8 ? 8 + ks : 2 * ks)>(z, z.so);
      if constexpr (H == 1 && ks < 8) hp[2] = mulf(relu(acc[0][et][es][4 * (ks & 1) + 2]), acc[0][et][es][4 * (ks & 1) + 10]);
      if constexpr (H == 1 && ks < 8) hp[3] = mulf(relu(acc[0][et][es][4 * (ks & 1) + 3]), acc[0][et][es][4 * (ks & 1) + 11]);
      if constexpr (sn >= 0) sumsq1(ss[sn & 1], sq[sn & 1].y);
      if constexpr (H == 1 && ks > 0) stream_issue<ks - 1, 1>(W, hA, hB, x3, srd, lane16, dn);
      if constexpr (H == 0 && ks >= 16 && (ks & 3) == 0) {
        constexpr int i = (ks - 16) / 4;
        if constexpr (i < 2) bstore_sc1(sv, rsf, fo_x, i * 64 * XS);
        else bstore_sc1(~sv, rsf, fo_z, (i - 2) * ZS);
      }
      SB();
      mfma_w<ks == 0, true>(acc[H][0][1], W[2 * ks], bf[ks & 1][1]);
      SB();
      if constexpr (ks + 1 < 32 && !edge) bf[(ks + 1) & 1][1] = tr2(zb[1] + (ks + 1) * 2048, 1024);
      if constexpr (rp >= 0) { zm = andacc(zm, rbk[1]); pinu(zm); }
      if constexpr (dec >= 0) { bad = bit0_clear(zm); pins(bad); }
      if constexpr (H == 1 && ks < 8) { pk_tag(hp[0], hp[1], hp[2], hp[3], layer, hq[2 * (ks & 1)], hq[2 * (ks & 1) + 1]); pinu(hq[2 * (ks & 1)]); pinu(hq[2 * (ks & 1) + 1]); }
      if constexpr (sn >= 0) sumsq1(ss[sn & 1], sq[sn & 1].z);
      SB();
      mfma_w<ks == 0, true>(acc[H][1][1], W[2 * ks + 1], bf[ks & 1][1]);
      SB();
      if constexpr (dec >= 0) { pins(bad); z_fix<dec, ks>(bad, z, zown, L, layer, w); }
      if constexpr (rd >= 0) { vmwait<z_younger(rd + 1, ks)>(); rbk[0] = z_back<rd>(zown); rbk[1] = z_back<rd + 1>(zown); }
      if constexpr (sn >= 0) sumsq1(ss[sn & 1], sq[sn & 1].w);
      if constexpr (sr >= 0) sq[sr & 1] = tr2(zq[sr & 1] + (sr >> 1) * 2048, 1024);
      if constexpr (H == 0 && ks < NW_LATE) load_w<64 - NW_LATE + ks>(W, ws);
      if constexpr (H == 0 && ks >= 8 && ks < 16) z_issue<2 * ks + 1>(z, z.so);
      if constexpr (H == 0 && ks >= 16) {
        constexpr int n = ks - 16;
        vload<((n >> 1) & 3) * 1024>(Ve[n], srd, lane16, dn.vbase + (n & 1) * 112 * 1024 + dn.blk(n >> 3));
      }
      if constexpr (H == 1) {
        if constexpr (ks < 8 && (ks & 1)) {
          bstore_sc1(v4u{hq[0], hq[1], hq[2], hq[3]}, et == 0 ? rs : rs2, lane16, hoff + ((4 * rb + es) * 112 + ks0 + et) * 1024);
          if constexpr (et == 1) { if (idft) x3_piece(acc[0][1][es], rs, lane, OFF_X3 + (128 * rb + 32 * es) * XS + fs * 64); }
        }
        stream_issue<ks, (ks == 31 ? 2 : 0)>(W, hA, hB, x3, srd, lane16, dn);
      }
      if constexpr (edge) {
        BAR();
#pragma unroll
        for (int s = 0; s < 2; ++s) bf[(ks + 1) & 1][s] = tr2(zb[s] + (ks + 1) * 2048, 1024);
      }
      SB();
    });
    STAMP(8 + 2 * H);
    if constexpr (H == 0) BAR();
    STAMP(9 + 2 * H);
  });
  mfma_done(acc[1][0][0], acc[1][0][1], acc[1][1][0], acc[1][1][1]);
#pragma unroll
  for (int s = 0; s < 2; ++s) h_piece(acc[1][0][s], layer, rs, lane, hoff + ((4 * rb + 2 + s) * 112 + ks0) * 1024);
  if (two) {
#pragma unroll
    for (int s = 0; s < 2; ++s) h_piece(acc[1][1][s], layer, rs, lane, hoff + ((4 * rb + 2 + s) * 112 + ks0 + 1) * 1024);
  } else if (idft) {
#pragma unroll
    for (int s = 0; s < 2; ++s) x3_piece(acc[1][1][s], rs, lane, OFF_X3 + (128 * rb + 64 + 32 * s) * XS + fs * 64);
  }
  SB();
  stream_issue<32, 2>(W, hA, hB, x3, srd, lane16, dn);
  {
    auto x = __builtin_amdgcn_permlane32_swap(asu(ss[0]), asu(ss[1]), false, false);
    *(LAS float*)(L + M_S1 + w * 256 + lane * 4) = asf(x[0]) + asf(x[1]);
  }
  const int row = 16 * w + (lane >> 2);
  {
    const int n = nb * 64 + row;
    LAS unsigned char* ra = L + M_Z + n * 128 + (((4 * pi + (lane & 3)) ^ ((n & 2) << 1)) * 16);
    res[0] = *(LAS v4u*)ra; res[1] = *(LAS v4u*)(ra + 65536);
  }
  STAMP(12);
  CSTAMP(21);
  unsigned hm = 0;
  sfor<0, 7>([&](auto G) { h_acc<G.v, STR.end[32]>(hA, hB, hm, layer); });
  h_decide<0, STR.end[32]>(hA, hB, bit0_set(hm), srd, lane16, dn, L, layer, w);
  hm = 0;
  BAR();
  STAMP(13);
  LAS unsigned char* rw = L + M_Z + w * 9216 + r * 144 + h * 16;
  LAS unsigned char* rr = L + M_Z + row * 144 + (lane & 3) * 32;
  f32x16 dac[2][2];
  f32x4 fa[2][2], tq[2];
  auto red_write = [&](auto HH, auto K) {
    constexpr int H = HH.v, t = K.v >> 2, g = K.v & 3;
    *(LAS f32x4*)(rw + H * M_RB + t * 32 * 144 + g * 32) = f32x4{dac[H][t][4 * g], dac[H][t][4 * g + 1], dac[H][t][4 * g + 2], dac[H][t][4 * g + 3]};
  };
  sfor<0, 56>([&](auto GG) {
    constexpr int g = GG.v, H = g / 28, i = g % 28;
    v4u& hx = H == 0 ? hA[i] : hB[i];
    unsigned long long bad = 0ull;
    if constexpr (2 * i < NV_EARLY) mfma_v<i == 0>(dac[H][0], Ve[2 * i], hx);
    else mfma_w<false, false>(dac[H][0], W[2 * i - NV_EARLY], hx);
    SB();
    if constexpr (g + 7 < 56) h_acc<g + 7, STR.end[32 + g]>(hA, hB, hm, layer);
    if constexpr (g + 7 < 56 && g % 7 == 6) { bad = bit0_set(hm); pins(bad); hm = 0; }
    if constexpr (g == 1) {
      LAS float* sp = (LAS float*)(L + M_S1 + lane * 4);
      *(LAS float*)(L + M_S1 + 1024 + w * 256 + lane * 4) =
          __builtin_amdgcn_rcpf(__builtin_fmaf((sp[0] + sp[64]) + (sp[128] + sp[192]), 1.f / 512.f, 1e-5f));
    }
    if constexpr (g >= 51 && g <= 53) addf4(fa[0][0], tq[0]);
    SB();
    if constexpr (2 * i < NV_EARLY) mfma_v<i == 0>(dac[H][1], Ve[2 * i + 1], hx);
    else mfma_w<false, false>(dac[H][1], W[2 * i + 1 - NV_EARLY], hx);
    SB();
    if constexpr (g + 7 < 56 && g % 7 == 6) { pins(bad); h_decide<g + 1, STR.end[32 + g]>(hA, hB, bad, srd, lane16, dn, L, layer, w); }
    stream_issue<33 + g, 2>(W, hA, hB, x3, srd, lane16, dn);
    if constexpr (g >= 30 && g <= 46 && g % 2 == 0 && g != 34) red_write(IC<0>{}, IC<(g < 34 ? (g - 30) / 2 : (g - 32) / 2)>{});
    if constexpr (g == 47) BAR();
    if constexpr (g == 49) { fa[0][0] = *(LAS f32x4*)rr; fa[0][1] = *(LAS f32x4*)(rr + 16); }
    if constexpr (g >= 51 && g <= 53) addf4(fa[0][1], tq[1]);
    if constexpr (g >= 50 && g <= 52) { tq[0] = *(LAS f32x4*)(rr + (g - 49) * 9216); tq[1] = *(LAS f32x4*)(rr + (g - 49) * 9216 + 16); }
    SB();
    if constexpr (g == 27) STAMP(14);
  });
  STAMP(15);
  asm volatile("s_nop 12" : "+v"(dac[1][0]), "+v"(dac[1][1]));
  sfor<0, 8>([&](auto K) { red_write(IC<1>{}, K); });
  vmwait<0>();
  pin(x3[0]); pin(x3[1]);
  if (__builtin_expect(layer == 1 && any_sent(mxacc(mxacc(0u, x3[0]), x3[1])), 0)) {
    unsigned spin = 0;
    do {
      RETRY(29);
      ldx4<0>(x3[0], srd, dn.xvo, dn.xso); ldx4<0>(x3[1], srd, dn.xvo, dn.xso + 64 * XS);
      vmwait<0>();
      pin(x3[0]); pin(x3[1]);
    } while (any_sent(mxacc(mxacc(0u, x3[0]), x3[1])) && ++spin < SPIN_MAX);
  }
  STAMP(16);
  BAR();
  STAMP(17);
  fa[1][0] = *(LAS f32x4*)(rr + M_RB); fa[1][1] = *(LAS f32x4*)(rr + M_RB + 16);
#pragma unroll
  for (int ww = 1; ww < 4; ++ww) { fa[1][0] += *(LAS f32x4*)(rr + M_RB + ww * 9216); fa[1][1] += *(LAS f32x4*)(rr + M_RB + ww * 9216 + 16); }
  f32x4 sc[2][2];
#pragma unroll
  for (int H = 0; H < 2; ++H)
#pragma unroll
    for (int x = 0; x < 2; ++x) sc[H][x] = *(LAS f32x4*)(L + M_S1 + 1024 + w * 256 + H * 128 + (lane & 3) * 32 + x * 16);
  if (layer == 0) {
    const bool dc = nb == 0 && row == 0;
#pragma unroll
    for (int H = 0; H < 2; ++H) {
      unsigned o[4];
      float sq[8];
#pragma unroll
      for (int x = 0; x < 2; ++x)
        pk_tag(__builtin_fmaf(fa[H][x][0], sc[H][x][0], blo(res[H][2 * x])), __builtin_fmaf(fa[H][x][1], sc[H][x][1], bhi(res[H][2 * x])),
               __builtin_fmaf(fa[H][x][2], sc[H][x][2], blo(res[H][2 * x + 1])), __builtin_fmaf(fa[H][x][3], sc[H][x][3], bhi(res[H][2 * x + 1])), 0u,
               o[2 * x], o[2 * x + 1]);
      bstore_sc1(v4u{o[0], o[1], o[2], o[3]}, rs, row * ZS + (lane & 3) * 16, OFF_ZA + nb * 64 * ZS + pt * 64 + H * 128);
#pragma unroll
      for (int j = 0; j < 4; ++j) {
        const unsigned u = dc ? 0u : o[j];
        sq[2 * j] = blo(u) * blo(u); sq[2 * j + 1] = bhi(u) * bhi(u);
      }
      float t4[4], t2[2];
#pragma unroll
      for (int j = 0; j < 4; ++j) {
        auto x = __builtin_amdgcn_permlane32_swap(asu(sq[j]), asu(sq[j + 4]), false, false);
        t4[j] = asf(x[0]) + asf(x[1]);
      }
#pragma unroll
      for (int j = 0; j < 2; ++j) {
        auto x = __builtin_amdgcn_permlane16_swap(asu(t4[j]), asu(t4[j + 2]), false, false);
        t2[j] = asf(x[0]) + asf(x[1]);
        t2[j] += dppf<0x124>(t2[j]);
        t2[j] += dppf<0x128>(t2[j]);
      }
      *(LAS f32x2*)(L + M_S2 + w * 256 + H * 128 + (8 * (lane & 3) + 4 * h + 2 * ((lane >> 4) & 1)) * 4) = f32x2{t2[0], t2[1]};
    }
    BAR();
    if (w == 0) {
      LAS float* sp = (LAS float*)(L + M_S2 + lane * 4);
      const float t = (sp[0] + sp[64]) + (sp[128] + sp[192]);
      __builtin_amdgcn_raw_buffer_store_b32(nosent(asu(t)), rs, stat_of(h * 64 + r), OFF_ST + nb * 8192 + pt * 64, SC1);
    }
  } else {
    LAS unsigned char* tb = L + M_TB + w * 2560;
#pragma unroll
    for (int H = 0; H < 2; ++H) {
      const f32x4 a = fa[H][0] * sc[H][0], bq = fa[H][1] * sc[H][1];
      *(LAS v4u*)(tb + H * 1280 + (lane >> 2) * 80 + (lane & 3) * 16) = v4u{pk(a.x, a.y), pk(a.z, a.w), pk(bq.x, bq.y), pk(bq.z, bq.w)};
    }
#pragma unroll
    for (int H = 0; H < 2; ++H) {
      const v4u y = tr2(tb + H * 1280 + (8 * h + q) * 80 + ((lane >> 4) & 1) * 32 + (lane & 3) * 8, 4 * 80);
      v4u o;
#pragma unroll
      for (int j = 0; j < 4; ++j) o[j] = pk(blo(x3[H][j]) + blo(y[j]), bhi(x3[H][j]) + bhi(y[j]));
      const int tok = token_of(32 * (pt + 2 * H) + r);
      __builtin_amdgcn_raw_buffer_store_b128(o, __builtin_amdgcn_make_buffer_rsrc((void*)p.out, 0, 2048 * 1024, 0x00020000), tok * 1024 + nb * 128 + 32 * w + 16 * h, 0, SC1);
    }
  }
  SB();
}

#define XMAP 1
__global__ void __launch_bounds__(256, 1) k_main(Params p) {
  extern __shared__ __attribute__((aligned(16))) unsigned char smem[];
  LAS unsigned char* L = (LAS unsigned char*)smem;
  const rsrc_t rs = __builtin_amdgcn_make_buffer_rsrc((void*)p.ws, 0, WS_BYTES, 0x00020000);
  const v4u srd = {(unsigned)(size_t)p.ws, (unsigned)((size_t)p.ws >> 32) & 0xFFFFu, (unsigned)WS_BYTES, 0x00020000u};
#pragma unroll 1
  for (int layer = 0; layer < 2; ++layer) {
    int tid = threadIdx.x;
    asm volatile("" : "+v"(tid));
    const int c = opaque(blockIdx.x);
    const int lane = tid & 63, w = __builtin_amdgcn_readfirstlane(tid >> 6);
    const int rb = 4 * (c & 3) + (c >> 6), fs = 8 * ((c >> 2) & 1) + ((c >> 3) & 7), pi = fs >> 3, nb = fs & 7;
    WSrc ws;
    ws.srd = srd; ws.srd1 = srd;
    if (w == 3 && layer == 0) ws.srd1.z = 0u;
    ws.lane16 = lane * 16; ws.base = OFF_W1 + (fs * 8 + 2 * w) * 32768;
    v4u W[64];
    unsigned wf[8];
    const int wfvo = (16 * w + (lane & 15) + 64 * 4 * (lane >> 4)) * 4;
    STAMP(0);
    CSTAMP(22);
    v4u zr, zi, sa, sb;
    asm volatile("" : "=v"(zr), "=v"(zi), "=v"(sa), "=v"(sb));
    const int first = c == 0;
    if (layer == 0) {
      ldx4<0>(zr, srd, tid * 16, OFF_ZA + (2 * c + first) * ZS); ldx4<0>(zi, srd, tid * 16, OFF_ZA + (2 * c + 1 - first) * ZS);
      ldx4<0>(sa, srd, tid * 16, OFF_R0); ldx4<0>(sb, srd, tid * 16, OFF_R0 + 4096);
    }
    asm volatile("buffer_load_dword %0, %8, %9, %10 offen\n\tbuffer_load_dword %1, %8, %9, %10 offen offset:256\n\t"
                 "buffer_load_dword %2, %8, %9, %10 offen offset:512\n\tbuffer_load_dword %3, %8, %9, %10 offen offset:768\n\t"
                 "buffer_load_dword %4, %8, %9, %11 offen\n\tbuffer_load_dword %5, %8, %9, %11 offen offset:256\n\t"
                 "buffer_load_dword %6, %8, %9, %11 offen offset:512\n\tbuffer_load_dword %7, %8, %9, %11 offen offset:768"
                 : "=&v"(wf[0]), "=&v"(wf[1]), "=&v"(wf[2]), "=&v"(wf[3]), "=&v"(wf[4]), "=&v"(wf[5]), "=&v"(wf[6]), "=&v"(wf[7])
                 : "v"(wfvo), "s"(srd), "s"(OFF_WT + c * WS_), "s"(OFF_WT + c * WS_ + 4096));
    SB();
    ColConst k;
    col_consts(k, w, lane, W, ws);
    SB();
    vmwait<16>();
    asm volatile("" : "+v"(wf[0]), "+v"(wf[1]), "+v"(wf[2]), "+v"(wf[3]), "+v"(wf[4]), "+v"(wf[5]), "+v"(wf[6]), "+v"(wf[7]));
    pin(zr); pin(zi); pin(sa); pin(sb);
    float rstd[8], fr[8], fi[8];
#pragma unroll
    for (int e = 0; e < 8; ++e) { fr[e] = blo(wf[e]) * (1.f / 2048.f); fi[e] = bhi(wf[e]) * (1.f / 2048.f); pinf(fr[e]); pinf(fi[e]); }
    STAMP(1);
    if (layer == 1) {
      __builtin_amdgcn_s_sleep(DELAY_C);
      unsigned spin = 0;
      bool again;
      do {
        RETRY(28);
        v4u st[16];
        ldx4<0>(zr, srd, tid * 16, OFF_ZA + (2 * c + first) * ZS); ldx4<0>(zi, srd, tid * 16, OFF_ZA + (2 * c + 1 - first) * ZS);
        sfor<0, 16>([&](auto I) { ldx4<0>(st[I.v], srd, tid * 16, OFF_ST + I.v * 4096); });
        vmwait<0>();
        pin(zr); pin(zi);
#pragma unroll
        for (int i = 0; i < 16; ++i) pin(st[i]);
        f32x4 a = __builtin_bit_cast(f32x4, st[0]), bq = __builtin_bit_cast(f32x4, st[1]);
#pragma unroll
        for (int n = 1; n < 8; ++n) { a += __builtin_bit_cast(f32x4, st[2 * n]); bq += __builtin_bit_cast(f32x4, st[2 * n + 1]); }
        sa = __builtin_bit_cast(v4u, a); sb = __builtin_bit_cast(v4u, bq);
        const f32x4 t = a + bq;
        const float u = (t.x + t.y) + (t.z + t.w);
        unsigned m = 0u;
        if (__builtin_expect(__builtin_amdgcn_ballot_w64(u != u) != 0ull, 0)) {
#pragma unroll
          for (int i = 0; i < 16; ++i) m = mxacc(m, st[i]);
        }
        again = (__builtin_amdgcn_ballot_w64(m == SENT) | bit0_set(tagacc(tagacc(0u, zr, 0u), zi, 0u))) != 0ull;
      } while (__builtin_expect(again, 0) && ++spin < SPIN_MAX);
    }
    load_ws<16, 18>(W, ws);
#pragma unroll
    for (int e = 0; e < 4; ++e) {
      rstd[e] = __builtin_amdgcn_rsqf(__builtin_fmaf(asf(sa[e]), 1.f / 512.f, 1e-5f));
      rstd[4 + e] = __builtin_amdgcn_rsqf(__builtin_fmaf(asf(sb[e]), 1.f / 512.f, 1e-5f));
    }
    load_ws<18, 20>(W, ws);
    STAMP(2);
    col_compute<20>(L, rs, k, fr, fi, zr, zi, rstd, c, w, lane, OFF_ZA + layer * (OFF_ZB - OFF_ZA), W, ws);
    STAMP(3);
    CSTAMP(23);
    load_ws<50, 64 - NW_LATE>(W, ws);
    __builtin_amdgcn_s_sleep(DELAY_A);
    ffn(L, rs, ws, p, W, layer, c, rb, fs, pi, nb, w, lane);
    STAMP(18);
  }
}

extern "C" void kernel_launch(void* const* d_in, const int* in_sizes, int n_in, void* d_out, int out_size, void* d_ws, size_t ws_size,
                              hipStream_t stream) {
  Params p;
  p.x = (const u16*)d_in[0]; p.wr = (const u16*)d_in[1]; p.wi = (const u16*)d_in[2];
  p.wg = (const u16*)d_in[4]; p.wu = (const u16*)d_in[5]; p.wd = (const u16*)d_in[6];
  p.out = (u16*)d_out; p.ws = (unsigned char*)d_ws;
  (void)hipFuncSetAttribute((const void*)k_prep, hipFuncAttributeMaxDynamicSharedMemorySize, P_LDS);
  (void)hipFuncSetAttribute((const void*)k_main, hipFuncAttributeMaxDynamicSharedMemorySize, M_LDS);
  k_prep<<<256, 512, P_LDS, stream>>>(p);
  k_main<<<256, 256, M_LDS, stream>>>(p);
}
```

```cpp
#include <hip/hip_runtime.h>
#include <hip/hip_bf16.h>

#define LAS __attribute__((address_space(3)))
#define DI __device__ __forceinline__
typedef unsigned short u16;
typedef unsigned v4u __attribute__((ext_vector_type(4)));
typedef unsigned v2u __attribute__((ext_vector_type(2)));
typedef float f32x2 __attribute__((ext_vector_type(2)));
typedef float f32x4 __attribute__((ext_vector_type(4)));
typedef float f32x16 __attribute__((ext_vector_type(16)));
typedef short s16x4 __attribute__((ext_vector_type(4)));
typedef __bf16 bf16x2 __attribute__((ext_vector_type(2)));
typedef __bf16 bf16x8 __attribute__((ext_vector_type(8)));
typedef __amdgpu_buffer_rsrc_t rsrc_t;

constexpr int MB = 1 << 20;
constexpr int ZS = 4224, XS = 1152, WS_ = 8320;
constexpr int OFF_ZA = 0, OFF_ZB = 9 * MB / 4;
constexpr int OFF_WT = 9 * MB;
constexpr int OFF_W1 = 12 * MB;
constexpr int OFF_W2 = 16 * MB;
constexpr int OFF_W3 = 18 * MB;
constexpr int OFF_H = 20 * MB;
constexpr int OFF_X3 = 36 * MB;
constexpr int OFF_R0 = 39 * MB;
constexpr int OFF_ST = 39 * MB + 65536;
constexpr int OFF_CTL = 39 * MB + 2 * 65536;
constexpr int OFF_FL = OFF_CTL + 4096;
constexpr int WS_BYTES = 40 * MB;
constexpr int SC1 = 16;
constexpr unsigned SENT = 0xFFFFFFFFu;
constexpr unsigned SPIN_MAX = 1u << 16;

struct Params {
  const u16* x; const u16* wr; const u16* wi; const u16* wg; const u16* wu; const u16* wd;
  u16* out; unsigned char* ws;
};

DI unsigned pk(float a, float b) { f32x2 v = {a, b}; return __builtin_bit_cast(unsigned, __builtin_convertvector(v, bf16x2)); }
DI float asf(unsigned u) { return __builtin_bit_cast(float, u); }
DI unsigned asu(float f) { return __builtin_bit_cast(unsigned, f); }
DI bf16x2 asb2(unsigned u) { return __builtin_bit_cast(bf16x2, u); }
template <int V> struct IC { static constexpr int v = V; };
template <int I, int N, class Fn> DI void sfor(Fn&& fn) { if constexpr (I < N) { fn(IC<I>{}); sfor<I + 1, N>(fn); } }
DI int opaque(int x) { asm volatile("" : "+s"(x)); return x; }
DI int is_zero(int x) { int n; asm volatile("s_min_u32 %0, %1, 1" : "=s"(n) : "s"(x) : "scc"); return 1 - n; }
DI float blo(unsigned u) { return __builtin_bit_cast(float, u << 16); }
DI float bhi(unsigned u) { return __builtin_bit_cast(float, u & 0xFFFF0000u); }
DI float rbf(float a) { return blo(pk(a, a)); }
DI void pk_tag(float a0, float b0, float a1, float b1, unsigned tag, unsigned& d0, unsigned& d1) {
  const unsigned c = pk(b0, b1), up = (tag ^ 1u) << 16;
  d0 = (__builtin_amdgcn_perm(c, asu(a0) + up, 0x05040302u) & ~1u) | tag;
  d1 = (__builtin_amdgcn_perm(c, asu(a1) + up, 0x07060302u) & ~1u) | tag;
}
DI unsigned nosent(unsigned u) { return u < 0xFFFFFFFEu ? u : 0xFFFFFFFEu; }
DI v4u nosent4(v4u v) { v.x = nosent(v.x); v.y = nosent(v.y); v.z = nosent(v.z); v.w = nosent(v.w); return v; }
DI unsigned umax(unsigned a, unsigned b) { return a > b ? a : b; }
DI unsigned mx4(v4u v) { return umax(umax(v.x, v.y), umax(v.z, v.w)); }
DI bool any_sent(unsigned m) { return __builtin_amdgcn_ballot_w64(m == SENT) != 0ull; }
DI f32x16 mfma32(v4u a, v4u b, f32x16 c) {
  return __builtin_amdgcn_mfma_f32_32x32x16_bf16(__builtin_bit_cast(bf16x8, a), __builtin_bit_cast(bf16x8, b), c, 0, 0, 0);
}
DI f32x4 mfma16(v4u a, v4u b, f32x4 c) {
  return __builtin_amdgcn_mfma_f32_16x16x32_bf16(__builtin_bit_cast(bf16x8, a), __builtin_bit_cast(bf16x8, b), c, 0, 0, 0);
}
template <bool FIRST, bool WA> DI void mfma_w(f32x16& acc, const v4u& wt, const v4u& x) {
  if constexpr (FIRST && WA) asm volatile("v_mfma_f32_32x32x16_bf16 %0, %1, %2, 0" : "=&v"(acc) : "a"(wt), "v"(x));
  if constexpr (FIRST && !WA) asm volatile("v_mfma_f32_32x32x16_bf16 %0, %2, %1, 0" : "=&v"(acc) : "a"(wt), "v"(x));
  if constexpr (!FIRST && WA) asm volatile("v_mfma_f32_32x32x16_bf16 %0, %1, %2, %0" : "+v"(acc) : "a"(wt), "v"(x));
  if constexpr (!FIRST && !WA) asm volatile("v_mfma_f32_32x32x16_bf16 %0, %2, %1, %0" : "+v"(acc) : "a"(wt), "v"(x));
}
template <bool FIRST> DI void mfma_v(f32x16& acc, const v4u& wt, const v4u& x) {
  if constexpr (FIRST) asm volatile("v_mfma_f32_32x32x16_bf16 %0, %2, %1, 0" : "=&v"(acc) : "v"(wt), "v"(x));
  else asm volatile("v_mfma_f32_32x32x16_bf16 %0, %2, %1, %0" : "+v"(acc) : "v"(wt), "v"(x));
}
template <bool FIRST> DI void mfma_vv(f32x16& acc, const v4u& a, const v4u& b) {
  if constexpr (FIRST) asm volatile("v_mfma_f32_32x32x16_bf16 %0, %1, %2, 0" : "=&v"(acc) : "v"(a), "v"(b));
  else asm volatile("v_mfma_f32_32x32x16_bf16 %0, %1, %2, %0" : "+v"(acc) : "v"(a), "v"(b));
}
template <int IMM> DI void vload(v4u& dst, const v4u& srd, int lane16, int so) {
  asm volatile("buffer_load_dwordx4 %0, %1, %2, %3 offen offset:%4 sc1" : "=v"(dst) : "v"(lane16), "s"(srd), "s"(so), "i"(IMM));
}
template <int IMM, bool REUSE> DI void wload(v4u& dst, const v4u& srd, int lane16, int so) {
  if constexpr (REUSE) asm volatile("buffer_load_dwordx4 %0, %1, %2, %3 offen offset:%4 sc1" : "+a"(dst) : "v"(lane16), "s"(srd), "s"(so), "i"(IMM));
  else asm volatile("buffer_load_dwordx4 %0, %1, %2, %3 offen offset:%4 sc1" : "=a"(dst) : "v"(lane16), "s"(srd), "s"(so), "i"(IMM));
}
DI void mfma_done(f32x16& a, f32x16& b, f32x16& c, f32x16& d) { asm volatile("s_nop 12" : "+v"(a), "+v"(b), "+v"(c), "+v"(d)); }
DI v2u trread(LAS unsigned char* p) { return __builtin_bit_cast(v2u, __builtin_amdgcn_ds_read_tr16_b64_v4i16((LAS s16x4*)p)); }
DI v4u tr2(LAS unsigned char* p, int d) { v2u a = trread(p), b = trread(p + d); return v4u{a.x, a.y, b.x, b.y}; }
DI v4u bload(rsrc_t rs, int voff, int soff) { return __builtin_amdgcn_raw_buffer_load_b128(rs, voff, soff, 0); }
DI v4u bload_sc1(rsrc_t rs, int voff, int soff) { return __builtin_amdgcn_raw_buffer_load_b128(rs, voff, soff, SC1); }
DI void bstore(v4u v, rsrc_t rs, int voff, int soff) { __builtin_amdgcn_raw_buffer_store_b128(v, rs, voff + soff, 0, 0); }
DI void bstore_sc1(v4u v, rsrc_t rs, int voff, int soff) { __builtin_amdgcn_raw_buffer_store_b128(v, rs, voff + soff, 0, SC1); }
template <int I> DI v4u fload(rsrc_t rs, int lane16, int base) { return bload(rs, lane16 + (I & 3) * 1024, base + (I >> 2) * 4096); }
template <int I> DI v4u fload_sc1(rsrc_t rs, int lane16, int base) { return bload_sc1(rs, lane16 + (I & 3) * 1024, base + (I >> 2) * 4096); }
#define SB() __builtin_amdgcn_sched_barrier(0)
#define MEMFENCE() asm volatile("" ::: "memory")
#define BAR() asm volatile("s_waitcnt lgkmcnt(0)\n\ts_barrier" ::: "memory")
template <int N> DI void vmwait() { asm volatile("s_waitcnt vmcnt(%0)" :: "i"(N) : "memory"); }
DI void pin(v4u& a) { asm volatile("" : "+v"(a)); }
DI f32x16 zero16() { f32x16 z; for (int i = 0; i < 16; ++i) z[i] = 0.f; return z; }
DI v4u pack8(const f32x16& a, int s, float sc) {
  return v4u{pk(a[8 * s] * sc, a[8 * s + 1] * sc), pk(a[8 * s + 2] * sc, a[8 * s + 3] * sc),
             pk(a[8 * s + 4] * sc, a[8 * s + 5] * sc), pk(a[8 * s + 6] * sc, a[8 * s + 7] * sc)};
}
DI void widen(const f32x16& a, float sc, v4u& o0, v4u& o1) {
  unsigned d[8];
#pragma unroll
  for (int i = 0; i < 8; ++i) d[i] = pk(a[2 * i] * sc, a[2 * i + 1] * sc);
  auto r0 = __builtin_amdgcn_permlane32_swap(d[0], d[2], false, false);
  auto r1 = __builtin_amdgcn_permlane32_swap(d[1], d[3], false, false);
  auto r2 = __builtin_amdgcn_permlane32_swap(d[4], d[6], false, false);
  auto r3 = __builtin_amdgcn_permlane32_swap(d[5], d[7], false, false);
  o0 = v4u{r0[0], r1[0], r0[1], r1[1]};
  o1 = v4u{r2[0], r3[0], r2[1], r3[1]};
}
template <int CTRL> DI float dppf(float v) {
  return __builtin_bit_cast(float, __builtin_amdgcn_update_dpp(0, __builtin_bit_cast(int, v), CTRL, 0xF, 0xF, true));
}
DI float sum32(float v) {
  v += dppf<0xB1>(v); v += dppf<0x4E>(v); v += dppf<0x141>(v); v += dppf<0x140>(v);
  unsigned u = __builtin_bit_cast(unsigned, v);
  auto r = __builtin_amdgcn_permlane16_swap(u, u, false, false);
  return __builtin_bit_cast(float, (unsigned)r[0]) + __builtin_bit_cast(float, (unsigned)r[1]);
}
DI int stat_of(int p) { return ((p >> 2) & 1) * 4096 + (p >> 3) * 16 + (p & 3) * 4; }
DI int token_of(int p) {
  const int l = (p >> 3) & 63, e = p & 7;
  return 32 * (16 * (p >> 9) + 4 * (l >> 4) + (e & 3)) + (l & 15) + 16 * (e >> 2);
}
DI v4u make_srd(const void* base, unsigned bytes) { return v4u{(unsigned)(size_t)base, (unsigned)((size_t)base >> 32) & 0xFFFFu, bytes, 0x00020000u}; }
DI void glds_in(const v4u& srd, int voff, int soff, unsigned lds) {
  asm volatile("s_mov_b32 m0, %3\n\ts_nop 0\n\tbuffer_load_dwordx4 %0, %1, %2 offen nt lds" :: "v"(voff), "s"(srd), "s"(soff), "s"(lds) : "memory");
}
template <int IMM> DI void ld2u16(unsigned& a, unsigned& b, int voff, const v4u& sa, const v4u& sb, int soff) {
  asm volatile("buffer_load_ushort %0, %2, %3, %5 offen offset:%6\n\tbuffer_load_ushort %1, %2, %4, %5 offen offset:%6"
               : "=&v"(a), "=&v"(b) : "v"(voff), "s"(sa), "s"(sb), "s"(soff), "i"(IMM));
}
DI v4u cosfrag(float a0, float step, int off) {
  float v[8];
#pragma unroll
  for (int j = 0; j < 8; ++j) v[j] = __builtin_amdgcn_cosf(__builtin_fmaf(step, (float)(off + j), a0));
  return v4u{pk(v[0], v[1]), pk(v[2], v[3]), pk(v[4], v[5]), pk(v[6], v[7])};
}

constexpr int M_Z = 0;
constexpr int M_RB = 40960;
constexpr int M_TB = 81920;
constexpr int M_S1 = 131072;
constexpr int M_S2 = M_S1 + 2048;
constexpr int M_C1 = M_S2 + 1024;
constexpr int M_C2 = M_C1 + 2 * 4608;
constexpr int M_LDS = M_C2 + 2 * 4608;
#define STAMP(i)
#define PST(i)
#define KSTAMP(i)
#define CSTAMP(i)
#define RETRY(i)
#define DELAY_A 6
#define DELAY_B 4
#define DELAY_C 2

template <int IMM> DI void ldx4(v4u& dst, const v4u& srd, int voff, int soff) {
  asm volatile("buffer_load_dwordx4 %0, %1, %2, %3 offen offset:%4 sc1" : "=v"(dst) : "v"(voff), "s"(srd), "s"(soff), "i"(IMM));
}
struct WSrc { v4u srd, srd1; int lane16, base; };
template <int N> DI void load_w(v4u (&W)[64], const WSrc& s) {
  wload<((N >> 1) & 3) * 1024, false>(W[N], (N & 1) ? s.srd1 : s.srd, s.lane16, s.base + (N & 1) * 32768 + (N >> 3) * 4096);
}
template <int LO, int HI> DI void load_ws(v4u (&W)[64], const WSrc& s) { SB(); sfor<LO, HI>([&](auto J) { load_w<J.v>(W, s); }); SB(); }
DI unsigned mxacc(unsigned m, const v4u& v) { return umax(umax(umax(umax(m, v.x), v.y), v.z), v.w); }
DI unsigned tagacc(unsigned m, const v4u& v, unsigned tag) { return m | ((v.x ^ tag) | (v.y ^ tag)) | ((v.z ^ tag) | (v.w ^ tag)); }
DI unsigned andacc(unsigned m, const v4u& v) { return (m & v.x & v.y) & (v.z & v.w); }
DI unsigned long long bit0_set(unsigned m) { return __builtin_amdgcn_ballot_w64((m & 1u) != 0u); }
DI unsigned long long bit0_clear(unsigned m) { return __builtin_amdgcn_ballot_w64((m & 1u) == 0u); }
DI void pins(unsigned long long& k) { asm volatile("" : "+s"(k)); }
DI void pinf(float& a) { asm volatile("" : "+v"(a)); }
DI void pinu(unsigned& a) { asm volatile("" : "+v"(a)); }

DI void publish(rsrc_t rs, int which, int c, unsigned long long tag, int w, int lane) {
  if (w == 0 && lane < 8)
    __builtin_amdgcn_raw_buffer_store_b64(v2u{(unsigned)tag, (unsigned)(tag >> 32)}, rs, OFF_FL + which * 16384 + lane * 2048 + c * 8, 0, SC1);
}
DI void flags_load(v4u (&f)[2], const v4u& srd, int which, int c, int lane) {
  ldx4<0>(f[0], srd, lane * 16, OFF_FL + which * 16384 + (c & 7) * 2048);
  ldx4<1024>(f[1], srd, lane * 16, OFF_FL + which * 16384 + (c & 7) * 2048);
}
DI void flags_wait(v4u (&f)[2], const v4u& srd, int which, int c, int lane, unsigned long long tag) {
  const unsigned lo = (unsigned)tag, hi = (unsigned)(tag >> 32);
  auto missing = [&]() {
    pin(f[0]); pin(f[1]);
    const unsigned d = ((f[0].x ^ lo) | (f[0].y ^ hi) | (f[0].z ^ lo)) | ((f[0].w ^ hi) | (f[1].x ^ lo) | (f[1].y ^ hi)) | ((f[1].z ^ lo) | (f[1].w ^ hi));
    return __builtin_amdgcn_ballot_w64(d != 0u) != 0ull;
  };
  if (__builtin_expect(missing(), 0)) {
    unsigned spin = 0;
    do { __builtin_amdgcn_s_sleep(2); flags_load(f, srd, which, c, lane); vmwait<0>(); } while (missing() && ++spin < SPIN_MAX);
  }
}

struct ColConst { v4u C1[2], S1[2], N1[2], C2[2], S2[2], N2[2]; float tc[8], ts[8]; };

template <bool LOADS> DI void col_consts(ColConst& k, int w, int lane, v4u (&W)[64], const WSrc& ws) {
  const int g = lane >> 4, r = lane & 15;
  const float k1 = (float)(16 * w + r);
  const unsigned neg = 0x80008000u;
  sfor<0, 2>([&](auto CS) {
    constexpr int cs = CS.v;
    const float st = k1 * (1.f / 64.f), a0 = st * (float)(8 * g);
    if constexpr (LOADS) load_ws<3 * cs, 3 * cs + 2>(W, ws);
    k.C1[cs] = cosfrag(a0, st, 32 * cs);
    pin(k.C1[cs]);
    if constexpr (LOADS) load_ws<3 * cs + 2, 3 * cs + 3>(W, ws);
    k.S1[cs] = cosfrag(a0 - 0.25f, st, 32 * cs);
    k.N1[cs] = k.S1[cs] ^ neg;
    pin(k.S1[cs]); pin(k.N1[cs]);
  });
  sfor<0, 2>([&](auto T) {
    constexpr int t = T.v;
    const float st = (float)(16 * t + r) * (1.f / 32.f), a0 = st * (float)(4 * g);
    float c[8], s[8];
    if constexpr (LOADS) load_ws<6 + 3 * t, 8 + 3 * t>(W, ws);
#pragma unroll
    for (int j = 0; j < 8; ++j) c[j] = __builtin_amdgcn_cosf(__builtin_fmaf(st, (float)(16 * (j >> 2) + (j & 3)), a0));
    k.C2[t] = v4u{pk(c[0], c[1]), pk(c[2], c[3]), pk(c[4], c[5]), pk(c[6], c[7])};
    pin(k.C2[t]);
    if constexpr (LOADS) load_ws<8 + 3 * t, 9 + 3 * t>(W, ws);
#pragma unroll
    for (int j = 0; j < 8; ++j) s[j] = __builtin_amdgcn_sinf(__builtin_fmaf(st, (float)(16 * (j >> 2) + (j & 3)), a0));
    k.S2[t] = v4u{pk(s[0], s[1]), pk(s[2], s[3]), pk(s[4], s[5]), pk(s[6], s[7])};
    k.N2[t] = k.S2[t] ^ neg;
    pin(k.S2[t]); pin(k.N2[t]);
  });
  sfor<0, 2>([&](auto T) {
    constexpr int t = T.v;
    if constexpr (LOADS) load_ws<12 + 2 * t, 14 + 2 * t>(W, ws);
#pragma unroll
    for (int e = 4 * t; e < 4 * t + 4; ++e) {
      const float a = k1 * (1.f / 2048.f) * (float)(16 * (e >> 2) + 4 * g + (e & 3));
      k.tc[e] = __builtin_amdgcn_cosf(a); k.ts[e] = __builtin_amdgcn_sinf(a);
      pinf(k.tc[e]); pinf(k.ts[e]);
    }
  });
}

template <bool FIRST> DI void m16(f32x4& acc, const v4u& a, const v4u& b) {
  SB();
  if constexpr (FIRST) asm volatile("v_mfma_f32_16x16x32_bf16 %0, %1, %2, 0" : "=&v"(acc) : "v"(a), "v"(b));
  else asm volatile("v_mfma_f32_16x16x32_bf16 %0, %1, %2, %0" : "+v"(acc) : "v"(a), "v"(b));
  SB();
}
DI void m16_done(f32x4& a, f32x4& b) { asm volatile("s_nop 7" : "+v"(a), "+v"(b)); }
DI void m16_ready(v4u& a, v4u& b) { asm volatile("s_nop 1" : "+v"(a), "+v"(b)); }
template <int PART> DI float cmul(float a, float b, float c, float s) {
  float r = PART == 0 ? __builtin_fmaf(a, c, -(b * s)) : __builtin_fmaf(a, s, b * c);
  pinf(r);
  return r;
}

template <int WB>
DI void col_compute(LAS unsigned char* L, rsrc_t rs, const ColConst& k, const float (&fr)[8], const float (&fi)[8], v4u zr, v4u zi,
                    const float (&rstd)[8], int c, int w, int lane, int out_off, v4u (&W)[64], const WSrc& ws) {
  const int g = lane >> 4, r = lane & 15;
  const int first = is_zero(c);
  const float f0 = first ? 0.f : 1.f;
  float xr[8], xi[8];
#pragma unroll
  for (int i = 0; i < 4; ++i) { xr[2 * i] = blo(zr[i]); xr[2 * i + 1] = bhi(zr[i]); xi[2 * i] = blo(zi[i]); xi[2 * i + 1] = bhi(zi[i]); }
#pragma unroll
  for (int t = 0; t < 2; ++t) {
    const f32x4 rv = {rstd[4 * t], rstd[4 * t + 1], rstd[4 * t + 2], rstd[4 * t + 3]};
    const f32x4 a = f32x4{xr[4 * t], xr[4 * t + 1], xr[4 * t + 2], xr[4 * t + 3]} * rv;
    const f32x4 bq = f32x4{xi[4 * t], xi[4 * t + 1], xi[4 * t + 2], xi[4 * t + 3]} * (rv * f0);
    LAS unsigned char* d = L + M_C1 + (r + 16 * t) * 144 + (16 * w + 4 * g) * 2;
    *(LAS v2u*)d = v2u{pk(a[0], a[1]), pk(a[2], a[3])};
    *(LAS v2u*)(d + 4608) = v2u{pk(bq[0], bq[1]), pk(bq[2], bq[3])};
  }
  load_ws<WB, WB + 3>(W, ws);
  BAR();
  f32x4 dr[2], di[2];
  float ur[8], ui[8];
  {
    v4u ar[2][2], ai[2][2];
#pragma unroll
    for (int t = 0; t < 2; ++t)
#pragma unroll
      for (int cs = 0; cs < 2; ++cs) {
        LAS unsigned char* s = L + M_C1 + (r + 16 * t) * 144 + (32 * cs + 8 * g) * 2;
        ar[t][cs] = *(LAS v4u*)s; ai[t][cs] = *(LAS v4u*)(s + 4608);
      }
    load_ws<WB + 3, WB + 5>(W, ws);
    sfor<0, 16>([&](auto N) {
      constexpr int n = N.v, t = n >> 3, cs = (n >> 2) & 1, m = n & 3;
      if constexpr (m == 0) m16<cs == 0>(dr[t], ar[t][cs], k.C1[cs]);
      if constexpr (m == 1) m16<cs == 0>(di[t], ai[t][cs], k.C1[cs]);
      if constexpr (m == 2) m16<false>(dr[t], ai[t][cs], k.S1[cs]);
      if constexpr (m == 3) m16<false>(di[t], ar[t][cs], k.N1[cs]);
      if constexpr (m == 3) load_w<WB + 5 + (n >> 2)>(W, ws);
      if constexpr (n >= 9) {
        constexpr int e = (n - 9) >> 1;
        if constexpr (n & 1) ur[e] = cmul<0>(dr[0][e], di[0][e], k.tc[e], -k.ts[e]);
        else ui[e] = cmul<1>(dr[0][e], di[0][e], k.tc[e], -k.ts[e]);
      }
    });
    ui[3] = cmul<1>(dr[0][3], di[0][3], k.tc[3], -k.ts[3]);
  }
  v4u br, bi;
  br.x = pk(ur[0], ur[1]); br.y = pk(ur[2], ur[3]); bi.x = pk(ui[0], ui[1]); bi.y = pk(ui[2], ui[3]);
  SB();
  m16_done(dr[1], di[1]);
  load_ws<WB + 9, WB + 11>(W, ws);
#pragma unroll
  for (int e = 4; e < 8; ++e) {
    ur[e] = dr[1][e - 4] * k.tc[e] + di[1][e - 4] * k.ts[e]; ui[e] = di[1][e - 4] * k.tc[e] - dr[1][e - 4] * k.ts[e];
  }
  br.z = pk(ur[4], ur[5]); br.w = pk(ur[6], ur[7]); bi.z = pk(ui[4], ui[5]); bi.w = pk(ui[6], ui[7]);
  m16_ready(br, bi);
  sfor<0, 8>([&](auto N) {
    constexpr int n = N.v, t = n >> 2, m = n & 3;
    if constexpr (m == 0) m16<true>(dr[t], k.C2[t], br);
    if constexpr (m == 1) m16<true>(di[t], k.C2[t], bi);
    if constexpr (m == 2) m16<false>(dr[t], k.S2[t], bi);
    if constexpr (m == 3) m16<false>(di[t], k.N2[t], br);
    if constexpr (m == 3) load_w<WB + 11 + t>(W, ws);
    if constexpr (n == 5) ur[0] = cmul<0>(dr[0][0], di[0][0], fr[0], fi[0]);
    if constexpr (n == 6) ui[0] = cmul<1>(dr[0][0], di[0][0], fr[0], fi[0]);
  });
#pragma unroll
  for (int e = 1; e < 4; ++e) { ur[e] = dr[0][e] * fr[e] - di[0][e] * fi[e]; ui[e] = dr[0][e] * fi[e] + di[0][e] * fr[e]; }
  br.x = pk(ur[0], ur[1]); br.y = pk(ur[2], ur[3]); bi.x = pk(ui[0], ui[1]); bi.y = pk(ui[2], ui[3]);
  SB();
  m16_done(dr[1], di[1]);
  load_ws<WB + 13, WB + 15>(W, ws);
#pragma unroll
  for (int e = 4; e < 8; ++e) { ur[e] = dr[1][e - 4] * fr[e] - di[1][e - 4] * fi[e]; ui[e] = dr[1][e - 4] * fi[e] + di[1][e - 4] * fr[e]; }
  br.z = pk(ur[4], ur[5]); br.w = pk(ur[6], ur[7]); bi.z = pk(ui[4], ui[5]); bi.w = pk(ui[6], ui[7]);
  m16_ready(br, bi);
  sfor<0, 8>([&](auto N) {
    constexpr int n = N.v, t = n >> 2, m = n & 3;
    if constexpr (m == 0) m16<true>(dr[t], k.C2[t], br);
    if constexpr (m == 1) m16<true>(di[t], k.C2[t], bi);
    if constexpr (m == 2) m16<false>(dr[t], k.N2[t], bi);
    if constexpr (m == 3) m16<false>(di[t], k.S2[t], br);
    if constexpr (m == 3) load_w<WB + 15 + t>(W, ws);
    if constexpr (n == 5) ur[0] = cmul<0>(dr[0][0], di[0][0], k.tc[0], k.ts[0]);
    if constexpr (n == 6) ui[0] = cmul<1>(dr[0][0], di[0][0], k.tc[0], k.ts[0]);
  });
#pragma unroll
  for (int t = 0; t < 2; ++t) {
    if (t == 1) { SB(); m16_done(dr[1], di[1]); load_ws<WB + 17, WB + 19>(W, ws); }
#pragma unroll
    for (int i = (t == 0 ? 1 : 0); i < 4; ++i) {
      const int e = 4 * t + i;
      ur[e] = dr[t][i] * k.tc[e] - di[t][i] * k.ts[e]; ui[e] = di[t][i] * k.tc[e] + dr[t][i] * k.ts[e];
    }
    LAS unsigned char* d = L + M_C2 + (16 * w + r) * 72 + (16 * t + 4 * g) * 2;
    *(LAS v2u*)d = v2u{pk(ur[4 * t], ur[4 * t + 1]), pk(ur[4 * t + 2], ur[4 * t + 3])};
    *(LAS v2u*)(d + 4608) = v2u{pk(ui[4 * t], ui[4 * t + 1]), pk(ui[4 * t + 2], ui[4 * t + 3])};
  }
  load_ws<WB + 19, WB + 21>(W, ws);
  BAR();
  unsigned od[8];
  {
    v4u tr_[2][2], ti_[2][2];
#pragma unroll
    for (int t = 0; t < 2; ++t)
#pragma unroll
      for (int cs = 0; cs < 2; ++cs) {
        LAS unsigned char* s = L + M_C2 + (32 * cs + 8 * g + (r >> 2)) * 72 + (16 * t + 4 * (r & 3)) * 2;
        tr_[t][cs] = tr2(s, 4 * 72); ti_[t][cs] = tr2(s + 4608, 4 * 72);
      }
    load_ws<WB + 21, WB + 23>(W, ws);
    sfor<0, 16>([&](auto N) {
      constexpr int n = N.v, t = n >> 3, cs = (n >> 2) & 1, m = n & 3;
      if constexpr (m == 0) m16<cs == 0>(dr[t], k.C1[cs], tr_[t][cs]);
      if constexpr (m == 1) m16<cs == 0>(di[t], k.C1[cs], ti_[t][cs]);
      if constexpr (m == 2) m16<false>(dr[t], k.N1[cs], ti_[t][cs]);
      if constexpr (m == 3) m16<false>(di[t], k.S1[cs], tr_[t][cs]);
      if constexpr (m == 3) load_w<WB + 23 + (n >> 2)>(W, ws);
      if constexpr (n >= 9 && n < 13) {
        constexpr int e = n - 9;
        xr[e] += dr[0][e]; xi[e] = __builtin_fmaf(di[0][e], f0, xi[e]);
        pinf(xr[e]); pinf(xi[e]);
      }
      if constexpr (n == 14) { pk_tag(xr[0], xr[1], xr[2], xr[3], 1u, od[0], od[1]); pinu(od[0]); pinu(od[1]); }
    });
    pk_tag(xi[0], xi[1], xi[2], xi[3], 1u, od[4], od[5]);
    SB();
    m16_done(dr[1], di[1]);
#pragma unroll
    for (int e = 4; e < 8; ++e) { xr[e] += dr[1][e - 4]; xi[e] = __builtin_fmaf(di[1][e - 4], f0, xi[e]); }
    pk_tag(xr[4], xr[5], xr[6], xr[7], 1u, od[2], od[3]);
    pk_tag(xi[4], xi[5], xi[6], xi[7], 1u, od[6], od[7]);
  }
  const int vo = (w * 64 + lane) * 16;
  bstore_sc1(v4u{od[0], od[1], od[2], od[3]}, rs, vo, out_off + (2 * c + first) * ZS);
  bstore_sc1(v4u{od[4], od[5], od[6], od[7]}, rs, vo, out_off + (2 * c + 1 - first) * ZS);
  load_ws<WB + 27, WB + 30>(W, ws);
}

constexpr int NW_LATE = 8;
constexpr int NV_EARLY = 16;
constexpr int ZG[6] = {0, 2, 4, 8, 16, 32};
constexpr int ZR[5] = {-1, 1, 4, 10, 21};
constexpr int z_read_at(int ks) {
  for (int G = 1; G < 5; ++G) if (ks >= ZR[G] && 2 * (ks - ZR[G]) < ZG[G + 1] - ZG[G]) return ZG[G] + 2 * (ks - ZR[G]);
  return -1;
}
constexpr int z_first(int j) { for (int G = 0; G < 5; ++G) if (ZG[G] == j) return 1; return 0; }
constexpr int z_decide_at(int ks) { for (int G = 1; G < 5; ++G) if (ks == ZR[G] + (ZG[G + 1] - ZG[G]) / 2) return G; return -1; }
constexpr int z_requested(int ks) { return ks < 8 ? 9 + ks : ks < 16 ? 2 * ks + 1 : 32; }
constexpr int z_younger(int j, int ks) {
  int n = z_requested(ks) - j - 1;
  for (int k = 0; k < ks; ++k) if ((k < 8 && j < 16 && k >= j - 8) || k >= 16) ++n;
  return n;
}
struct ZSrc { v4u srd; int vo, so; unsigned lds; };
template <int J> DI void z_issue(const ZSrc& z, int so) {
  asm volatile("s_add_u32 m0, %0, %4\n\ts_nop 0\n\tbuffer_load_dwordx4 %1, %2, %3 offen sc1 lds"
               :: "s"(z.lds), "v"(z.vo), "s"(z.srd), "s"(so + (J & 15) * 32 * ZS + (J >> 4) * 128), "i"((J >> 4) * 65536 + (J & 15) * 4096)
               : "memory", "scc");
}
template <int J> DI v4u z_back(LAS unsigned char* zown) { return *(LAS v4u*)(zown + (J >> 4) * 65536 + (J & 15) * 4096); }
template <int G, int KS> DI void z_fix(unsigned long long bad, const ZSrc& z, LAS unsigned char* zown, LAS unsigned char* L, int layer, int w) {
  if (__builtin_expect(bad != 0ull, 0)) {
    unsigned spin = 0, m;
    vmwait<0>();
    do {
      RETRY(G == 0 ? 24 : 25);
      const int zo = opaque(z.so);
      sfor<ZG[G], z_requested(KS)>([&](auto I) { z_issue<I.v>(z, zo); });
      vmwait<0>();
      m = 1u;
      sfor<ZG[G], ZG[G + 1]>([&](auto I) { m = andacc(m, z_back<I.v>(zown)); });
    } while (bit0_clear(m) != 0ull && ++spin < SPIN_MAX);
  }
}

#define HA_START 14
#define HB_START 6
#define H_ROOM 16
constexpr int NSTR = 56 - NV_EARLY + 58;
struct Stream { int kind[NSTR], idx[NSTR], end[89]; };
constexpr Stream make_stream() {
  Stream s{};
  int kind[NSTR] = {}, idx[NSTR] = {}, rel[NSTR] = {}, need[NSTR] = {};
  bool done[NSTR] = {};
  int n = 0;
  for (int i = NV_EARLY; i < 56; ++i) { kind[n] = 0; idx[n] = i; rel[n] = (i - NV_EARLY) >> 1; need[n] = i >> 1; ++n; }
  for (int i = 0; i < 28; ++i) { kind[n] = 1; idx[n] = i; rel[n] = HA_START; need[n] = i; ++n; }
  for (int i = 0; i < 28; ++i) { kind[n] = 2; idx[n] = i; rel[n] = 33 + HB_START; need[n] = 28 + i; ++n; }
  for (int i = 0; i < 2; ++i) { kind[n] = 3; idx[n] = i; rel[n] = 33 + HB_START; need[n] = 60; ++n; }
  int e = 0, held = 0;
  for (int sl = 0; sl < 89; ++sl) {
    int cap = sl < 8 ? 1 : sl < 32 ? 2 - (sl % 4 == 3) : sl == 32 ? 6 : (sl - 33) % 7 == 6 ? 0 : 2;
    for (; cap > 0; --cap) {
      int best = -1;
      const int live = held - (sl < 33 ? 0 : sl - 32), room = sl < 32 ? H_ROOM : H_ROOM + 6;
      for (int i = 0; i < NSTR; ++i)
        if (!done[i] && rel[i] <= sl && (kind[i] == 0 || live < room) && (best < 0 || need[i] < need[best])) best = i;
      if (best < 0) break;
      if (kind[best] != 0) ++held;
      done[best] = true; s.kind[e] = kind[best]; s.idx[e] = idx[best]; ++e;
    }
    s.end[sl] = e;
  }
  return s;
}
constexpr Stream STR = make_stream();
static_assert(STR.end[88] == NSTR, "every load is issued");
constexpr int str_pos(int kind, int idx) { for (int i = 0; i < NSTR; ++i) if (STR.kind[i] == kind && STR.idx[i] == idx) return i; return -1; }
constexpr int str_count(int kind, int upto) { int n = 0; for (int i = 0; i < upto; ++i) if (STR.kind[i] == kind) ++n; return n; }
struct Down {
  int vbase, hbase, xvo, xso;
  DI int blk(int g) const { return g * 4096; }
};
template <int SLOT, int PART> DI void stream_issue(v4u (&W)[64], v4u (&hA)[28], v4u (&hB)[28], v4u (&x3)[2], const v4u& srd, int lane16, const Down& d) {
  constexpr int lo = SLOT == 0 ? 0 : STR.end[SLOT - 1], hi = STR.end[SLOT];
  sfor<(PART == 1 ? lo + 1 : lo), (PART == 0 && hi > lo ? lo + 1 : hi)>([&](auto I) {
    constexpr int k = STR.kind[I.v], i = STR.idx[I.v];
    if constexpr (k == 0) wload<((i >> 1) & 3) * 1024, true>(W[i - NV_EARLY], srd, lane16, d.vbase + (i & 1) * 112 * 1024 + d.blk(i >> 3));
    if constexpr (k == 1) ldx4<(i & 3) * 1024>(hA[i], srd, lane16, d.hbase + d.blk(i >> 2));
    if constexpr (k == 2) ldx4<(i & 3) * 1024>(hB[i], srd, lane16, d.hbase + 224 * 1024 + d.blk(i >> 2));
    if constexpr (k == 3) ldx4<0>(x3[i], srd, d.xvo, d.xso + i * 64 * XS);
  });
}
constexpr int dn_need(int g) {
  const int H = g / 28, i = g % 28;
  const int ph = str_pos(1 + H, i), pv = H == 0 && 2 * i >= NV_EARLY ? str_pos(0, 2 * i + 1) : 0;
  return ph > pv ? ph : pv;
}
template <int G, int ISSUED> DI void h_acc(v4u (&hA)[28], v4u (&hB)[28], unsigned& m, unsigned tag) {
  static_assert(dn_need(G) < ISSUED, "requested in time");
  vmwait<ISSUED - 1 - dn_need(G)>();
  v4u& x = G < 28 ? hA[G % 28] : hB[G % 28];
  pin(x);
  m = tagacc(m, x, tag);
  pinu(m);
}
template <int G, int ISSUED> DI void h_decide(v4u (&hA)[28], v4u (&hB)[28], unsigned long long bad, const v4u& srd, int lane16, const Down& d,
                                              LAS unsigned char* L, int layer, int w) {
  constexpr int H = G / 28, LO = G % 28;
  v4u (&hh)[28] = H == 0 ? hA : hB;
  if (__builtin_expect(bad != 0ull, 0)) {
    unsigned spin = 0, m;
    vmwait<0>();
    do {
      RETRY(26 + H);
      const int bo = opaque(d.hbase);
      sfor<LO, str_count(1 + H, ISSUED)>([&](auto I) { ldx4<(I.v & 3) * 1024>(hh[I.v], srd, lane16, bo + H * 224 * 1024 + d.blk(I.v >> 2)); });
      vmwait<0>();
      m = 0;
#pragma unroll
      for (int i = LO; i < LO + 7; ++i) { pin(hh[i]); m = tagacc(m, hh[i], layer); }
    } while (bit0_set(m) != 0ull && ++spin < SPIN_MAX);
  }
}

DI void sumsq1(float& ss, unsigned a) {
  const float x = blo(a), y = bhi(a);
  asm volatile("v_fmac_f32 %0, %1, %1\n\tv_fmac_f32 %0, %2, %2" : "+v"(ss) : "v"(x), "v"(y));
}
DI float mulf(float a, float b) { float r; asm volatile("v_mul_f32 %0, %1, %2" : "=v"(r) : "v"(a), "v"(b)); return r; }
DI void addf4(f32x4& a, const f32x4& b) {
  float x = a.x, y = a.y, z = a.z, t = a.w;
  asm volatile("v_add_f32 %0, %0, %4\n\tv_add_f32 %1, %1, %5\n\tv_add_f32 %2, %2, %6\n\tv_add_f32 %3, %3, %7"
               : "+v"(x), "+v"(y), "+v"(z), "+v"(t) : "v"(b.x), "v"(b.y), "v"(b.z), "v"(b.w));
  a = f32x4{x, y, z, t};
}
DI float relu(float a) { const int i = __builtin_bit_cast(int, a); return __builtin_bit_cast(float, i > 0 ? i : 0); }
DI void h_piece(const f32x16& a, unsigned tag, rsrc_t rs, int lane, int so) {
  unsigned d[4];
#pragma unroll
  for (int i = 0; i < 2; ++i)
    pk_tag(relu(a[4 * i]) * a[4 * i + 8], relu(a[4 * i + 1]) * a[4 * i + 9], relu(a[4 * i + 2]) * a[4 * i + 10], relu(a[4 * i + 3]) * a[4 * i + 11], tag,
           d[2 * i], d[2 * i + 1]);
  bstore_sc1(v4u{d[0], d[1], d[2], d[3]}, rs, lane * 16, so);
}
DI void x3_piece(const f32x16& a, rsrc_t rs, int lane, int so) {
  v4u o0, o1;
  widen(a, 1.f, o0, o1);
  bstore_sc1(nosent4(o0), rs, (lane & 31) * XS + (lane >> 5) * 16, so);
  bstore_sc1(nosent4(o1), rs, (lane & 31) * XS + (lane >> 5) * 16, so + 32);
}

DI void ffn(LAS unsigned char* L, rsrc_t rs, const WSrc& ws, const Params& p, v4u (&W)[64], int layer, int c, int rb, int fs, int pi, int nb,
            int w, int lane) {
  const v4u& srd = ws.srd;
  constexpr int hoff = OFF_H;
  const int h = lane >> 5, r = lane & 31, q = (lane & 15) >> 2, lane16 = lane * 16;
  ZSrc z;
  z.srd = srd;
  z.vo = (8 * w + (lane >> 3)) * ZS + (((lane & 7) ^ ((lane >> 2) & 4)) * 16);
  z.so = OFF_ZA + layer * (OFF_ZB - OFF_ZA) + rb * 256;
  z.lds = (unsigned)(size_t)L + M_Z + w * 1024;
  STAMP(4);
  sfor<0, 8>([&](auto I) { z_issue<I.v>(z, z.so); });
  SB();
  LAS unsigned char* zown = L + M_Z + w * 1024 + lane16;
  LAS unsigned char* zr[2];
#pragma unroll
  for (int s = 0; s < 2; ++s)
    zr[s] = L + M_Z + (4 * h + q) * 128 + (4 * (s ^ (q >> 1)) + 2 * ((lane >> 4) & 1) + ((lane & 3) >> 1)) * 16 + 8 * (lane & 1);
  const unsigned dcmask = (w == 0 && h == 0) ? 0xFFFF0000u : 0xFFFFFFFFu;
  const int pt = 4 * rb + pi;
  Down dn;
  dn.vbase = OFF_W2 + layer * (OFF_W3 - OFF_W2) + (nb * 2 * 112 + 28 * w) * 1024;
  dn.hbase = hoff + (pt * 112 + 28 * w) * 1024;
  dn.xvo = (lane & 31) * XS + h * 16;
  dn.xso = OFF_W1 + layer * (OFF_X3 - OFF_W1) + pt * 32 * XS + nb * 128 + w * 32;
  const int ks0 = fs * 7 + 2 * w;
  const bool two = w < 3;
  const bool idft = opaque(w + layer) == 4;
  const rsrc_t rs2 = __builtin_amdgcn_make_buffer_rsrc((void*)p.ws, 0, two ? WS_BYTES : 0, 0x00020000);
  v4u hA[28], hB[28], x3[2], res[2], Ve[NV_EARLY];
  unsigned s0, s1, s2, s3;
  asm volatile("v_mov_b32 %0, -1\n\tv_mov_b32 %1, -1\n\tv_mov_b32 %2, -1\n\tv_mov_b32 %3, -1" : "=v"(s0), "=v"(s1), "=v"(s2), "=v"(s3));
  const v4u sv = {s0, s1, s2, s3};
  const rsrc_t rsf = __builtin_amdgcn_make_buffer_rsrc((void*)p.ws, 0, layer == 0 ? WS_BYTES : 0, 0x00020000);
  const int fo_z = (w * 64 + lane) * 16 + OFF_ZB + 2 * c * ZS,
            fo_x = (w * 16 + (lane >> 2)) * XS + (lane & 3) * 16 + OFF_X3 + 128 * rb * XS + fs * 64;
  STAMP(5);
  vmwait<z_younger(1, -1)>();
  z_fix<0, -1>(bit0_clear(andacc(andacc(1u, z_back<0>(zown)), z_back<1>(zown))), z, zown, L, layer, w);
  STAMP(6);
  BAR();
  STAMP(7);
  CSTAMP(20);
  f32x16 acc[2][2][2];
  float ss[2] = {0.f, 0.f};
  sfor<0, 2>([&](auto HH) {
    constexpr int H = HH.v;
    v4u bf[2][2], sq[2], rbk[2];
    unsigned hq[4];
    float hp[4];
    unsigned zm;
    unsigned long long bad;
    LAS unsigned char* zb[2] = {zr[0] + H * 65536, zr[1] + H * 65536};
    asm volatile("" : "+v"(zb[0]), "+v"(zb[1]));
    LAS unsigned char* zq[2];
    if constexpr (H == 1) { zq[0] = (pi ? zr[1] : zr[0]) + w * 16384; zq[1] = zq[0] + 65536; asm volatile("" : "+v"(zq[0]), "+v"(zq[1])); }
#pragma unroll
    for (int s = 0; s < 2; ++s) bf[0][s] = tr2(zb[s], 1024);
    sfor<0, 32>([&](auto KS) {
      constexpr int ks = KS.v;
      constexpr bool edge = H == 0 && (ks == 3 || ks == 7 || ks == 15);
      constexpr int e = ks >> 1, es = e & 1, et = e >> 1;
      constexpr int sr = H == 1 && ks >= 8 && (ks - 8) % 3 < 2 ? 2 * ((ks - 8) / 3) + (ks - 8) % 3 : -1;
      constexpr int sn = H == 1 && ks >= 9 && (ks - 9) % 3 < 2 ? 2 * ((ks - 9) / 3) + (ks - 9) % 3 : -1;
      constexpr int rd = H == 0 ? z_read_at(ks) : -1, rp = H == 0 && ks > 0 ? z_read_at(ks - 1) : -1, dec = H == 0 ? z_decide_at(ks) : -1;
      mfma_w<ks == 0, true>(acc[H][0][0], W[2 * ks], bf[ks & 1][0]);
      SB();
      if constexpr (ks + 1 < 32 && !edge) bf[(ks + 1) & 1][0] = tr2(zb[0] + (ks + 1) * 2048, 1024);
      if constexpr (rp >= 0) { zm = andacc(z_first(rp) ? 1u : zm, rbk[0]); pinu(zm); }
      if constexpr (H == 1 && ks < 8) hp[0] = mulf(relu(acc[0][et][es][4 * (ks & 1)]), acc[0][et][es][4 * (ks & 1) + 8]);
      if constexpr (H == 1 && ks < 8) hp[1] = mulf(relu(acc[0][et][es][4 * (ks & 1) + 1]), acc[0][et][es][4 * (ks & 1) + 9]);
      if constexpr (sn >= 0) sumsq1(ss[sn & 1], sn < 2 ? sq[sn & 1].x & dcmask : sq[sn & 1].x);
      SB();
      mfma_w<ks == 0, true>(acc[H][1][0], W[2 * ks + 1], bf[ks & 1][0]);
      SB();
      if constexpr (H == 0 && ks < 16) z_issue<(ks < 8 ? 8 + ks : 2 * ks)>(z, z.so);
      if constexpr (H == 1 && ks < 8) hp[2] = mulf(relu(acc[0][et][es][4 * (ks & 1) + 2]), acc[0][et][es][4 * (ks & 1) + 10]);
      if constexpr (H == 1 && ks < 8) hp[3] = mulf(relu(acc[0][et][es][4 * (ks & 1) + 3]), acc[0][et][es][4 * (ks & 1) + 11]);
      if constexpr (sn >= 0) sumsq1(ss[sn & 1], sq[sn & 1].y);
      if constexpr (H == 1 && ks > 0) stream_issue<ks - 1, 1>(W, hA, hB, x3, srd, lane16, dn);
      if constexpr (H == 0 && ks >= 16 && (ks & 3) == 0) {
        constexpr int i = (ks - 16) / 4;
        if constexpr (i < 2) bstore_sc1(sv, rsf, fo_x, i * 64 * XS);
        else bstore_sc1(~sv, rsf, fo_z, (i - 2) * ZS);
      }
      SB();
      mfma_w<ks == 0, true>(acc[H][0][1], W[2 * ks], bf[ks & 1][1]);
      SB();
      if constexpr (ks + 1 < 32 && !edge) bf[(ks + 1) & 1][1] = tr2(zb[1] + (ks + 1) * 2048, 1024);
      if constexpr (rp >= 0) { zm = andacc(zm, rbk[1]); pinu(zm); }
      if constexpr (dec >= 0) { bad = bit0_clear(zm); pins(bad); }
      if constexpr (H == 1 && ks < 8) { pk_tag(hp[0], hp[1], hp[2], hp[3], layer, hq[2 * (ks & 1)], hq[2 * (ks & 1) + 1]); pinu(hq[2 * (ks & 1)]); pinu(hq[2 * (ks & 1) + 1]); }
      if constexpr (sn >= 0) sumsq1(ss[sn & 1], sq[sn & 1].z);
      SB();
      mfma_w<ks == 0, true>(acc[H][1][1], W[2 * ks + 1], bf[ks & 1][1]);
      SB();
      if constexpr (dec >= 0) { pins(bad); z_fix<dec, ks>(bad, z, zown, L, layer, w); }
      if constexpr (rd >= 0) { vmwait<z_younger(rd + 1, ks)>(); rbk[0] = z_back<rd>(zown); rbk[1] = z_back<rd + 1>(zown); }
      if constexpr (sn >= 0) sumsq1(ss[sn & 1], sq[sn & 1].w);
      if constexpr (sr >= 0) sq[sr & 1] = tr2(zq[sr & 1] + (sr >> 1) * 2048, 1024);
      if constexpr (H == 0 && ks < NW_LATE) load_w<64 - NW_LATE + ks>(W, ws);
      if constexpr (H == 0 && ks >= 8 && ks < 16) z_issue<2 * ks + 1>(z, z.so);
      if constexpr (H == 0 && ks >= 16) {
        constexpr int n = ks - 16;
        vload<((n >> 1) & 3) * 1024>(Ve[n], srd, lane16, dn.vbase + (n & 1) * 112 * 1024 + dn.blk(n >> 3));
      }
      if constexpr (H == 1) {
        if constexpr (ks < 8 && (ks & 1)) {
          bstore_sc1(v4u{hq[0], hq[1], hq[2], hq[3]}, et == 0 ? rs : rs2, lane16, hoff + ((4 * rb + es) * 112 + ks0 + et) * 1024);
          if constexpr (et == 1) { if (idft) x3_piece(acc[0][1][es], rs, lane, OFF_X3 + (128 * rb + 32 * es) * XS + fs * 64); }
        }
        stream_issue<ks, (ks == 31 ? 2 : 0)>(W, hA, hB, x3, srd, lane16, dn);
      }
      if constexpr (edge) {
        BAR();
#pragma unroll
        for (int s = 0; s < 2; ++s) bf[(ks + 1) & 1][s] = tr2(zb[s] + (ks + 1) * 2048, 1024);
      }
      SB();
    });
    STAMP(8 + 2 * H);
    if constexpr (H == 0) BAR();
    STAMP(9 + 2 * H);
  });
  mfma_done(acc[1][0][0], acc[1][0][1], acc[1][1][0], acc[1][1][1]);
#pragma unroll
  for (int s = 0; s < 2; ++s) h_piece(acc[1][0][s], layer, rs, lane, hoff + ((4 * rb + 2 + s) * 112 + ks0) * 1024);
  if (two) {
#pragma unroll
    for (int s = 0; s < 2; ++s) h_piece(acc[1][1][s], layer, rs, lane, hoff + ((4 * rb + 2 + s) * 112 + ks0 + 1) * 1024);
  } else if (idft) {
#pragma unroll
    for (int s = 0; s < 2; ++s) x3_piece(acc[1][1][s], rs, lane, OFF_X3 + (128 * rb + 64 + 32 * s) * XS + fs * 64);
  }
  SB();
  stream_issue<32, 2>(W, hA, hB, x3, srd, lane16, dn);
  {
    auto x = __builtin_amdgcn_permlane32_swap(asu(ss[0]), asu(ss[1]), false, false);
    *(LAS float*)(L + M_S1 + w * 256 + lane * 4) = asf(x[0]) + asf(x[1]);
  }
  const int row = 16 * w + (lane >> 2);
  {
    const int n = nb * 64 + row;
    LAS unsigned char* ra = L + M_Z + n * 128 + (((4 * pi + (lane & 3)) ^ ((n & 2) << 1)) * 16);
    res[0] = *(LAS v4u*)ra; res[1] = *(LAS v4u*)(ra + 65536);
  }
  STAMP(12);
  CSTAMP(21);
  unsigned hm = 0;
  sfor<0, 7>([&](auto G) { h_acc<G.v, STR.end[32]>(hA, hB, hm, layer); });
  h_decide<0, STR.end[32]>(hA, hB, bit0_set(hm), srd, lane16, dn, L, layer, w);
  hm = 0;
  BAR();
  STAMP(13);
  LAS unsigned char* rw = L + M_Z + w * 9216 + r * 144 + h * 16;
  LAS unsigned char* rr = L + M_Z + row * 144 + (lane & 3) * 32;
  f32x16 dac[2][2];
  f32x4 fa[2][2], tq[2];
  auto red_write = [&](auto HH, auto K) {
    constexpr int H = HH.v, t = K.v >> 2, g = K.v & 3;
    *(LAS f32x4*)(rw + H * M_RB + t * 32 * 144 + g * 32) = f32x4{dac[H][t][4 * g], dac[H][t][4 * g + 1], dac[H][t][4 * g + 2], dac[H][t][4 * g + 3]};
  };
  sfor<0, 56>([&](auto GG) {
    constexpr int g = GG.v, H = g / 28, i = g % 28;
    v4u& hx = H == 0 ? hA[i] : hB[i];
    unsigned long long bad = 0ull;
    if constexpr (2 * i < NV_EARLY) mfma_v<i == 0>(dac[H][0], Ve[2 * i], hx);
    else mfma_w<false, false>(dac[H][0], W[2 * i - NV_EARLY], hx);
    SB();
    if constexpr (g + 7 < 56) h_acc<g + 7, STR.end[32 + g]>(hA, hB, hm, layer);
    if constexpr (g + 7 < 56 && g % 7 == 6) { bad = bit0_set(hm); pins(bad); hm = 0; }
    if constexpr (g == 1) {
      LAS float* sp = (LAS float*)(L + M_S1 + lane * 4);
      *(LAS float*)(L + M_S1 + 1024 + w * 256 + lane * 4) =
          __builtin_amdgcn_rcpf(__builtin_fmaf((sp[0] + sp[64]) + (sp[128] + sp[192]), 1.f / 512.f, 1e-5f));
    }
    if constexpr (g >= 51 && g <= 53) addf4(fa[0][0], tq[0]);
    SB();
    if constexpr (2 * i < NV_EARLY) mfma_v<i == 0>(dac[H][1], Ve[2 * i + 1], hx);
    else mfma_w<false, false>(dac[H][1], W[2 * i + 1 - NV_EARLY], hx);
    SB();
    if constexpr (g + 7 < 56 && g % 7 == 6) { pins(bad); h_decide<g + 1, STR.end[32 + g]>(hA, hB, bad, srd, lane16, dn, L, layer, w); }
    stream_issue<33 + g, 2>(W, hA, hB, x3, srd, lane16, dn);
    if constexpr (g >= 30 && g <= 46 && g % 2 == 0 && g != 34) red_write(IC<0>{}, IC<(g < 34 ? (g - 30) / 2 : (g - 32) / 2)>{});
    if constexpr (g == 47) BAR();
    if constexpr (g == 49) { fa[0][0] = *(LAS f32x4*)rr; fa[0][1] = *(LAS f32x4*)(rr + 16); }
    if constexpr (g >= 51 && g <= 53) addf4(fa[0][1], tq[1]);
    if constexpr (g >= 50 && g <= 52) { tq[0] = *(LAS f32x4*)(rr + (g - 49) * 9216); tq[1] = *(LAS f32x4*)(rr + (g - 49) * 9216 + 16); }
    SB();
    if constexpr (g == 27) STAMP(14);
  });
  STAMP(15);
  asm volatile("s_nop 12" : "+v"(dac[1][0]), "+v"(dac[1][1]));
  sfor<0, 8>([&](auto K) { red_write(IC<1>{}, K); });
  vmwait<0>();
  pin(x3[0]); pin(x3[1]);
  if (__builtin_expect(layer == 1 && any_sent(mxacc(mxacc(0u, x3[0]), x3[1])), 0)) {
    unsigned spin = 0;
    do {
      RETRY(29);
      ldx4<0>(x3[0], srd, dn.xvo, dn.xso); ldx4<0>(x3[1], srd, dn.xvo, dn.xso + 64 * XS);
      vmwait<0>();
      pin(x3[0]); pin(x3[1]);
    } while (any_sent(mxacc(mxacc(0u, x3[0]), x3[1])) && ++spin < SPIN_MAX);
  }
  STAMP(16);
  BAR();
  STAMP(17);
  fa[1][0] = *(LAS f32x4*)(rr + M_RB); fa[1][1] = *(LAS f32x4*)(rr + M_RB + 16);
#pragma unroll
  for (int ww = 1; ww < 4; ++ww) { fa[1][0] += *(LAS f32x4*)(rr + M_RB + ww * 9216); fa[1][1] += *(LAS f32x4*)(rr + M_RB + ww * 9216 + 16); }
  f32x4 sc[2][2];
#pragma unroll
  for (int H = 0; H < 2; ++H)
#pragma unroll
    for (int x = 0; x < 2; ++x) sc[H][x] = *(LAS f32x4*)(L + M_S1 + 1024 + w * 256 + H * 128 + (lane & 3) * 32 + x * 16);
  if (layer == 0) {
    const bool dc = nb == 0 && row == 0;
#pragma unroll
    for (int H = 0; H < 2; ++H) {
      unsigned o[4];
      float sq[8];
#pragma unroll
      for (int x = 0; x < 2; ++x)
        pk_tag(__builtin_fmaf(fa[H][x][0], sc[H][x][0], blo(res[H][2 * x])), __builtin_fmaf(fa[H][x][1], sc[H][x][1], bhi(res[H][2 * x])),
               __builtin_fmaf(fa[H][x][2], sc[H][x][2], blo(res[H][2 * x + 1])), __builtin_fmaf(fa[H][x][3], sc[H][x][3], bhi(res[H][2 * x + 1])), 0u,
               o[2 * x], o[2 * x + 1]);
      bstore_sc1(v4u{o[0], o[1], o[2], o[3]}, rs, row * ZS + (lane & 3) * 16, OFF_ZA + nb * 64 * ZS + pt * 64 + H * 128);
#pragma unroll
      for (int j = 0; j < 4; ++j) {
        const unsigned u = dc ? 0u : o[j];
        sq[2 * j] = blo(u) * blo(u); sq[2 * j + 1] = bhi(u) * bhi(u);
      }
      float t4[4], t2[2];
#pragma unroll
      for (int j = 0; j < 4; ++j) {
        auto x = __builtin_amdgcn_permlane32_swap(asu(sq[j]), asu(sq[j + 4]), false, false);
        t4[j] = asf(x[0]) + asf(x[1]);
      }
#pragma unroll
      for (int j = 0; j < 2; ++j) {
        auto x = __builtin_amdgcn_permlane16_swap(asu(t4[j]), asu(t4[j + 2]), false, false);
        t2[j] = asf(x[0]) + asf(x[1]);
        t2[j] += dppf<0x124>(t2[j]);
        t2[j] += dppf<0x128>(t2[j]);
      }
      *(LAS f32x2*)(L + M_S2 + w * 256 + H * 128 + (8 * (lane & 3) + 4 * h + 2 * ((lane >> 4) & 1)) * 4) = f32x2{t2[0], t2[1]};
    }
    BAR();
    if (w == 0) {
      LAS float* sp = (LAS float*)(L + M_S2 + lane * 4);
      const float t = (sp[0] + sp[64]) + (sp[128] + sp[192]);
      __builtin_amdgcn_raw_buffer_store_b32(nosent(asu(t)), rs, stat_of(h * 64 + r), OFF_ST + nb * 8192 + pt * 64, SC1);
    }
  } else {
    LAS unsigned char* tb = L + M_TB + w * 2560;
#pragma unroll
    for (int H = 0; H < 2; ++H) {
      const f32x4 a = fa[H][0] * sc[H][0], bq = fa[H][1] * sc[H][1];
      *(LAS v4u*)(tb + H * 1280 + (lane >> 2) * 80 + (lane & 3) * 16) = v4u{pk(a.x, a.y), pk(a.z, a.w), pk(bq.x, bq.y), pk(bq.z, bq.w)};
    }
#pragma unroll
    for (int H = 0; H < 2; ++H) {
      const v4u y = tr2(tb + H * 1280 + (8 * h + q) * 80 + ((lane >> 4) & 1) * 32 + (lane & 3) * 8, 4 * 80);
      v4u o;
#pragma unroll
      for (int j = 0; j < 4; ++j) o[j] = pk(blo(x3[H][j]) + blo(y[j]), bhi(x3[H][j]) + bhi(y[j]));
      const int tok = token_of(32 * (pt + 2 * H) + r);
      *(v4u*)(p.out + (size_t)tok * 512 + nb * 64 + 16 * w + 8 * h) = o;
    }
  }
  SB();
}

constexpr int CODE_BYTES = 44 * 1024;
constexpr int P_XL = 0, P_GL = 33280, P_DL = P_GL + 65536, P_ZB = P_DL + 33280, P_WB = P_ZB + 4 * 2560;
static_assert(P_WB + 64 * 144 <= M_C2 + 2 * 4608, "fits");

struct Late { v4u d[2], t[2]; int vo, so; rsrc_t rs; };
DI void prep(LAS unsigned char* L, const Params& p, rsrc_t rs, const v4u& srd, unsigned long long tag, unsigned touch0, Late& late) {
  const unsigned Lb = (unsigned)(size_t)L;
  const int tid = threadIdx.x, lane = tid & 63, w = __builtin_amdgcn_readfirstlane(tid >> 6);
  const int b = blockIdx.x, xcd = b & 7, q = (b >> 3) & 3, tgi = b >> 5;
  const int t1 = xcd * 8 + tgi, tg = xcd * 7 + tgi;
  const bool heavy = tgi < 7;
  const int up = tg >= 28, cb = tg - 28 * up;
  const int h = lane >> 5, r = lane & 31, ft = 4 * q + w;
  PST(0);
  {
    const v4u sx = make_srd(p.x, 2048u * 1024u);
    const int tok0 = token_of(32 * t1 + 2 * w);
#pragma unroll
    for (int i = 0; i < 8; ++i) {
      const int a = ((i >> 1) + q) & 3;
      glds_in(sx, lane * 16, (tok0 + a + 32 * (i & 1)) * 1024, Lb + P_XL + (8 * a + 2 * w + (i & 1)) * 1040);
    }
  }
  const unsigned hm = opaque((tgi - 7) >> 31);
  v4u sd = make_srd(p.wd, 1792u * 1024u & hm), sg = make_srd(up ? p.wu : p.wg, 512u * 3584u & hm);
  v4u sr = make_srd(p.wr, 2048u * 514u), si = make_srd(p.wi, 2048u * 514u);
  asm volatile("" : "+s"(sg));
  unsigned touch, touch2;
  {
    v4u st = w == 2 ? sd : w == 3 ? sr : sg;
    asm volatile("" : "+s"(st));
    const int so = w == 2 ? (32 * tg + 8 * q) * 1024 : w == 3 ? (32 * t1 * 257 + 64 * q) * 2 : (128 * q + 64 * w) * 3584 + 128 * cb;
    const int vo = lane * (w == 2 ? 128 : w == 3 ? 0 : 3584) + (w == 3 ? r * 514 : 0);
    asm volatile("buffer_load_ushort %0, %1, %2, %3 offen nt" : "=v"(touch) : "v"(vo), "s"(st), "s"(so));
    asm volatile("buffer_load_ushort %0, %1, %2, %3 offen nt" : "=v"(touch2) : "v"(r * 514), "s"(si), "s"((32 * t1 * 257 + 64 * q) * 2));
  }
  SB();
  const int feat = 32 * ft + r;
  const float step = (feat == 0 ? 0.f : feat == 1 ? 256.f : (float)(feat >> 1)) * (1.f / 512.f);
  const float ph = (feat > 1 && (feat & 1)) ? 0.25f : 0.f;
  const float a0 = ph + step * (float)(8 * h);
  const unsigned sgn = (feat > 1 && (feat & 2)) ? 0x80008000u : 0u;
  auto fill = [&](int i) { bstore_sc1(v4u{SENT, SENT, SENT, SENT}, rs, tid * 16, OFF_H + b * 28672 + i * 4096); };
  v4u F[32];
  sfor<0, 16>([&](auto KS) {
    constexpr int ks = KS.v;
    F[ks] = cosfrag(a0, step, 16 * ks); F[ks + 16] = F[ks] ^ sgn;
    pin(F[ks]); pin(F[ks + 16]);
    if constexpr (ks % 5 == 2) { SB(); fill(ks / 5); SB(); }
  });
  const float lsc = feat < 2 ? 0.04419417382415922f : 0.0625f;
  v4u I[2];
#pragma unroll
  for (int s = 0; s < 2; ++s) {
    const int e = r - 16 * s - 8 * h;
    const unsigned one = (e & 1) ? 0x3F800000u : 0x00003F80u;
    I[s] = v4u{(e >> 1) == 0 ? one : 0u, (e >> 1) == 1 ? one : 0u, (e >> 1) == 2 ? one : 0u, (e >> 1) == 3 ? one : 0u};
  }
  pin(I[0]); pin(I[1]);
  const int gvo = (lane >> 3) * 3584 + (((lane & 7) ^ ((lane >> 2) & 4)) * 16);
  const int fcol = 64 * q + lane, fvo = (fcol == 0 ? 256 : fcol) * 2, fso = (32 * t1 + 8 * w) * 514;
  if (!heavy) {
#pragma unroll
    for (int i = 0; i < 2; ++i) {
      const int dt = 2 * xcd + i;
      const float b0 = __builtin_amdgcn_fractf(__builtin_fmaf(step, (float)(32 * dt), a0));
      f32x16 aI;
      v4u c0 = cosfrag(b0, step, 0), c1 = cosfrag(b0, step, 16);
      m16_ready(c0, c1);
      mfma_vv<true>(aI, c0, I[0]);
      mfma_vv<false>(aI, c1, I[1]);
      asm volatile("s_nop 12" : "+v"(aI));
      if (ft == 0 && h == 0) { aI[0] *= 0.70710678118654752f; aI[1] *= 0.70710678118654752f; }
#pragma unroll
      for (int s = 0; s < 2; ++s) bstore_sc1(pack8(aI, s, 0.0625f), rs, lane * 16, OFF_W1 + (dt * 8 + 7) * 32768 + (2 * ft + s) * 1024);
    }
  }
  const rsrc_t rsh = __builtin_amdgcn_make_buffer_rsrc((void*)p.ws, 0, WS_BYTES & hm, 0x00020000);
  f32x16 aZ;
  unsigned fr[8], fi[8];
  v4u zo[2], su[2], wo[2];
  unsigned zd[8];
  float sv_[16], ssum;
  auto z_out = [&](auto STEP) {
    constexpr int n = STEP.v;
    LAS unsigned char* zb = L + P_ZB + w * 2560;
    if constexpr (n < 4) {
      pk_tag(aZ[4 * n] * lsc, aZ[4 * n + 1] * lsc, aZ[4 * n + 2] * lsc, aZ[4 * n + 3] * lsc, 0u, zd[2 * n], zd[2 * n + 1]);
      pinu(zd[2 * n]); pinu(zd[2 * n + 1]);
    }
    if constexpr (n == 4 || n == 5) {
      auto r0 = __builtin_amdgcn_permlane32_swap(zd[4 * (n - 4)], zd[4 * (n - 4) + 2], false, false);
      auto r1 = __builtin_amdgcn_permlane32_swap(zd[4 * (n - 4) + 1], zd[4 * (n - 4) + 3], false, false);
      *(LAS v4u*)(zb + r * 80 + h * 16 + 32 * (n - 4)) = v4u{r0[0], r1[0], r0[1], r1[1]};
    }
    if constexpr (n == 6) { zo[0] = *(LAS v4u*)(zb + (lane >> 2) * 80 + (lane & 3) * 16); zo[1] = *(LAS v4u*)(zb + (16 + (lane >> 2)) * 80 + (lane & 3) * 16); }
    if constexpr (n == 7 || n == 8) bstore_sc1(zo[n - 7], rs, (32 * ft + 16 * (n - 7) + (lane >> 2)) * ZS + (lane & 3) * 16, OFF_ZA + t1 * 64);
  };
  auto stat = [&](auto STEP) {
    constexpr int n = STEP.v;
    const int row = 8 * q + 2 * w + h;
    if constexpr (n == 0) { su[0] = *(LAS v4u*)(L + P_XL + row * 1040 + r * 32); su[1] = *(LAS v4u*)(L + P_XL + row * 1040 + r * 32 + 16); ssum = 0.f; }
    if constexpr (n >= 1 && n <= 4) {
      constexpr int k = n - 1;
      sv_[4 * k] = blo(su[k >> 1][2 * (k & 1)]); sv_[4 * k + 1] = bhi(su[k >> 1][2 * (k & 1)]);
      sv_[4 * k + 2] = blo(su[k >> 1][2 * (k & 1) + 1]); sv_[4 * k + 3] = bhi(su[k >> 1][2 * (k & 1) + 1]);
      ssum += (sv_[4 * k] + sv_[4 * k + 1]) + (sv_[4 * k + 2] + sv_[4 * k + 3]);
    }
    if constexpr (n == 5) { const float m = sum32(ssum) * (1.f / 512.f); ssum = m; }
    if constexpr (n >= 6 && n <= 9) {
#pragma unroll
      for (int i = 4 * (n - 6); i < 4 * (n - 6) + 4; ++i) { sv_[i] -= ssum; pinf(sv_[i]); }
    }
    if constexpr (n == 10) ssum = 0.f;
    if constexpr (n >= 10 && n <= 12) {
#pragma unroll
      for (int i = 6 * (n - 10); i < 6 * (n - 10) + 6 && i < 16; ++i) ssum = __builtin_fmaf(sv_[i], sv_[i], ssum);
    }
    if constexpr (n == 13) ssum = sum32(ssum);
    if constexpr (n == 14) { if (r == 0) __builtin_amdgcn_raw_buffer_store_b32(asu(ssum), rs, stat_of(32 * t1 + row), OFF_R0, SC1); }
    pinf(ssum);
  };
  auto f_out = [&](auto N, const f32x16& a, float sc, v4u& o, int vo, int so) {
    constexpr int k = 8 * (N.v >> 1) + 4 * (N.v & 1);
    o[2 * (N.v & 1)] = pk(a[k] * sc, a[k + 1] * sc); o[2 * (N.v & 1) + 1] = pk(a[k + 2] * sc, a[k + 3] * sc);
    if constexpr (N.v & 1) bstore_sc1(o, rsh, vo, so + (N.v >> 1) * 1024); else pin(o);
  };
  auto filt = [&](auto STEP) {
    constexpr int n = STEP.v;
    if constexpr (n == 0) {
      asm volatile("" : "+v"(fr[0]), "+v"(fr[1]), "+v"(fr[2]), "+v"(fr[3]), "+v"(fr[4]), "+v"(fr[5]), "+v"(fr[6]), "+v"(fr[7]),
                        "+v"(fi[0]), "+v"(fi[1]), "+v"(fi[2]), "+v"(fi[3]), "+v"(fi[4]), "+v"(fi[5]), "+v"(fi[6]), "+v"(fi[7]));
      v4u o0, o1;
#pragma unroll
      for (int i = 0; i < 4; ++i) { o0[i] = fr[i] | (fi[i] << 16); o1[i] = fr[4 + i] | (fi[4 + i] << 16); }
      *(LAS v4u*)(L + P_WB + lane * 144 + w * 32) = o0;
      *(LAS v4u*)(L + P_WB + lane * 144 + w * 32 + 16) = o1;
    }
    if constexpr (n == 1) {
#pragma unroll
      for (int i = 0; i < 2; ++i) wo[i] = *(LAS v4u*)(L + P_WB + (16 * w + 8 * i + (lane >> 3)) * 144 + (lane & 7) * 16);
    }
    if constexpr (n >= 2) bstore_sc1(wo[n - 2], rs, (64 * q + 16 * w + 8 * (n - 2) + (lane >> 3)) * WS_ + (lane & 7) * 16, OFF_WT + t1 * 128);
  };
  auto gu = [&](int i) {
    const int blk = 16 * (((i >> 2) + q) & 3) + 4 * w + (i & 3);
    glds_in(sg, gvo, blk * 8 * 3584 + cb * 128, Lb + P_GL + blk * 1024);
  };
#pragma unroll
  for (int i = 0; i < 6; ++i) gu(i);
  fill(3);
  PST(1);
  vmwait<12>();
  BAR();
  PST(2);
  {
    LAS unsigned char* xa = L + P_XL + r * 1040 + h * 16;
    v4u a[4];
#pragma unroll
    for (int i = 0; i < 4; ++i) a[i] = *(LAS v4u*)(xa + i * 32);
    sfor<0, 32>([&](auto KS) {
      constexpr int ks = KS.v;
      mfma_vv<ks == 0>(aZ, a[ks & 3], F[ks]);
      SB();
      if constexpr (ks + 4 < 32) a[ks & 3] = *(LAS v4u*)(xa + (ks + 4) * 32);
      if constexpr (ks < 20 && !(ks & 1)) gu(6 + ks / 2);
      if constexpr (ks >= 20 && ks < 28) ld2u16<(ks - 20) * 514>(fr[ks - 20], fi[ks - 20], fvo, sr, si, fso);
      if constexpr ((ks & 1) && ks < 30) stat(IC<(ks >> 1)>{});
      if constexpr (ks == 8 || ks == 19 || ks == 30) fill(4 + ks / 11);
      SB();
    });
  }
  asm volatile("s_nop 12\n\ts_waitcnt vmcnt(16)" : "+v"(aZ) : "v"(touch), "v"(touch2), "v"(touch0) : "memory");
  PST(3);
  BAR();
  PST(4);
  f32x16 aG0, aG1;
  {
    const int q4 = (lane & 15) >> 2;
    LAS unsigned char* ba = L + P_GL + (8 * h + q4) * 128 + (4 * (q4 >> 1) + 2 * ((lane >> 4) & 1) + ((lane & 3) >> 1)) * 16 + 8 * (lane & 1);
    LAS unsigned char* bb = L + P_GL + (8 * h + q4) * 128 + (4 * (1 - (q4 >> 1)) + 2 * ((lane >> 4) & 1) + ((lane & 3) >> 1)) * 16 + 8 * (lane & 1);
    v4u b0[2], b1[2];
#pragma unroll
    for (int i = 0; i < 2; ++i) { b0[i] = tr2(ba + i * 2048, 512); b1[i] = tr2(bb + i * 2048, 512); }
    sfor<0, 32>([&](auto KS) {
      constexpr int ks = KS.v;
      mfma_vv<ks == 0>(aG0, F[ks], b0[ks & 1]);
      SB();
      if constexpr (ks + 2 < 32) b0[ks & 1] = tr2(ba + (ks + 2) * 2048, 512);
      if constexpr (ks < 9) z_out(KS);
      if constexpr (ks >= 11 && ks < 14) filt(IC<ks - 10>{});
      if constexpr (ks == 25) publish(rs, 0, b, tag, w, lane);
      SB();
      mfma_vv<ks == 0>(aG1, F[ks], b1[ks & 1]);
      SB();
      if constexpr (ks + 2 < 32) b1[ks & 1] = tr2(bb + (ks + 2) * 2048, 512);
      if constexpr (ks == 9) { vmwait<8>(); filt(IC<0>{}); }
      if constexpr (ks >= 1 && ks < 9) {
        const int row = 8 * ((((ks - 1) >> 1) + q) & 3) + 2 * w + ((ks - 1) & 1);
        glds_in(sd, lane * 16, (32 * tg + row) * 1024, Lb + P_DL + row * 1040);
      }
      if constexpr (ks == 10) BAR();
      if constexpr (ks == 24) { vmwait<0>(); asm volatile("s_barrier" ::: "memory"); }
      SB();
    });
  }
  PST(5);
  f32x16 aD, aT;
  v4u fl[2];
  {
    const int g0 = 4 * cb + (r >> 4), g1 = g0 + 2, slot = ((r & 15) + 16 * up + 32 * h) * 16;
    const int o0 = ((g0 / 7) * 8 + g0 % 7) * 32768 + slot, o1 = ((g1 / 7) * 8 + g1 % 7) * 32768 + slot;
    v4u fo = {0u, 0u, 0u, 0u};
    LAS unsigned char* da = L + P_DL + r * 1040 + h * 16;
    const v4u t0 = *(LAS v4u*)(da + ft * 64), t1_ = *(LAS v4u*)(da + ft * 64 + 32);
    v4u a[4];
#pragma unroll
    for (int i = 0; i < 4; ++i) a[i] = *(LAS v4u*)(da + i * 32);
    mfma_vv<true>(aT, t0, I[0]);
    mfma_vv<false>(aT, t1_, I[1]);
    sfor<0, 32>([&](auto KS) {
      constexpr int ks = KS.v;
      mfma_vv<ks == 0>(aD, a[ks & 3], F[ks]);
      SB();
      if constexpr (ks + 4 < 32) a[ks & 3] = *(LAS v4u*)(da + (ks + 4) * 32);
      if constexpr (ks == 0) {
        const bool edge = ft == 0 && h == 0;
        aG0[0] = edge ? 0.f : aG0[0]; aG1[0] = edge ? 0.f : aG1[0];
        aG0[1] *= edge ? 0.70710678118654752f : 1.f; aG1[1] *= edge ? 0.70710678118654752f : 1.f;
      }
      if constexpr (ks >= 1 && ks < 5) f_out(IC<ks - 1>{}, aG0, 0.0625f, fo, o0, OFF_W1 + 2 * ft * 1024);
      if constexpr (ks >= 5 && ks < 9) f_out(IC<ks - 5>{}, aG1, 0.0625f, fo, o1, OFF_W1 + 2 * ft * 1024);
      if constexpr (ks == 10 || ks == 12) { late.t[ks == 12] = pack8(aT, ks == 12, 1.f); pin(late.t[ks == 12]); }
      if constexpr (ks == 22) flags_load(fl, srd, 0, b, lane);
      SB();
    });
  }
  PST(6);
  asm volatile("s_nop 12\n\ts_waitcnt vmcnt(0)\n\ts_barrier" : "+v"(aD) :: "memory");
  publish(rs, 1, b, tag, w, lane);
  late.d[0] = pack8(aD, 0, lsc); late.d[1] = pack8(aD, 1, lsc);
  late.vo = lane * 16; late.so = (ft * 112 + 2 * tg) * 1024; late.rs = rsh;
  PST(7);
  flags_wait(fl, srd, 0, b, lane, tag);
  PST(8);
}

struct Tile {
  int rb, fs, pi, nb;
  DI Tile(int c) { rb = 4 * (c & 3) + (c >> 6); fs = 8 * ((c >> 2) & 1) + ((c >> 3) & 7); pi = fs >> 3; nb = fs & 7; }
};

template <int layer>
DI void column(LAS unsigned char* L, rsrc_t rs, const v4u& srd, v4u (&W)[64], unsigned long long tag, const Late& late) {
  int tid = threadIdx.x;
  asm volatile("" : "+v"(tid));
  const int c = opaque(blockIdx.x);
  const int lane = tid & 63, w = __builtin_amdgcn_readfirstlane(tid >> 6);
  WSrc ws;
  ws.srd = srd; ws.srd1 = srd;
  if (w == 3 && layer == 0) ws.srd1.z = 0u;
  ws.lane16 = lane * 16; ws.base = OFF_W1 + (Tile(c).fs * 8 + 2 * w) * 32768;
  unsigned wf[8];
  const int wfvo = (16 * w + (lane & 15) + 64 * 4 * (lane >> 4)) * 4;
  auto filter_in = [&]() {
    asm volatile("buffer_load_dword %0, %8, %9, %10 offen sc1\n\tbuffer_load_dword %1, %8, %9, %10 offen offset:256 sc1\n\t"
                 "buffer_load_dword %2, %8, %9, %10 offen offset:512 sc1\n\tbuffer_load_dword %3, %8, %9, %10 offen offset:768 sc1\n\t"
                 "buffer_load_dword %4, %8, %9, %11 offen sc1\n\tbuffer_load_dword %5, %8, %9, %11 offen offset:256 sc1\n\t"
                 "buffer_load_dword %6, %8, %9, %11 offen offset:512 sc1\n\tbuffer_load_dword %7, %8, %9, %11 offen offset:768 sc1"
                 : "=&v"(wf[0]), "=&v"(wf[1]), "=&v"(wf[2]), "=&v"(wf[3]), "=&v"(wf[4]), "=&v"(wf[5]), "=&v"(wf[6]), "=&v"(wf[7])
                 : "v"(wfvo), "s"(srd), "s"(OFF_WT + c * WS_), "s"(OFF_WT + c * WS_ + 4096));
  };
  float rstd[8], fr[8], fi[8];
  auto filter_unpack = [&]() {
    asm volatile("" : "+v"(wf[0]), "+v"(wf[1]), "+v"(wf[2]), "+v"(wf[3]), "+v"(wf[4]), "+v"(wf[5]), "+v"(wf[6]), "+v"(wf[7]));
#pragma unroll
    for (int e = 0; e < 8; ++e) { fr[e] = blo(wf[e]) * (1.f / 2048.f); fi[e] = bhi(wf[e]) * (1.f / 2048.f); pinf(fr[e]); pinf(fi[e]); }
  };
  STAMP(0);
  CSTAMP(22);
  v4u zr, zi, sa, sb;
  const int first = is_zero(c);
  ColConst k;
  if constexpr (layer == 0) {
    v4u fl[2];
    ldx4<0>(zr, srd, tid * 16, OFF_ZA + (2 * c + first) * ZS); ldx4<0>(zi, srd, tid * 16, OFF_ZA + (2 * c + 1 - first) * ZS);
    ldx4<0>(sa, srd, tid * 16, OFF_R0); ldx4<0>(sb, srd, tid * 16, OFF_R0 + 4096);
    filter_in();
    SB();
#pragma unroll
    for (int i = 0; i < 2; ++i) {
      bstore_sc1(late.d[i], late.rs, late.vo, OFF_W2 + late.so + i * 1024);
      bstore_sc1(late.t[i], late.rs, late.vo, OFF_W3 + late.so + i * 1024);
    }
    if (tid < 16) bstore_sc1(v4u{SENT, SENT, SENT, SENT}, rs, tid * 16, OFF_ST + c * 256);
    if (c == 0 && tid == 0) __builtin_amdgcn_raw_buffer_store_b64(v2u{(unsigned)tag, (unsigned)(tag >> 32)}, rs, OFF_CTL, 0, SC1);
    SB();
    col_consts<false>(k, w, lane, W, ws);
    SB();
    flags_load(fl, srd, 1, c, lane);
    STAMP(1);
    vmwait<2>();
    pin(zr); pin(zi); pin(sa); pin(sb);
    filter_unpack();
    vmwait<0>();
    flags_wait(fl, srd, 1, c, lane, tag);
    load_ws<0, 6>(W, ws);
  } else {
    filter_in();
    SB();
    col_consts<true>(k, w, lane, W, ws);
    SB();
    vmwait<16>();
    filter_unpack();
    STAMP(1);
    __builtin_amdgcn_s_sleep(DELAY_C);
    unsigned spin = 0;
    bool again;
    do {
      RETRY(28);
      v4u st[16];
      ldx4<0>(zr, srd, tid * 16, OFF_ZA + (2 * c + first) * ZS); ldx4<0>(zi, srd, tid * 16, OFF_ZA + (2 * c + 1 - first) * ZS);
      sfor<0, 16>([&](auto I) { ldx4<0>(st[I.v], srd, tid * 16, OFF_ST + I.v * 4096); });
      vmwait<0>();
      pin(zr); pin(zi);
#pragma unroll
      for (int i = 0; i < 16; ++i) pin(st[i]);
      f32x4 a = __builtin_bit_cast(f32x4, st[0]), bq = __builtin_bit_cast(f32x4, st[1]);
#pragma unroll
      for (int n = 1; n < 8; ++n) { a += __builtin_bit_cast(f32x4, st[2 * n]); bq += __builtin_bit_cast(f32x4, st[2 * n + 1]); }
      sa = __builtin_bit_cast(v4u, a); sb = __builtin_bit_cast(v4u, bq);
      const f32x4 t = a + bq;
      const float u = (t.x + t.y) + (t.z + t.w);
      unsigned m = 0u;
      if (__builtin_expect(__builtin_amdgcn_ballot_w64(u != u) != 0ull, 0)) {
#pragma unroll
        for (int i = 0; i < 16; ++i) m = mxacc(m, st[i]);
      }
      again = (__builtin_amdgcn_ballot_w64(m == SENT) | bit0_set(tagacc(tagacc(0u, zr, 0u), zi, 0u))) != 0ull;
    } while (__builtin_expect(again, 0) && ++spin < SPIN_MAX);
  }
  constexpr int W0 = layer == 0 ? 6 : 16;
  load_ws<W0, W0 + 2>(W, ws);
#pragma unroll
  for (int e = 0; e < 4; ++e) {
    rstd[e] = __builtin_amdgcn_rsqf(__builtin_fmaf(asf(sa[e]), 1.f / 512.f, 1e-5f));
    rstd[4 + e] = __builtin_amdgcn_rsqf(__builtin_fmaf(asf(sb[e]), 1.f / 512.f, 1e-5f));
  }
  load_ws<W0 + 2, W0 + 4>(W, ws);
  STAMP(2);
  col_compute<W0 + 4>(L, rs, k, fr, fi, zr, zi, rstd, c, w, lane, layer == 0 ? OFF_ZA : OFF_ZB, W, ws);
  STAMP(3);
  CSTAMP(23);
  load_ws<W0 + 34, 64 - NW_LATE>(W, ws);
  if constexpr (layer == 1) __builtin_amdgcn_s_sleep(DELAY_A);
}

__global__ void __launch_bounds__(256, 1) k_main(Params p) {
  extern __shared__ __attribute__((aligned(16))) unsigned char smem[];
  LAS unsigned char* L = (LAS unsigned char*)smem;
  const rsrc_t rs = __builtin_amdgcn_make_buffer_rsrc((void*)p.ws, 0, WS_BYTES, 0x00020000);
  const v4u srd = make_srd(p.ws, WS_BYTES);
  unsigned touch0;
  {
    unsigned lo, hi;
    asm volatile("s_getpc_b64 s[20:21]\n\ts_add_u32 %0, s20, _Z6k_main6Params@rel32@lo+4\n\ts_addc_u32 %1, s21, _Z6k_main6Params@rel32@hi+12"
                 : "=s"(lo), "=s"(hi) :: "s20", "s21", "scc");
    const int off = threadIdx.x * 128 + (threadIdx.x >= 128 ? (blockIdx.x & 8) * 2048 : 0);
    asm volatile("global_load_dword %0, %1, %2" : "=v"(touch0) : "v"(off < CODE_BYTES ? off : 0), "s"(((unsigned long long)hi << 32) | lo));
    SB();
  }
  unsigned long long tag = *(const unsigned long long*)(p.ws + OFF_CTL) + 0x9E3779B97F4A7C15ull;
  tag = (tag ^ (tag >> 30)) * 0xBF58476D1CE4E5B9ull;
  tag = (tag ^ (tag >> 27)) * 0x94D049BB133111EBull;
  tag ^= tag >> 31;
  Late late;
  prep(L, p, rs, srd, tag, touch0, late);
  v4u W[64];
  column<0>(L, rs, srd, W, tag, late);
#pragma unroll 1
  for (int layer = 0; layer < 2; ++layer) {
    int tid = threadIdx.x;
    asm volatile("" : "+v"(tid));
    const int c = opaque(blockIdx.x);
    const int lane = tid & 63, w = __builtin_amdgcn_readfirstlane(tid >> 6);
    const Tile t(c);
    WSrc ws;
    ws.srd = srd; ws.srd1 = srd;
    if (w == 3 && layer == 0) ws.srd1.z = 0u;
    ws.lane16 = lane * 16; ws.base = OFF_W1 + (t.fs * 8 + 2 * w) * 32768;
    ffn(L, rs, ws, p, W, layer, c, t.rb, t.fs, t.pi, t.nb, w, lane);
    STAMP(18);
    if (layer == 0) column<1>(L, rs, srd, W, tag, late);
  }
}

extern "C" void kernel_launch(void* const* d_in, const int* in_sizes, int n_in, void* d_out, int out_size, void* d_ws, size_t ws_size,
                              hipStream_t stream) {
  Params p;
  p.x = (const u16*)d_in[0]; p.wr = (const u16*)d_in[1]; p.wi = (const u16*)d_in[2];
  p.wg = (const u16*)d_in[4]; p.wu = (const u16*)d_in[5]; p.wd = (const u16*)d_in[6];
  p.out = (u16*)d_out; p.ws = (unsigned char*)d_ws;
  (void)hipFuncSetAttribute((const void*)k_main, hipFuncAttributeMaxDynamicSharedMemorySize, M_LDS);
  k_main<<<256, 256, M_LDS, stream>>>(p);
}
```

```cpp
#include <hip/hip_runtime.h>
#include <hip/hip_bf16.h>

#define LAS __attribute__((address_space(3)))
#define DI __device__ __forceinline__
typedef unsigned short u16;
typedef unsigned v4u __attribute__((ext_vector_type(4)));
typedef unsigned v2u __attribute__((ext_vector_type(2)));
typedef float f32x2 __attribute__((ext_vector_type(2)));
typedef float f32x4 __attribute__((ext_vector_type(4)));
typedef float f32x16 __attribute__((ext_vector_type(16)));
typedef short s16x4 __attribute__((ext_vector_type(4)));
typedef __bf16 bf16x2 __attribute__((ext_vector_type(2)));
typedef __bf16 bf16x8 __attribute__((ext_vector_type(8)));
typedef __amdgpu_buffer_rsrc_t rsrc_t;

constexpr int MB = 1 << 20;
constexpr int ZS = 4224, XS = 1152, WS_ = 8320;
constexpr int OFF_ZA = 0, OFF_ZB = 9 * MB / 4;
constexpr int OFF_WT = 9 * MB;
constexpr int OFF_W1 = 12 * MB;
constexpr int OFF_W2 = 16 * MB;
constexpr int OFF_W3 = 18 * MB;
constexpr int OFF_H = 20 * MB;
constexpr int OFF_X3 = 36 * MB;
constexpr int OFF_R0 = 39 * MB;
constexpr int OFF_ST = 39 * MB + 65536;
constexpr int OFF_CTL = 39 * MB + 2 * 65536;
constexpr int OFF_FL = OFF_CTL + 4096;
constexpr int WS_BYTES = 40 * MB;
constexpr int SC1 = 16;
constexpr unsigned SENT = 0xFFFFFFFFu;
constexpr unsigned SPIN_MAX = 1u << 16;

struct Params {
  const u16* x; const u16* wr; const u16* wi; const u16* wg; const u16* wu; const u16* wd;
  u16* out; unsigned char* ws;
};

DI unsigned pk(float a, float b) { f32x2 v = {a, b}; return __builtin_bit_cast(unsigned, __builtin_convertvector(v, bf16x2)); }
DI float asf(unsigned u) { return __builtin_bit_cast(float, u); }
DI unsigned asu(float f) { return __builtin_bit_cast(unsigned, f); }
DI bf16x2 asb2(unsigned u) { return __builtin_bit_cast(bf16x2, u); }
template <int V> struct IC { static constexpr int v = V; };
template <int I, int N, class Fn> DI void sfor(Fn&& fn) { if constexpr (I < N) { fn(IC<I>{}); sfor<I + 1, N>(fn); } }
DI int opaque(int x) { asm volatile("" : "+s"(x)); return x; }
DI int is_zero(int x) { int n; asm volatile("s_min_u32 %0, %1, 1" : "=s"(n) : "s"(x) : "scc"); return 1 - n; }
DI float blo(unsigned u) { return __builtin_bit_cast(float, u << 16); }
DI float bhi(unsigned u) { return __builtin_bit_cast(float, u & 0xFFFF0000u); }
DI float rbf(float a) { return blo(pk(a, a)); }
DI void pk_tag(float a0, float b0, float a1, float b1, unsigned tag, unsigned& d0, unsigned& d1) {
  const unsigned c = pk(b0, b1), up = (tag ^ 1u) << 16;
  d0 = (__builtin_amdgcn_perm(c, asu(a0) + up, 0x05040302u) & ~1u) | tag;
  d1 = (__builtin_amdgcn_perm(c, asu(a1) + up, 0x07060302u) & ~1u) | tag;
}
DI unsigned nosent(unsigned u) { return u < 0xFFFFFFFEu ? u : 0xFFFFFFFEu; }
DI v4u nosent4(v4u v) { v.x = nosent(v.x); v.y = nosent(v.y); v.z = nosent(v.z); v.w = nosent(v.w); return v; }
DI unsigned umax(unsigned a, unsigned b) { return a > b ? a : b; }
DI unsigned mx4(v4u v) { return umax(umax(v.x, v.y), umax(v.z, v.w)); }
DI bool any_sent(unsigned m) { return __builtin_amdgcn_ballot_w64(m == SENT) != 0ull; }
DI f32x16 mfma32(v4u a, v4u b, f32x16 c) {
  return __builtin_amdgcn_mfma_f32_32x32x16_bf16(__builtin_bit_cast(bf16x8, a), __builtin_bit_cast(bf16x8, b), c, 0, 0, 0);
}
DI f32x4 mfma16(v4u a, v4u b, f32x4 c) {
  return __builtin_amdgcn_mfma_f32_16x16x32_bf16(__builtin_bit_cast(bf16x8, a), __builtin_bit_cast(bf16x8, b), c, 0, 0, 0);
}
template <bool FIRST, bool WA> DI void mfma_w(f32x16& acc, const v4u& wt, const v4u& x) {
  if constexpr (FIRST && WA) asm volatile("v_mfma_f32_32x32x16_bf16 %0, %1, %2, 0" : "=&v"(acc) : "a"(wt), "v"(x));
  if constexpr (FIRST && !WA) asm volatile("v_mfma_f32_32x32x16_bf16 %0, %2, %1, 0" : "=&v"(acc) : "a"(wt), "v"(x));
  if constexpr (!FIRST && WA) asm volatile("v_mfma_f32_32x32x16_bf16 %0, %1, %2, %0" : "+v"(acc) : "a"(wt), "v"(x));
  if constexpr (!FIRST && !WA) asm volatile("v_mfma_f32_32x32x16_bf16 %0, %2, %1, %0" : "+v"(acc) : "a"(wt), "v"(x));
}
template <bool FIRST> DI void mfma_v(f32x16& acc, const v4u& wt, const v4u& x) {
  if constexpr (FIRST) asm volatile("v_mfma_f32_32x32x16_bf16 %0, %2, %1, 0" : "=&v"(acc) : "v"(wt), "v"(x));
  else asm volatile("v_mfma_f32_32x32x16_bf16 %0, %2, %1, %0" : "+v"(acc) : "v"(wt), "v"(x));
}
template <bool FIRST> DI void mfma_vv(f32x16& acc, const v4u& a, const v4u& b) {
  if constexpr (FIRST) asm volatile("v_mfma_f32_32x32x16_bf16 %0, %1, %2, 0" : "=&v"(acc) : "v"(a), "v"(b));
  else asm volatile("v_mfma_f32_32x32x16_bf16 %0, %1, %2, %0" : "+v"(acc) : "v"(a), "v"(b));
}
template <int IMM> DI void vload(v4u& dst, const v4u& srd, int lane16, int so) {
  asm volatile("buffer_load_dwordx4 %0, %1, %2, %3 offen offset:%4 sc1" : "=v"(dst) : "v"(lane16), "s"(srd), "s"(so), "i"(IMM));
}
template <int IMM, bool REUSE> DI void wload(v4u& dst, const v4u& srd, int lane16, int so) {
  if constexpr (REUSE) asm volatile("buffer_load_dwordx4 %0, %1, %2, %3 offen offset:%4 sc1" : "+a"(dst) : "v"(lane16), "s"(srd), "s"(so), "i"(IMM));
  else asm volatile("buffer_load_dwordx4 %0, %1, %2, %3 offen offset:%4 sc1" : "=a"(dst) : "v"(lane16), "s"(srd), "s"(so), "i"(IMM));
}
DI void mfma_done(f32x16& a, f32x16& b, f32x16& c, f32x16& d) { asm volatile("s_nop 12" : "+v"(a), "+v"(b), "+v"(c), "+v"(d)); }
DI v2u trread(LAS unsigned char* p) { return __builtin_bit_cast(v2u, __builtin_amdgcn_ds_read_tr16_b64_v4i16((LAS s16x4*)p)); }
DI v4u tr2(LAS unsigned char* p, int d) { v2u a = trread(p), b = trread(p + d); return v4u{a.x, a.y, b.x, b.y}; }
DI v4u bload(rsrc_t rs, int voff, int soff) { return __builtin_amdgcn_raw_buffer_load_b128(rs, voff, soff, 0); }
DI v4u bload_sc1(rsrc_t rs, int voff, int soff) { return __builtin_amdgcn_raw_buffer_load_b128(rs, voff, soff, SC1); }
DI void bstore(v4u v, rsrc_t rs, int voff, int soff) { __builtin_amdgcn_raw_buffer_store_b128(v, rs, voff + soff, 0, 0); }
DI void bstore_sc1(v4u v, rsrc_t rs, int voff, int soff) { __builtin_amdgcn_raw_buffer_store_b128(v, rs, voff + soff, 0, SC1); }
template <int I> DI v4u fload(rsrc_t rs, int lane16, int base) { return bload(rs, lane16 + (I & 3) * 1024, base + (I >> 2) * 4096); }
template <int I> DI v4u fload_sc1(rsrc_t rs, int lane16, int base) { return bload_sc1(rs, lane16 + (I & 3) * 1024, base + (I >> 2) * 4096); }
#define SB() __builtin_amdgcn_sched_barrier(0)
#define MEMFENCE() asm volatile("" ::: "memory")
#define BAR() asm volatile("s_waitcnt lgkmcnt(0)\n\ts_barrier" ::: "memory")
template <int N> DI void vmwait() { asm volatile("s_waitcnt vmcnt(%0)" :: "i"(N) : "memory"); }
DI void pin(v4u& a) { asm volatile("" : "+v"(a)); }
DI f32x16 zero16() { f32x16 z; for (int i = 0; i < 16; ++i) z[i] = 0.f; return z; }
DI v4u pack8(const f32x16& a, int s, float sc) {
  return v4u{pk(a[8 * s] * sc, a[8 * s + 1] * sc), pk(a[8 * s + 2] * sc, a[8 * s + 3] * sc),
             pk(a[8 * s + 4] * sc, a[8 * s + 5] * sc), pk(a[8 * s + 6] * sc, a[8 * s + 7] * sc)};
}
DI void widen(const f32x16& a, float sc, v4u& o0, v4u& o1) {
  unsigned d[8];
#pragma unroll
  for (int i = 0; i < 8; ++i) d[i] = pk(a[2 * i] * sc, a[2 * i + 1] * sc);
  auto r0 = __builtin_amdgcn_permlane32_swap(d[0], d[2], false, false);
  auto r1 = __builtin_amdgcn_permlane32_swap(d[1], d[3], false, false);
  auto r2 = __builtin_amdgcn_permlane32_swap(d[4], d[6], false, false);
  auto r3 = __builtin_amdgcn_permlane32_swap(d[5], d[7], false, false);
  o0 = v4u{r0[0], r1[0], r0[1], r1[1]};
  o1 = v4u{r2[0], r3[0], r2[1], r3[1]};
}
template <int CTRL> DI float dppf(float v) {
  return __builtin_bit_cast(float, __builtin_amdgcn_update_dpp(0, __builtin_bit_cast(int, v), CTRL, 0xF, 0xF, true));
}
DI float sum32(float v) {
  v += dppf<0xB1>(v); v += dppf<0x4E>(v); v += dppf<0x141>(v); v += dppf<0x140>(v);
  unsigned u = __builtin_bit_cast(unsigned, v);
  auto r = __builtin_amdgcn_permlane16_swap(u, u, false, false);
  return __builtin_bit_cast(float, (unsigned)r[0]) + __builtin_bit_cast(float, (unsigned)r[1]);
}
DI int stat_of(int p) { return ((p >> 2) & 1) * 4096 + (p >> 3) * 16 + (p & 3) * 4; }
DI int token_of(int p) {
  const int l = (p >> 3) & 63, e = p & 7;
  return 32 * (16 * (p >> 9) + 4 * (l >> 4) + (e & 3)) + (l & 15) + 16 * (e >> 2);
}
DI v4u make_srd(const void* base, unsigned bytes) { return v4u{(unsigned)(size_t)base, (unsigned)((size_t)base >> 32) & 0xFFFFu, bytes, 0x00020000u}; }
DI void glds_in(const v4u& srd, int voff, int soff, unsigned lds) {
  asm volatile("s_mov_b32 m0, %3\n\ts_nop 0\n\tbuffer_load_dwordx4 %0, %1, %2 offen nt lds" :: "v"(voff), "s"(srd), "s"(soff), "s"(lds) : "memory");
}
template <int IMM> DI void ld2u16(unsigned& a, unsigned& b, int voff, const v4u& sa, const v4u& sb, int soff) {
  asm volatile("buffer_load_ushort %0, %2, %3, %5 offen offset:%6\n\tbuffer_load_ushort %1, %2, %4, %5 offen offset:%6"
               : "=&v"(a), "=&v"(b) : "v"(voff), "s"(sa), "s"(sb), "s"(soff), "i"(IMM));
}
DI v4u cosfrag(float a0, float step, int off) {
  float v[8];
#pragma unroll
  for (int j = 0; j < 8; ++j) v[j] = __builtin_amdgcn_cosf(__builtin_fmaf(step, (float)(off + j), a0));
  return v4u{pk(v[0], v[1]), pk(v[2], v[3]), pk(v[4], v[5]), pk(v[6], v[7])};
}

constexpr int M_Z = 0;
constexpr int M_RB = 40960;
constexpr int M_TB = 81920;
constexpr int M_S1 = 131072;
constexpr int M_S2 = M_S1 + 2048;
constexpr int M_C1 = M_S2 + 1024;
constexpr int M_C2 = M_C1 + 2 * 4608;
constexpr int M_LDS = M_C2 + 2 * 4608;
#define STAMP(i)
#define PST(i)
#define KSTAMP(i)
#define CSTAMP(i)
#define RETRY(i)
#define DELAY_A 6
#define DELAY_B 4
#define DELAY_C 2

template <int IMM> DI void ldx4(v4u& dst, const v4u& srd, int voff, int soff) {
  asm volatile("buffer_load_dwordx4 %0, %1, %2, %3 offen offset:%4 sc1" : "=v"(dst) : "v"(voff), "s"(srd), "s"(soff), "i"(IMM));
}
struct WSrc { v4u srd, srd1; int lane16, base; };
template <int N> DI void load_w(v4u (&W)[64], const WSrc& s) {
  wload<((N >> 1) & 3) * 1024, false>(W[N], (N & 1) ? s.srd1 : s.srd, s.lane16, s.base + (N & 1) * 32768 + (N >> 3) * 4096);
}
template <int LO, int HI> DI void load_ws(v4u (&W)[64], const WSrc& s) { SB(); sfor<LO, HI>([&](auto J) { load_w<J.v>(W, s); }); SB(); }
DI unsigned mxacc(unsigned m, const v4u& v) { return umax(umax(umax(umax(m, v.x), v.y), v.z), v.w); }
DI unsigned tagacc(unsigned m, const v4u& v, unsigned tag) { return m | ((v.x ^ tag) | (v.y ^ tag)) | ((v.z ^ tag) | (v.w ^ tag)); }
DI unsigned andacc(unsigned m, const v4u& v) { return (m & v.x & v.y) & (v.z & v.w); }
DI unsigned long long bit0_set(unsigned m) { return __builtin_amdgcn_ballot_w64((m & 1u) != 0u); }
DI unsigned long long bit0_clear(unsigned m) { return __builtin_amdgcn_ballot_w64((m & 1u) == 0u); }
DI void pins(unsigned long long& k) { asm volatile("" : "+s"(k)); }
DI void pinf(float& a) { asm volatile("" : "+v"(a)); }
DI void pinu(unsigned& a) { asm volatile("" : "+v"(a)); }

struct ColConst { v4u C1[2], S1[2], N1[2], C2[2], S2[2], N2[2]; float tc[8], ts[8]; };

template <bool LOADS> DI void col_consts(ColConst& k, int w, int lane, v4u (&W)[64], const WSrc& ws) {
  const int g = lane >> 4, r = lane & 15;
  const float k1 = (float)(16 * w + r);
  const unsigned neg = 0x80008000u;
  sfor<0, 2>([&](auto CS) {
    constexpr int cs = CS.v;
    const float st = k1 * (1.f / 64.f), a0 = st * (float)(8 * g);
    if constexpr (LOADS) load_ws<3 * cs, 3 * cs + 2>(W, ws);
    k.C1[cs] = cosfrag(a0, st, 32 * cs);
    pin(k.C1[cs]);
    if constexpr (LOADS) load_ws<3 * cs + 2, 3 * cs + 3>(W, ws);
    k.S1[cs] = cosfrag(a0 - 0.25f, st, 32 * cs);
    k.N1[cs] = k.S1[cs] ^ neg;
    pin(k.S1[cs]); pin(k.N1[cs]);
  });
  sfor<0, 2>([&](auto T) {
    constexpr int t = T.v;
    const float st = (float)(16 * t + r) * (1.f / 32.f), a0 = st * (float)(4 * g);
    float c[8], s[8];
    if constexpr (LOADS) load_ws<6 + 3 * t, 8 + 3 * t>(W, ws);
#pragma unroll
    for (int j = 0; j < 8; ++j) c[j] = __builtin_amdgcn_cosf(__builtin_fmaf(st, (float)(16 * (j >> 2) + (j & 3)), a0));
    k.C2[t] = v4u{pk(c[0], c[1]), pk(c[2], c[3]), pk(c[4], c[5]), pk(c[6], c[7])};
    pin(k.C2[t]);
    if constexpr (LOADS) load_ws<8 + 3 * t, 9 + 3 * t>(W, ws);
#pragma unroll
    for (int j = 0; j < 8; ++j) s[j] = __builtin_amdgcn_sinf(__builtin_fmaf(st, (float)(16 * (j >> 2) + (j & 3)), a0));
    k.S2[t] = v4u{pk(s[0], s[1]), pk(s[2], s[3]), pk(s[4], s[5]), pk(s[6], s[7])};
    k.N2[t] = k.S2[t] ^ neg;
    pin(k.S2[t]); pin(k.N2[t]);
  });
  sfor<0, 2>([&](auto T) {
    constexpr int t = T.v;
    if constexpr (LOADS) load_ws<12 + 2 * t, 14 + 2 * t>(W, ws);
#pragma unroll
    for (int e = 4 * t; e < 4 * t + 4; ++e) {
      const float a = k1 * (1.f / 2048.f) * (float)(16 * (e >> 2) + 4 * g + (e & 3));
      k.tc[e] = __builtin_amdgcn_cosf(a); k.ts[e] = __builtin_amdgcn_sinf(a);
      pinf(k.tc[e]); pinf(k.ts[e]);
    }
  });
}

template <bool FIRST> DI void m16(f32x4& acc, const v4u& a, const v4u& b) {
  SB();
  if constexpr (FIRST) asm volatile("v_mfma_f32_16x16x32_bf16 %0, %1, %2, 0" : "=&v"(acc) : "v"(a), "v"(b));
  else asm volatile("v_mfma_f32_16x16x32_bf16 %0, %1, %2, %0" : "+v"(acc) : "v"(a), "v"(b));
  SB();
}
DI void m16_done(f32x4& a, f32x4& b) { asm volatile("s_nop 7" : "+v"(a), "+v"(b)); }
DI void m16_ready(v4u& a, v4u& b) { asm volatile("s_nop 1" : "+v"(a), "+v"(b)); }
template <int PART> DI float cmul(float a, float b, float c, float s) {
  float r = PART == 0 ? __builtin_fmaf(a, c, -(b * s)) : __builtin_fmaf(a, s, b * c);
  pinf(r);
  return r;
}

template <int WB>
DI void col_compute(LAS unsigned char* L, rsrc_t rs, const ColConst& k, const float (&fr)[8], const float (&fi)[8], v4u zr, v4u zi,
                    const float (&rstd)[8], int c, int w, int lane, int out_off, v4u (&W)[64], const WSrc& ws) {
  const int g = lane >> 4, r = lane & 15;
  const int first = is_zero(c);
  const float f0 = first ? 0.f : 1.f;
  float xr[8], xi[8];
#pragma unroll
  for (int i = 0; i < 4; ++i) { xr[2 * i] = blo(zr[i]); xr[2 * i + 1] = bhi(zr[i]); xi[2 * i] = blo(zi[i]); xi[2 * i + 1] = bhi(zi[i]); }
#pragma unroll
  for (int t = 0; t < 2; ++t) {
    const f32x4 rv = {rstd[4 * t], rstd[4 * t + 1], rstd[4 * t + 2], rstd[4 * t + 3]};
    const f32x4 a = f32x4{xr[4 * t], xr[4 * t + 1], xr[4 * t + 2], xr[4 * t + 3]} * rv;
    const f32x4 bq = f32x4{xi[4 * t], xi[4 * t + 1], xi[4 * t + 2], xi[4 * t + 3]} * (rv * f0);
    LAS unsigned char* d = L + M_C1 + (r + 16 * t) * 144 + (16 * w + 4 * g) * 2;
    *(LAS v2u*)d = v2u{pk(a[0], a[1]), pk(a[2], a[3])};
    *(LAS v2u*)(d + 4608) = v2u{pk(bq[0], bq[1]), pk(bq[2], bq[3])};
  }
  load_ws<WB, WB + 3>(W, ws);
  BAR();
  f32x4 dr[2], di[2];
  float ur[8], ui[8];
  {
    v4u ar[2][2], ai[2][2];
#pragma unroll
    for (int t = 0; t < 2; ++t)
#pragma unroll
      for (int cs = 0; cs < 2; ++cs) {
        LAS unsigned char* s = L + M_C1 + (r + 16 * t) * 144 + (32 * cs + 8 * g) * 2;
        ar[t][cs] = *(LAS v4u*)s; ai[t][cs] = *(LAS v4u*)(s + 4608);
      }
    load_ws<WB + 3, WB + 5>(W, ws);
    sfor<0, 16>([&](auto N) {
      constexpr int n = N.v, t = n >> 3, cs = (n >> 2) & 1, m = n & 3;
      if constexpr (m == 0) m16<cs == 0>(dr[t], ar[t][cs], k.C1[cs]);
      if constexpr (m == 1) m16<cs == 0>(di[t], ai[t][cs], k.C1[cs]);
      if constexpr (m == 2) m16<false>(dr[t], ai[t][cs], k.S1[cs]);
      if constexpr (m == 3) m16<false>(di[t], ar[t][cs], k.N1[cs]);
      if constexpr (m == 3) load_w<WB + 5 + (n >> 2)>(W, ws);
      if constexpr (n >= 9) {
        constexpr int e = (n - 9) >> 1;
        if constexpr (n & 1) ur[e] = cmul<0>(dr[0][e], di[0][e], k.tc[e], -k.ts[e]);
        else ui[e] = cmul<1>(dr[0][e], di[0][e], k.tc[e], -k.ts[e]);
      }
    });
    ui[3] = cmul<1>(dr[0][3], di[0][3], k.tc[3], -k.ts[3]);
  }
  v4u br, bi;
  br.x = pk(ur[0], ur[1]); br.y = pk(ur[2], ur[3]); bi.x = pk(ui[0], ui[1]); bi.y = pk(ui[2], ui[3]);
  SB();
  m16_done(dr[1], di[1]);
  load_ws<WB + 9, WB + 11>(W, ws);
#pragma unroll
  for (int e = 4; e < 8; ++e) {
    ur[e] = dr[1][e - 4] * k.tc[e] + di[1][e - 4] * k.ts[e]; ui[e] = di[1][e - 4] * k.tc[e] - dr[1][e - 4] * k.ts[e];
  }
  br.z = pk(ur[4], ur[5]); br.w = pk(ur[6], ur[7]); bi.z = pk(ui[4], ui[5]); bi.w = pk(ui[6], ui[7]);
  m16_ready(br, bi);
  sfor<0, 8>([&](auto N) {
    constexpr int n = N.v, t = n >> 2, m = n & 3;
    if constexpr (m == 0) m16<true>(dr[t], k.C2[t], br);
    if constexpr (m == 1) m16<true>(di[t], k.C2[t], bi);
    if constexpr (m == 2) m16<false>(dr[t], k.S2[t], bi);
    if constexpr (m == 3) m16<false>(di[t], k.N2[t], br);
    if constexpr (m == 3) load_w<WB + 11 + t>(W, ws);
    if constexpr (n == 5) ur[0] = cmul<0>(dr[0][0], di[0][0], fr[0], fi[0]);
    if constexpr (n == 6) ui[0] = cmul<1>(dr[0][0], di[0][0], fr[0], fi[0]);
  });
#pragma unroll
  for (int e = 1; e < 4; ++e) { ur[e] = dr[0][e] * fr[e] - di[0][e] * fi[e]; ui[e] = dr[0][e] * fi[e] + di[0][e] * fr[e]; }
  br.x = pk(ur[0], ur[1]); br.y = pk(ur[2], ur[3]); bi.x = pk(ui[0], ui[1]); bi.y = pk(ui[2], ui[3]);
  SB();
  m16_done(dr[1], di[1]);
  load_ws<WB + 13, WB + 15>(W, ws);
#pragma unroll
  for (int e = 4; e < 8; ++e) { ur[e] = dr[1][e - 4] * fr[e] - di[1][e - 4] * fi[e]; ui[e] = dr[1][e - 4] * fi[e] + di[1][e - 4] * fr[e]; }
  br.z = pk(ur[4], ur[5]); br.w = pk(ur[6], ur[7]); bi.z = pk(ui[4], ui[5]); bi.w = pk(ui[6], ui[7]);
  m16_ready(br, bi);
  sfor<0, 8>([&](auto N) {
    constexpr int n = N.v, t = n >> 2, m = n & 3;
    if constexpr (m == 0) m16<true>(dr[t], k.C2[t], br);
    if constexpr (m == 1) m16<true>(di[t], k.C2[t], bi);
    if constexpr (m == 2) m16<false>(dr[t], k.N2[t], bi);
    if constexpr (m == 3) m16<false>(di[t], k.S2[t], br);
    if constexpr (m == 3) load_w<WB + 15 + t>(W, ws);
    if constexpr (n == 5) ur[0] = cmul<0>(dr[0][0], di[0][0], k.tc[0], k.ts[0]);
    if constexpr (n == 6) ui[0] = cmul<1>(dr[0][0], di[0][0], k.tc[0], k.ts[0]);
  });
#pragma unroll
  for (int t = 0; t < 2; ++t) {
    if (t == 1) { SB(); m16_done(dr[1], di[1]); load_ws<WB + 17, WB + 19>(W, ws); }
#pragma unroll
    for (int i = (t == 0 ? 1 : 0); i < 4; ++i) {
      const int e = 4 * t + i;
      ur[e] = dr[t][i] * k.tc[e] - di[t][i] * k.ts[e]; ui[e] = di[t][i] * k.tc[e] + dr[t][i] * k.ts[e];
    }
    LAS unsigned char* d = L + M_C2 + (16 * w + r) * 72 + (16 * t + 4 * g) * 2;
    *(LAS v2u*)d = v2u{pk(ur[4 * t], ur[4 * t + 1]), pk(ur[4 * t + 2], ur[4 * t + 3])};
    *(LAS v2u*)(d + 4608) = v2u{pk(ui[4 * t], ui[4 * t + 1]), pk(ui[4 * t + 2], ui[4 * t + 3])};
  }
  load_ws<WB + 19, WB + 21>(W, ws);
  BAR();
  unsigned od[8];
  {
    v4u tr_[2][2], ti_[2][2];
#pragma unroll
    for (int t = 0; t < 2; ++t)
#pragma unroll
      for (int cs = 0; cs < 2; ++cs) {
        LAS unsigned char* s = L + M_C2 + (32 * cs + 8 * g + (r >> 2)) * 72 + (16 * t + 4 * (r & 3)) * 2;
        tr_[t][cs] = tr2(s, 4 * 72); ti_[t][cs] = tr2(s + 4608, 4 * 72);
      }
    load_ws<WB + 21, WB + 23>(W, ws);
    sfor<0, 16>([&](auto N) {
      constexpr int n = N.v, t = n >> 3, cs = (n >> 2) & 1, m = n & 3;
      if constexpr (m == 0) m16<cs == 0>(dr[t], k.C1[cs], tr_[t][cs]);
      if constexpr (m == 1) m16<cs == 0>(di[t], k.C1[cs], ti_[t][cs]);
      if constexpr (m == 2) m16<false>(dr[t], k.N1[cs], ti_[t][cs]);
      if constexpr (m == 3) m16<false>(di[t], k.S1[cs], tr_[t][cs]);
      if constexpr (m == 3) load_w<WB + 23 + (n >> 2)>(W, ws);
      if constexpr (n >= 9 && n < 13) {
        constexpr int e = n - 9;
        xr[e] += dr[0][e]; xi[e] = __builtin_fmaf(di[0][e], f0, xi[e]);
        pinf(xr[e]); pinf(xi[e]);
      }
      if constexpr (n == 14) { pk_tag(xr[0], xr[1], xr[2], xr[3], 1u, od[0], od[1]); pinu(od[0]); pinu(od[1]); }
    });
    pk_tag(xi[0], xi[1], xi[2], xi[3], 1u, od[4], od[5]);
    SB();
    m16_done(dr[1], di[1]);
#pragma unroll
    for (int e = 4; e < 8; ++e) { xr[e] += dr[1][e - 4]; xi[e] = __builtin_fmaf(di[1][e - 4], f0, xi[e]); }
    pk_tag(xr[4], xr[5], xr[6], xr[7], 1u, od[2], od[3]);
    pk_tag(xi[4], xi[5], xi[6], xi[7], 1u, od[6], od[7]);
  }
  const int vo = (w * 64 + lane) * 16;
  bstore_sc1(v4u{od[0], od[1], od[2], od[3]}, rs, vo, out_off + (2 * c + first) * ZS);
  bstore_sc1(v4u{od[4], od[5], od[6], od[7]}, rs, vo, out_off + (2 * c + 1 - first) * ZS);
  load_ws<WB + 27, WB + 30>(W, ws);
}

constexpr int NW_LATE = 8;
constexpr int NV_EARLY = 16;
constexpr int ZG[6] = {0, 2, 4, 8, 16, 32};
constexpr int ZR[5] = {-1, 1, 4, 10, 21};
constexpr int z_read_at(int ks) {
  for (int G = 1; G < 5; ++G) if (ks >= ZR[G] && 2 * (ks - ZR[G]) < ZG[G + 1] - ZG[G]) return ZG[G] + 2 * (ks - ZR[G]);
  return -1;
}
constexpr int z_first(int j) { for (int G = 0; G < 5; ++G) if (ZG[G] == j) return 1; return 0; }
constexpr int z_decide_at(int ks) { for (int G = 1; G < 5; ++G) if (ks == ZR[G] + (ZG[G + 1] - ZG[G]) / 2) return G; return -1; }
constexpr int z_requested(int ks) { return ks < 8 ? 9 + ks : ks < 16 ? 2 * ks + 1 : 32; }
constexpr int z_younger(int j, int ks) {
  int n = z_requested(ks) - j - 1;
  for (int k = 0; k < ks; ++k) if ((k < 8 && j < 16 && k >= j - 8) || k >= 16) ++n;
  return n;
}
struct ZSrc { v4u srd; int vo, so; unsigned lds; };
template <int J> DI void z_issue(const ZSrc& z, int so) {
  asm volatile("s_add_u32 m0, %0, %4\n\ts_nop 0\n\tbuffer_load_dwordx4 %1, %2, %3 offen sc1 lds"
               :: "s"(z.lds), "v"(z.vo), "s"(z.srd), "s"(so + (J & 15) * 32 * ZS + (J >> 4) * 128), "i"((J >> 4) * 65536 + (J & 15) * 4096)
               : "memory", "scc");
}
template <int J> DI v4u z_back(LAS unsigned char* zown) { return *(LAS v4u*)(zown + (J >> 4) * 65536 + (J & 15) * 4096); }
template <int G, int KS> DI void z_fix(unsigned long long bad, const ZSrc& z, LAS unsigned char* zown, LAS unsigned char* L, int layer, int w) {
  if (__builtin_expect(bad != 0ull, 0)) {
    unsigned spin = 0, m;
    vmwait<0>();
    do {
      RETRY(G == 0 ? 24 : 25);
      const int zo = opaque(z.so);
      sfor<ZG[G], z_requested(KS)>([&](auto I) { z_issue<I.v>(z, zo); });
      vmwait<0>();
      m = 1u;
      sfor<ZG[G], ZG[G + 1]>([&](auto I) { m = andacc(m, z_back<I.v>(zown)); });
    } while (bit0_clear(m) != 0ull && ++spin < SPIN_MAX);
  }
}

#define HA_START 14
#define HB_START 6
#define H_ROOM 16
constexpr int NSTR = 56 - NV_EARLY + 58;
struct Stream { int kind[NSTR], idx[NSTR], end[89]; };
constexpr Stream make_stream() {
  Stream s{};
  int kind[NSTR] = {}, idx[NSTR] = {}, rel[NSTR] = {}, need[NSTR] = {};
  bool done[NSTR] = {};
  int n = 0;
  for (int i = NV_EARLY; i < 56; ++i) { kind[n] = 0; idx[n] = i; rel[n] = (i - NV_EARLY) >> 1; need[n] = i >> 1; ++n; }
  for (int i = 0; i < 28; ++i) { kind[n] = 1; idx[n] = i; rel[n] = HA_START; need[n] = i; ++n; }
  for (int i = 0; i < 28; ++i) { kind[n] = 2; idx[n] = i; rel[n] = 33 + HB_START; need[n] = 28 + i; ++n; }
  for (int i = 0; i < 2; ++i) { kind[n] = 3; idx[n] = i; rel[n] = 33 + HB_START; need[n] = 60; ++n; }
  int e = 0, held = 0;
  for (int sl = 0; sl < 89; ++sl) {
    int cap = sl < 8 ? 1 : sl < 32 ? 2 - (sl % 4 == 3) : sl == 32 ? 6 : (sl - 33) % 7 == 6 ? 0 : 2;
    for (; cap > 0; --cap) {
      int best = -1;
      const int live = held - (sl < 33 ? 0 : sl - 32), room = sl < 32 ? H_ROOM : H_ROOM + 6;
      for (int i = 0; i < NSTR; ++i)
        if (!done[i] && rel[i] <= sl && (kind[i] == 0 || live < room) && (best < 0 || need[i] < need[best])) best = i;
      if (best < 0) break;
      if (kind[best] != 0) ++held;
      done[best] = true; s.kind[e] = kind[best]; s.idx[e] = idx[best]; ++e;
    }
    s.end[sl] = e;
  }
  return s;
}
constexpr Stream STR = make_stream();
static_assert(STR.end[88] == NSTR, "every load is issued");
constexpr int str_pos(int kind, int idx) { for (int i = 0; i < NSTR; ++i) if (STR.kind[i] == kind && STR.idx[i] == idx) return i; return -1; }
constexpr int str_count(int kind, int upto) { int n = 0; for (int i = 0; i < upto; ++i) if (STR.kind[i] == kind) ++n; return n; }
struct Down {
  int vbase, hbase, xvo, xso;
  DI int blk(int g) const { return g * 4096; }
};
template <int SLOT, int PART> DI void stream_issue(v4u (&W)[64], v4u (&hA)[28], v4u (&hB)[28], v4u (&x3)[2], const v4u& srd, int lane16, const Down& d) {
  constexpr int lo = SLOT == 0 ? 0 : STR.end[SLOT - 1], hi = STR.end[SLOT];
  sfor<(PART == 1 ? lo + 1 : lo), (PART == 0 && hi > lo ? lo + 1 : hi)>([&](auto I) {
    constexpr int k = STR.kind[I.v], i = STR.idx[I.v];
    if constexpr (k == 0) wload<((i >> 1) & 3) * 1024, true>(W[i - NV_EARLY], srd, lane16, d.vbase + (i & 1) * 112 * 1024 + d.blk(i >> 3));
    if constexpr (k == 1) ldx4<(i & 3) * 1024>(hA[i], srd, lane16, d.hbase + d.blk(i >> 2));
    if constexpr (k == 2) ldx4<(i & 3) * 1024>(hB[i], srd, lane16, d.hbase + 224 * 1024 + d.blk(i >> 2));
    if constexpr (k == 3) ldx4<0>(x3[i], srd, d.xvo, d.xso + i * 64 * XS);
  });
}
constexpr int dn_need(int g) {
  const int H = g / 28, i = g % 28;
  const int ph = str_pos(1 + H, i), pv = H == 0 && 2 * i >= NV_EARLY ? str_pos(0, 2 * i + 1) : 0;
  return ph > pv ? ph : pv;
}
template <int G, int ISSUED> DI void h_acc(v4u (&hA)[28], v4u (&hB)[28], unsigned& m, unsigned tag) {
  static_assert(dn_need(G) < ISSUED, "requested in time");
  vmwait<ISSUED - 1 - dn_need(G)>();
  v4u& x = G < 28 ? hA[G % 28] : hB[G % 28];
  pin(x);
  m = tagacc(m, x, tag);
  pinu(m);
}
template <int G, int ISSUED> DI void h_decide(v4u (&hA)[28], v4u (&hB)[28], unsigned long long bad, const v4u& srd, int lane16, const Down& d,
                                              LAS unsigned char* L, int layer, int w) {
  constexpr int H = G / 28, LO = G % 28;
  v4u (&hh)[28] = H == 0 ? hA : hB;
  if (__builtin_expect(bad != 0ull, 0)) {
    unsigned spin = 0, m;
    vmwait<0>();
    do {
      RETRY(26 + H);
      const int bo = opaque(d.hbase);
      sfor<LO, str_count(1 + H, ISSUED)>([&](auto I) { ldx4<(I.v & 3) * 1024>(hh[I.v], srd, lane16, bo + H * 224 * 1024 + d.blk(I.v >> 2)); });
      vmwait<0>();
      m = 0;
#pragma unroll
      for (int i = LO; i < LO + 7; ++i) { pin(hh[i]); m = tagacc(m, hh[i], layer); }
    } while (bit0_set(m) != 0ull && ++spin < SPIN_MAX);
  }
}

DI void sumsq1(float& ss, unsigned a) {
  const float x = blo(a), y = bhi(a);
  asm volatile("v_fmac_f32 %0, %1, %1\n\tv_fmac_f32 %0, %2, %2" : "+v"(ss) : "v"(x), "v"(y));
}
DI float mulf(float a, float b) { float r; asm volatile("v_mul_f32 %0, %1, %2" : "=v"(r) : "v"(a), "v"(b)); return r; }
DI void addf4(f32x4& a, const f32x4& b) {
  float x = a.x, y = a.y, z = a.z, t = a.w;
  asm volatile("v_add_f32 %0, %0, %4\n\tv_add_f32 %1, %1, %5\n\tv_add_f32 %2, %2, %6\n\tv_add_f32 %3, %3, %7"
               : "+v"(x), "+v"(y), "+v"(z), "+v"(t) : "v"(b.x), "v"(b.y), "v"(b.z), "v"(b.w));
  a = f32x4{x, y, z, t};
}
DI float relu(float a) { const int i = __builtin_bit_cast(int, a); return __builtin_bit_cast(float, i > 0 ? i : 0); }
DI void h_piece(const f32x16& a, unsigned tag, rsrc_t rs, int lane, int so) {
  unsigned d[4];
#pragma unroll
  for (int i = 0; i < 2; ++i)
    pk_tag(relu(a[4 * i]) * a[4 * i + 8], relu(a[4 * i + 1]) * a[4 * i + 9], relu(a[4 * i + 2]) * a[4 * i + 10], relu(a[4 * i + 3]) * a[4 * i + 11], tag,
           d[2 * i], d[2 * i + 1]);
  bstore_sc1(v4u{d[0], d[1], d[2], d[3]}, rs, lane * 16, so);
}
DI void x3_piece(const f32x16& a, rsrc_t rs, int lane, int so) {
  v4u o0, o1;
  widen(a, 1.f, o0, o1);
  bstore_sc1(nosent4(o0), rs, (lane & 31) * XS + (lane >> 5) * 16, so);
  bstore_sc1(nosent4(o1), rs, (lane & 31) * XS + (lane >> 5) * 16, so + 32);
}

DI void ffn(LAS unsigned char* L, rsrc_t rs, const WSrc& ws, const Params& p, v4u (&W)[64], int layer, int c, int rb, int fs, int pi, int nb,
            int w, int lane) {
  const v4u& srd = ws.srd;
  constexpr int hoff = OFF_H;
  const int h = lane >> 5, r = lane & 31, q = (lane & 15) >> 2, lane16 = lane * 16;
  ZSrc z;
  z.srd = srd;
  z.vo = (8 * w + (lane >> 3)) * ZS + (((lane & 7) ^ ((lane >> 2) & 4)) * 16);
  z.so = OFF_ZA + layer * (OFF_ZB - OFF_ZA) + rb * 256;
  z.lds = (unsigned)(size_t)L + M_Z + w * 1024;
  STAMP(4);
  sfor<0, 8>([&](auto I) { z_issue<I.v>(z, z.so); });
  SB();
  LAS unsigned char* zown = L + M_Z + w * 1024 + lane16;
  LAS unsigned char* zr[2];
#pragma unroll
  for (int s = 0; s < 2; ++s)
    zr[s] = L + M_Z + (4 * h + q) * 128 + (4 * (s ^ (q >> 1)) + 2 * ((lane >> 4) & 1) + ((lane & 3) >> 1)) * 16 + 8 * (lane & 1);
  const unsigned dcmask = (w == 0 && h == 0) ? 0xFFFF0000u : 0xFFFFFFFFu;
  const int pt = 4 * rb + pi;
  Down dn;
  dn.vbase = OFF_W2 + layer * (OFF_W3 - OFF_W2) + (nb * 2 * 112 + 28 * w) * 1024;
  dn.hbase = hoff + (pt * 112 + 28 * w) * 1024;
  dn.xvo = (lane & 31) * XS + h * 16;
  dn.xso = OFF_W1 + layer * (OFF_X3 - OFF_W1) + pt * 32 * XS + nb * 128 + w * 32;
  const int ks0 = fs * 7 + 2 * w;
  const bool two = w < 3;
  const bool idft = opaque(w + layer) == 4;
  const rsrc_t rs2 = __builtin_amdgcn_make_buffer_rsrc((void*)p.ws, 0, two ? WS_BYTES : 0, 0x00020000);
  v4u hA[28], hB[28], x3[2], res[2], Ve[NV_EARLY];
  unsigned s0, s1, s2, s3;
  asm volatile("v_mov_b32 %0, -1\n\tv_mov_b32 %1, -1\n\tv_mov_b32 %2, -1\n\tv_mov_b32 %3, -1" : "=v"(s0), "=v"(s1), "=v"(s2), "=v"(s3));
  const v4u sv = {s0, s1, s2, s3};
  const rsrc_t rsf = __builtin_amdgcn_make_buffer_rsrc((void*)p.ws, 0, layer == 0 ? WS_BYTES : 0, 0x00020000);
  const int fo_z = (w * 64 + lane) * 16 + OFF_ZB + 2 * c * ZS,
            fo_x = (w * 16 + (lane >> 2)) * XS + (lane & 3) * 16 + OFF_X3 + 128 * rb * XS + fs * 64;
  STAMP(5);
  vmwait<z_younger(1, -1)>();
  z_fix<0, -1>(bit0_clear(andacc(andacc(1u, z_back<0>(zown)), z_back<1>(zown))), z, zown, L, layer, w);
  STAMP(6);
  BAR();
  STAMP(7);
  CSTAMP(20);
  f32x16 acc[2][2][2];
  float ss[2] = {0.f, 0.f};
  sfor<0, 2>([&](auto HH) {
    constexpr int H = HH.v;
    v4u bf[2][2], sq[2], rbk[2];
    unsigned hq[4];
    float hp[4];
    unsigned zm;
    unsigned long long bad;
    LAS unsigned char* zb[2] = {zr[0] + H * 65536, zr[1] + H * 65536};
    asm volatile("" : "+v"(zb[0]), "+v"(zb[1]));
    LAS unsigned char* zq[2];
    if constexpr (H == 1) { zq[0] = (pi ? zr[1] : zr[0]) + w * 16384; zq[1] = zq[0] + 65536; asm volatile("" : "+v"(zq[0]), "+v"(zq[1])); }
#pragma unroll
    for (int s = 0; s < 2; ++s) bf[0][s] = tr2(zb[s], 1024);
    sfor<0, 32>([&](auto KS) {
      constexpr int ks = KS.v;
      constexpr bool edge = H == 0 && (ks == 3 || ks == 7 || ks == 15);
      constexpr int e = ks >> 1, es = e & 1, et = e >> 1;
      constexpr int sr = H == 1 && ks >= 8 && (ks - 8) % 3 < 2 ? 2 * ((ks - 8) / 3) + (ks - 8) % 3 : -1;
      constexpr int sn = H == 1 && ks >= 9 && (ks - 9) % 3 < 2 ? 2 * ((ks - 9) / 3) + (ks - 9) % 3 : -1;
      constexpr int rd = H == 0 ? z_read_at(ks) : -1, rp = H == 0 && ks > 0 ? z_read_at(ks - 1) : -1, dec = H == 0 ? z_decide_at(ks) : -1;
      mfma_w<ks == 0, true>(acc[H][0][0], W[2 * ks], bf[ks & 1][0]);
      SB();
      if constexpr (ks + 1 < 32 && !edge) bf[(ks + 1) & 1][0] = tr2(zb[0] + (ks + 1) * 2048, 1024);
      if constexpr (rp >= 0) { zm = andacc(z_first(rp) ? 1u : zm, rbk[0]); pinu(zm); }
      if constexpr (H == 1 && ks < 8) hp[0] = mulf(relu(acc[0][et][es][4 * (ks & 1)]), acc[0][et][es][4 * (ks & 1) + 8]);
      if constexpr (H == 1 && ks < 8) hp[1] = mulf(relu(acc[0][et][es][4 * (ks & 1) + 1]), acc[0][et][es][4 * (ks & 1) + 9]);
      if constexpr (sn >= 0) sumsq1(ss[sn & 1], sn < 2 ? sq[sn & 1].x & dcmask : sq[sn & 1].x);
      SB();
      mfma_w<ks == 0, true>(acc[H][1][0], W[2 * ks + 1], bf[ks & 1][0]);
      SB();
      if constexpr (H == 0 && ks < 16) z_issue<(ks < 8 ? 8 + ks : 2 * ks)>(z, z.so);
      if constexpr (H == 1 && ks < 8) hp[2] = mulf(relu(acc[0][et][es][4 * (ks & 1) + 2]), acc[0][et][es][4 * (ks & 1) + 10]);
      if constexpr (H == 1 && ks < 8) hp[3] = mulf(relu(acc[0][et][es][4 * (ks & 1) + 3]), acc[0][et][es][4 * (ks & 1) + 11]);
      if constexpr (sn >= 0) sumsq1(ss[sn & 1], sq[sn & 1].y);
      if constexpr (H == 1 && ks > 0) stream_issue<ks - 1, 1>(W, hA, hB, x3, srd, lane16, dn);
      if constexpr (H == 0 && ks >= 16 && (ks & 3) == 0) {
        constexpr int i = (ks - 16) / 4;
        if constexpr (i < 2) bstore_sc1(sv, rsf, fo_x, i * 64 * XS);
        else bstore_sc1(~sv, rsf, fo_z, (i - 2) * ZS);
      }
      SB();
      mfma_w<ks == 0, true>(acc[H][0][1], W[2 * ks], bf[ks & 1][1]);
      SB();
      if constexpr (ks + 1 < 32 && !edge) bf[(ks + 1) & 1][1] = tr2(zb[1] + (ks + 1) * 2048, 1024);
      if constexpr (rp >= 0) { zm = andacc(zm, rbk[1]); pinu(zm); }
      if constexpr (dec >= 0) { bad = bit0_clear(zm); pins(bad); }
      if constexpr (H == 1 && ks < 8) { pk_tag(hp[0], hp[1], hp[2], hp[3], layer, hq[2 * (ks & 1)], hq[2 * (ks & 1) + 1]); pinu(hq[2 * (ks & 1)]); pinu(hq[2 * (ks & 1) + 1]); }
      if constexpr (sn >= 0) sumsq1(ss[sn & 1], sq[sn & 1].z);
      SB();
      mfma_w<ks == 0, true>(acc[H][1][1], W[2 * ks + 1], bf[ks & 1][1]);
      SB();
      if constexpr (dec >= 0) { pins(bad); z_fix<dec, ks>(bad, z, zown, L, layer, w); }
      if constexpr (rd >= 0) { vmwait<z_younger(rd + 1, ks)>(); rbk[0] = z_back<rd>(zown); rbk[1] = z_back<rd + 1>(zown); }
      if constexpr (sn >= 0) sumsq1(ss[sn & 1], sq[sn & 1].w);
      if constexpr (sr >= 0) sq[sr & 1] = tr2(zq[sr & 1] + (sr >> 1) * 2048, 1024);
      if constexpr (H == 0 && ks < NW_LATE) load_w<64 - NW_LATE + ks>(W, ws);
      if constexpr (H == 0 && ks >= 8 && ks < 16) z_issue<2 * ks + 1>(z, z.so);
      if constexpr (H == 0 && ks >= 16) {
        constexpr int n = ks - 16;
        vload<((n >> 1) & 3) * 1024>(Ve[n], srd, lane16, dn.vbase + (n & 1) * 112 * 1024 + dn.blk(n >> 3));
      }
      if constexpr (H == 1) {
        if constexpr (ks < 8 && (ks & 1)) {
          bstore_sc1(v4u{hq[0], hq[1], hq[2], hq[3]}, et == 0 ? rs : rs2, lane16, hoff + ((4 * rb + es) * 112 + ks0 + et) * 1024);
          if constexpr (et == 1) { if (idft) x3_piece(acc[0][1][es], rs, lane, OFF_X3 + (128 * rb + 32 * es) * XS + fs * 64); }
        }
        stream_issue<ks, (ks == 31 ? 2 : 0)>(W, hA, hB, x3, srd, lane16, dn);
      }
      if constexpr (edge) {
        BAR();
#pragma unroll
        for (int s = 0; s < 2; ++s) bf[(ks + 1) & 1][s] = tr2(zb[s] + (ks + 1) * 2048, 1024);
      }
      SB();
    });
    STAMP(8 + 2 * H);
    if constexpr (H == 0) BAR();
    STAMP(9 + 2 * H);
  });
  mfma_done(acc[1][0][0], acc[1][0][1], acc[1][1][0], acc[1][1][1]);
#pragma unroll
  for (int s = 0; s < 2; ++s) h_piece(acc[1][0][s], layer, rs, lane, hoff + ((4 * rb + 2 + s) * 112 + ks0) * 1024);
  if (two) {
#pragma unroll
    for (int s = 0; s < 2; ++s) h_piece(acc[1][1][s], layer, rs, lane, hoff + ((4 * rb + 2 + s) * 112 + ks0 + 1) * 1024);
  } else if (idft) {
#pragma unroll
    for (int s = 0; s < 2; ++s) x3_piece(acc[1][1][s], rs, lane, OFF_X3 + (128 * rb + 64 + 32 * s) * XS + fs * 64);
  }
  SB();
  stream_issue<32, 2>(W, hA, hB, x3, srd, lane16, dn);
  {
    auto x = __builtin_amdgcn_permlane32_swap(asu(ss[0]), asu(ss[1]), false, false);
    *(LAS float*)(L + M_S1 + w * 256 + lane * 4) = asf(x[0]) + asf(x[1]);
  }
  const int row = 16 * w + (lane >> 2);
  {
    const int n = nb * 64 + row;
    LAS unsigned char* ra = L + M_Z + n * 128 + (((4 * pi + (lane & 3)) ^ ((n & 2) << 1)) * 16);
    res[0] = *(LAS v4u*)ra; res[1] = *(LAS v4u*)(ra + 65536);
  }
  STAMP(12);
  CSTAMP(21);
  unsigned hm = 0;
  sfor<0, 7>([&](auto G) { h_acc<G.v, STR.end[32]>(hA, hB, hm, layer); });
  h_decide<0, STR.end[32]>(hA, hB, bit0_set(hm), srd, lane16, dn, L, layer, w);
  hm = 0;
  BAR();
  STAMP(13);
  LAS unsigned char* rw = L + M_Z + w * 9216 + r * 144 + h * 16;
  LAS unsigned char* rr = L + M_Z + row * 144 + (lane & 3) * 32;
  f32x16 dac[2][2];
  f32x4 fa[2][2], tq[2];
  auto red_write = [&](auto HH, auto K) {
    constexpr int H = HH.v, t = K.v >> 2, g = K.v & 3;
    *(LAS f32x4*)(rw + H * M_RB + t * 32 * 144 + g * 32) = f32x4{dac[H][t][4 * g], dac[H][t][4 * g + 1], dac[H][t][4 * g + 2], dac[H][t][4 * g + 3]};
  };
  sfor<0, 56>([&](auto GG) {
    constexpr int g = GG.v, H = g / 28, i = g % 28;
    v4u& hx = H == 0 ? hA[i] : hB[i];
    unsigned long long bad = 0ull;
    if constexpr (2 * i < NV_EARLY) mfma_v<i == 0>(dac[H][0], Ve[2 * i], hx);
    else mfma_w<false, false>(dac[H][0], W[2 * i - NV_EARLY], hx);
    SB();
    if constexpr (g + 7 < 56) h_acc<g + 7, STR.end[32 + g]>(hA, hB, hm, layer);
    if constexpr (g + 7 < 56 && g % 7 == 6) { bad = bit0_set(hm); pins(bad); hm = 0; }
    if constexpr (g == 1) {
      LAS float* sp = (LAS float*)(L + M_S1 + lane * 4);
      *(LAS float*)(L + M_S1 + 1024 + w * 256 + lane * 4) =
          __builtin_amdgcn_rcpf(__builtin_fmaf((sp[0] + sp[64]) + (sp[128] + sp[192]), 1.f / 512.f, 1e-5f));
    }
    if constexpr (g >= 51 && g <= 53) addf4(fa[0][0], tq[0]);
    SB();
    if constexpr (2 * i < NV_EARLY) mfma_v<i == 0>(dac[H][1], Ve[2 * i + 1], hx);
    else mfma_w<false, false>(dac[H][1], W[2 * i + 1 - NV_EARLY], hx);
    SB();
    if constexpr (g + 7 < 56 && g % 7 == 6) { pins(bad); h_decide<g + 1, STR.end[32 + g]>(hA, hB, bad, srd, lane16, dn, L, layer, w); }
    stream_issue<33 + g, 2>(W, hA, hB, x3, srd, lane16, dn);
    if constexpr (g >= 30 && g <= 46 && g % 2 == 0 && g != 34) red_write(IC<0>{}, IC<(g < 34 ? (g - 30) / 2 : (g - 32) / 2)>{});
    if constexpr (g == 47) BAR();
    if constexpr (g == 49) { fa[0][0] = *(LAS f32x4*)rr; fa[0][1] = *(LAS f32x4*)(rr + 16); }
    if constexpr (g >= 51 && g <= 53) addf4(fa[0][1], tq[1]);
    if constexpr (g >= 50 && g <= 52) { tq[0] = *(LAS f32x4*)(rr + (g - 49) * 9216); tq[1] = *(LAS f32x4*)(rr + (g - 49) * 9216 + 16); }
    SB();
    if constexpr (g == 27) STAMP(14);
  });
  STAMP(15);
  asm volatile("s_nop 12" : "+v"(dac[1][0]), "+v"(dac[1][1]));
  sfor<0, 8>([&](auto K) { red_write(IC<1>{}, K); });
  vmwait<0>();
  pin(x3[0]); pin(x3[1]);
  if (__builtin_expect(layer == 1 && any_sent(mxacc(mxacc(0u, x3[0]), x3[1])), 0)) {
    unsigned spin = 0;
    do {
      RETRY(29);
      ldx4<0>(x3[0], srd, dn.xvo, dn.xso); ldx4<0>(x3[1], srd, dn.xvo, dn.xso + 64 * XS);
      vmwait<0>();
      pin(x3[0]); pin(x3[1]);
    } while (any_sent(mxacc(mxacc(0u, x3[0]), x3[1])) && ++spin < SPIN_MAX);
  }
  STAMP(16);
  BAR();
  STAMP(17);
  fa[1][0] = *(LAS f32x4*)(rr + M_RB); fa[1][1] = *(LAS f32x4*)(rr + M_RB + 16);
#pragma unroll
  for (int ww = 1; ww < 4; ++ww) { fa[1][0] += *(LAS f32x4*)(rr + M_RB + ww * 9216); fa[1][1] += *(LAS f32x4*)(rr + M_RB + ww * 9216 + 16); }
  f32x4 sc[2][2];
#pragma unroll
  for (int H = 0; H < 2; ++H)
#pragma unroll
    for (int x = 0; x < 2; ++x) sc[H][x] = *(LAS f32x4*)(L + M_S1 + 1024 + w * 256 + H * 128 + (lane & 3) * 32 + x * 16);
  if (layer == 0) {
    const bool dc = nb == 0 && row == 0;
#pragma unroll
    for (int H = 0; H < 2; ++H) {
      unsigned o[4];
      float sq[8];
#pragma unroll
      for (int x = 0; x < 2; ++x)
        pk_tag(__builtin_fmaf(fa[H][x][0], sc[H][x][0], blo(res[H][2 * x])), __builtin_fmaf(fa[H][x][1], sc[H][x][1], bhi(res[H][2 * x])),
               __builtin_fmaf(fa[H][x][2], sc[H][x][2], blo(res[H][2 * x + 1])), __builtin_fmaf(fa[H][x][3], sc[H][x][3], bhi(res[H][2 * x + 1])), 0u,
               o[2 * x], o[2 * x + 1]);
      bstore_sc1(v4u{o[0], o[1], o[2], o[3]}, rs, row * ZS + (lane & 3) * 16, OFF_ZA + nb * 64 * ZS + pt * 64 + H * 128);
#pragma unroll
      for (int j = 0; j < 4; ++j) {
        const unsigned u = dc ? 0u : o[j];
        sq[2 * j] = blo(u) * blo(u); sq[2 * j + 1] = bhi(u) * bhi(u);
      }
      float t4[4], t2[2];
#pragma unroll
      for (int j = 0; j < 4; ++j) {
        auto x = __builtin_amdgcn_permlane32_swap(asu(sq[j]), asu(sq[j + 4]), false, false);
        t4[j] = asf(x[0]) + asf(x[1]);
      }
#pragma unroll
      for (int j = 0; j < 2; ++j) {
        auto x = __builtin_amdgcn_permlane16_swap(asu(t4[j]), asu(t4[j + 2]), false, false);
        t2[j] = asf(x[0]) + asf(x[1]);
        t2[j] += dppf<0x124>(t2[j]);
        t2[j] += dppf<0x128>(t2[j]);
      }
      *(LAS f32x2*)(L + M_S2 + w * 256 + H * 128 + (8 * (lane & 3) + 4 * h + 2 * ((lane >> 4) & 1)) * 4) = f32x2{t2[0], t2[1]};
    }
    BAR();
    if (w == 0) {
      LAS float* sp = (LAS float*)(L + M_S2 + lane * 4);
      const float t = (sp[0] + sp[64]) + (sp[128] + sp[192]);
      __builtin_amdgcn_raw_buffer_store_b32(nosent(asu(t)), rs, stat_of(h * 64 + r), OFF_ST + nb * 8192 + pt * 64, SC1);
    }
  } else {
    LAS unsigned char* tb = L + M_TB + w * 2560;
#pragma unroll
    for (int H = 0; H < 2; ++H) {
      const f32x4 a = fa[H][0] * sc[H][0], bq = fa[H][1] * sc[H][1];
      *(LAS v4u*)(tb + H * 1280 + (lane >> 2) * 80 + (lane & 3) * 16) = v4u{pk(a.x, a.y), pk(a.z, a.w), pk(bq.x, bq.y), pk(bq.z, bq.w)};
    }
#pragma unroll
    for (int H = 0; H < 2; ++H) {
      const v4u y = tr2(tb + H * 1280 + (8 * h + q) * 80 + ((lane >> 4) & 1) * 32 + (lane & 3) * 8, 4 * 80);
      v4u o;
#pragma unroll
      for (int j = 0; j < 4; ++j) o[j] = pk(blo(x3[H][j]) + blo(y[j]), bhi(x3[H][j]) + bhi(y[j]));
      const int tok = token_of(32 * (pt + 2 * H) + r);
      *(v4u*)(p.out + (size_t)tok * 512 + nb * 64 + 16 * w + 8 * h) = o;
    }
  }
  SB();
}

constexpr int CODE_BYTES = 44 * 1024;
constexpr int P_XL = 0, P_GL = 33280, P_DL = P_GL + 65536, P_ZB = P_DL + 33280, P_WB = P_ZB + 4 * 2560;
static_assert(P_WB + 64 * 144 <= M_C2 + 2 * 4608, "fits");

DI void publish(rsrc_t rs, int which, int c, unsigned long long tag, int w, int lane) {
  if (w == 0 && lane < 8)
    __builtin_amdgcn_raw_buffer_store_b64(v2u{(unsigned)tag, (unsigned)(tag >> 32)}, rs, OFF_FL + which * 16384 + lane * 2048 + c * 8, 0, SC1);
}
DI void flags_load(v4u (&f)[2], const v4u& srd, int which, int c, int lane) {
  ldx4<0>(f[0], srd, lane * 16, OFF_FL + which * 16384 + (c & 7) * 2048);
  ldx4<1024>(f[1], srd, lane * 16, OFF_FL + which * 16384 + (c & 7) * 2048);
}
DI void flags_wait(v4u (&f)[2], const v4u& srd, int which, int c, int lane, unsigned long long tag) {
  const unsigned lo = (unsigned)tag, hi = (unsigned)(tag >> 32);
  auto missing = [&]() {
    pin(f[0]); pin(f[1]);
    const unsigned d = ((f[0].x ^ lo) | (f[0].y ^ hi) | (f[0].z ^ lo)) | ((f[0].w ^ hi) | (f[1].x ^ lo) | (f[1].y ^ hi)) | ((f[1].z ^ lo) | (f[1].w ^ hi));
    return __builtin_amdgcn_ballot_w64(d != 0u) != 0ull;
  };
  if (__builtin_expect(missing(), 0)) {
    unsigned spin = 0;
    do { __builtin_amdgcn_s_sleep(2); flags_load(f, srd, which, c, lane); vmwait<0>(); } while (missing() && ++spin < SPIN_MAX);
  }
}

DI void prep(LAS unsigned char* L, const Params& p, rsrc_t rs, unsigned long long tag) {
  const unsigned Lb = (unsigned)(size_t)L;
  const int tid = threadIdx.x, lane = tid & 63, w = __builtin_amdgcn_readfirstlane(tid >> 6);
  const int b = blockIdx.x, xcd = b & 7, q = (b >> 3) & 3, tgi = b >> 5;
  const int t1 = xcd * 8 + tgi, tg = xcd * 7 + tgi;
  const bool heavy = tgi < 7;
  const int up = tg >= 28, cb = tg - 28 * up;
  const int h = lane >> 5, r = lane & 31, ft = 4 * q + w;
  PST(0);
  const unsigned hm = opaque((tgi - 7) >> 31);
  v4u sx = make_srd(p.x, 2048u * 1024u), sd = make_srd(p.wd, 1792u * 1024u & hm), sg = make_srd(up ? p.wu : p.wg, 512u * 3584u & hm);
  asm volatile("" : "+s"(sg));
  const v4u sr = make_srd(p.wr, 2048u * 514u), si = make_srd(p.wi, 2048u * 514u);
#pragma unroll
  for (int i = 0; i < 8; ++i) {
    const int row = 8 * (((i >> 1) + q) & 3) + 2 * w + (i & 1);
    glds_in(sx, lane * 16, token_of(32 * t1 + row) * 1024, Lb + P_XL + row * 1040);
  }
  unsigned touch, touch2;
  {
    const char* base = (const char*)(up ? p.wu : p.wg) + (128 * q + 64 * (w & 1)) * 3584 + (heavy ? 128 * cb : 0);
    int off = lane * 3584;
    if (w == 2) { base = (const char*)p.wd + (heavy ? 32 * tg + 8 * q : 0) * 1024; off = lane * 128; }
    if (w == 3) { base = (const char*)p.wr + (32 * t1 * 257 + 64 * q) * 2; off = r * 514; }
    const char* a = base + off + (w == 3 && h ? (const char*)p.wi - (const char*)p.wr : 0);
    asm volatile("global_load_ushort %0, %1, off nt" : "=v"(touch) : "v"(a));
    unsigned lo, hi;
    asm volatile("s_getpc_b64 s[20:21]\n\ts_add_u32 %0, s20, _Z6k_main6Params@rel32@lo+4\n\ts_addc_u32 %1, s21, _Z6k_main6Params@rel32@hi+12"
                 : "=s"(lo), "=s"(hi) :: "s20", "s21", "scc");
    off = (w < 2 ? w : w + 2 * ((b >> 3) & 1)) * 8192 + lane * 128;
    asm volatile("global_load_dword %0, %1, %2" : "=v"(touch2) : "v"(off < CODE_BYTES ? off : 0), "s"(((unsigned long long)hi << 32) | lo));
  }
  SB();
  const int feat = 32 * ft + r;
  const float step = (feat == 0 ? 0.f : feat == 1 ? 256.f : (float)(feat >> 1)) * (1.f / 512.f);
  const float ph = (feat > 1 && (feat & 1)) ? 0.25f : 0.f;
  const float a0 = ph + step * (float)(8 * h);
  const unsigned sgn = (feat > 1 && (feat & 2)) ? 0x80008000u : 0u;
  v4u F[32];
#pragma unroll
  for (int ks = 0; ks < 16; ++ks) { F[ks] = cosfrag(a0, step, 16 * ks); F[ks + 16] = F[ks] ^ sgn; }
  const float lsc = feat < 2 ? 0.04419417382415922f : 0.0625f;
#pragma unroll
  for (int ks = 0; ks < 32; ks += 8)
    asm volatile("" : "+v"(F[ks]), "+v"(F[ks + 1]), "+v"(F[ks + 2]), "+v"(F[ks + 3]), "+v"(F[ks + 4]), "+v"(F[ks + 5]), "+v"(F[ks + 6]), "+v"(F[ks + 7]));
  v4u I[2];
#pragma unroll
  for (int s = 0; s < 2; ++s) {
    const int e = r - 16 * s - 8 * h;
    const unsigned one = (e & 1) ? 0x3F800000u : 0x00003F80u;
    I[s] = v4u{(e >> 1) == 0 ? one : 0u, (e >> 1) == 1 ? one : 0u, (e >> 1) == 2 ? one : 0u, (e >> 1) == 3 ? one : 0u};
  }
  pin(I[0]); pin(I[1]);
  const int gvo = (lane >> 3) * 3584 + (((lane & 7) ^ ((lane >> 2) & 4)) * 16);
  const int fcol = 64 * q + lane, fvo = (fcol == 0 ? 256 : fcol) * 2, fso = (32 * t1 + 8 * w) * 514;
  if (!heavy) {
#pragma unroll
    for (int i = 0; i < 2; ++i) {
      const int dt = 2 * xcd + i;
      const float b0 = __builtin_amdgcn_fractf(__builtin_fmaf(step, (float)(32 * dt), a0));
      f32x16 aI;
      v4u c0 = cosfrag(b0, step, 0), c1 = cosfrag(b0, step, 16);
      m16_ready(c0, c1);
      mfma_vv<true>(aI, c0, I[0]);
      mfma_vv<false>(aI, c1, I[1]);
      asm volatile("s_nop 12" : "+v"(aI));
      if (ft == 0 && h == 0) { aI[0] *= 0.70710678118654752f; aI[1] *= 0.70710678118654752f; }
#pragma unroll
      for (int s = 0; s < 2; ++s) bstore_sc1(pack8(aI, s, 0.0625f), rs, lane * 16, OFF_W1 + (dt * 8 + 7) * 32768 + (2 * ft + s) * 1024);
    }
  }
  const rsrc_t rsh = __builtin_amdgcn_make_buffer_rsrc((void*)p.ws, 0, WS_BYTES & hm, 0x00020000);
  PST(1);
  vmwait<2>();
  BAR();
  PST(2);
  f32x16 aZ;
  unsigned fr[8], fi[8];
  {
    LAS unsigned char* xa = L + P_XL + r * 1040 + h * 16;
    v4u a[4];
#pragma unroll
    for (int i = 0; i < 4; ++i) a[i] = *(LAS v4u*)(xa + i * 32);
    sfor<0, 32>([&](auto KS) {
      constexpr int ks = KS.v;
      mfma_vv<ks == 0>(aZ, a[ks & 3], F[ks]);
      SB();
      if constexpr (ks + 4 < 32) a[ks & 3] = *(LAS v4u*)(xa + (ks + 4) * 32);
      if constexpr (ks < 8) {
        const int row = 8 * (((ks >> 1) + q) & 3) + 2 * w + (ks & 1);
        glds_in(sd, lane * 16, (32 * tg + row) * 1024, Lb + P_DL + row * 1040);
      } else if constexpr (ks < 24) {
        const int blk = 16 * ((((ks - 8) >> 2) + q) & 3) + 4 * w + (ks & 3);
        glds_in(sg, gvo, blk * 8 * 3584 + cb * 128, Lb + P_GL + blk * 1024);
      } else ld2u16<(ks - 24) * 514>(fr[ks - 24], fi[ks - 24], fvo, sr, si, fso);
      SB();
    });
  }
  v4u zo[2], su[2], wo[2];
  unsigned zd[8];
  float sv_[16], ssum;
  auto z_out = [&](auto STEP) {
    constexpr int n = STEP.v;
    LAS unsigned char* zb = L + P_ZB + w * 2560;
    if constexpr (n < 4) {
      pk_tag(aZ[4 * n] * lsc, aZ[4 * n + 1] * lsc, aZ[4 * n + 2] * lsc, aZ[4 * n + 3] * lsc, 0u, zd[2 * n], zd[2 * n + 1]);
      pinu(zd[2 * n]); pinu(zd[2 * n + 1]);
    }
    if constexpr (n == 4 || n == 5) {
      auto r0 = __builtin_amdgcn_permlane32_swap(zd[4 * (n - 4)], zd[4 * (n - 4) + 2], false, false);
      auto r1 = __builtin_amdgcn_permlane32_swap(zd[4 * (n - 4) + 1], zd[4 * (n - 4) + 3], false, false);
      *(LAS v4u*)(zb + r * 80 + h * 16 + 32 * (n - 4)) = v4u{r0[0], r1[0], r0[1], r1[1]};
    }
    if constexpr (n == 6) { zo[0] = *(LAS v4u*)(zb + (lane >> 2) * 80 + (lane & 3) * 16); zo[1] = *(LAS v4u*)(zb + (16 + (lane >> 2)) * 80 + (lane & 3) * 16); }
    if constexpr (n == 7 || n == 8) bstore_sc1(zo[n - 7], rs, (32 * ft + 16 * (n - 7) + (lane >> 2)) * ZS + (lane & 3) * 16, OFF_ZA + t1 * 64);
  };
  auto stat = [&](auto STEP) {
    constexpr int n = STEP.v;
    const int row = 8 * q + 2 * w + h;
    if constexpr (n == 0) { su[0] = *(LAS v4u*)(L + P_XL + row * 1040 + r * 32); su[1] = *(LAS v4u*)(L + P_XL + row * 1040 + r * 32 + 16); ssum = 0.f; }
    if constexpr (n >= 1 && n <= 4) {
      constexpr int k = n - 1;
      sv_[4 * k] = blo(su[k >> 1][2 * (k & 1)]); sv_[4 * k + 1] = bhi(su[k >> 1][2 * (k & 1)]);
      sv_[4 * k + 2] = blo(su[k >> 1][2 * (k & 1) + 1]); sv_[4 * k + 3] = bhi(su[k >> 1][2 * (k & 1) + 1]);
      ssum += (sv_[4 * k] + sv_[4 * k + 1]) + (sv_[4 * k + 2] + sv_[4 * k + 3]);
    }
    if constexpr (n == 5) { const float m = sum32(ssum) * (1.f / 512.f); ssum = m; }
    if constexpr (n >= 6 && n <= 9) {
#pragma unroll
      for (int i = 4 * (n - 6); i < 4 * (n - 6) + 4; ++i) { sv_[i] -= ssum; pinf(sv_[i]); }
    }
    if constexpr (n == 10) ssum = 0.f;
    if constexpr (n >= 10 && n <= 12) {
#pragma unroll
      for (int i = 6 * (n - 10); i < 6 * (n - 10) + 6 && i < 16; ++i) ssum = __builtin_fmaf(sv_[i], sv_[i], ssum);
    }
    if constexpr (n == 13) ssum = sum32(ssum);
    if constexpr (n == 14) { if (r == 0) __builtin_amdgcn_raw_buffer_store_b32(asu(ssum), rs, stat_of(32 * t1 + row), OFF_R0, SC1); }
    pinf(ssum);
  };
  auto f_out = [&](auto N, const f32x16& a, float sc, v4u& o, int vo, int so) {
    constexpr int k = 8 * (N.v >> 1) + 4 * (N.v & 1);
    o[2 * (N.v & 1)] = pk(a[k] * sc, a[k + 1] * sc); o[2 * (N.v & 1) + 1] = pk(a[k + 2] * sc, a[k + 3] * sc);
    if constexpr (N.v & 1) bstore_sc1(o, rsh, vo, so + (N.v >> 1) * 1024); else pin(o);
  };
  auto filt = [&](auto STEP) {
    constexpr int n = STEP.v;
    if constexpr (n == 0) {
      asm volatile("" : "+v"(fr[0]), "+v"(fr[1]), "+v"(fr[2]), "+v"(fr[3]), "+v"(fr[4]), "+v"(fr[5]), "+v"(fr[6]), "+v"(fr[7]),
                        "+v"(fi[0]), "+v"(fi[1]), "+v"(fi[2]), "+v"(fi[3]), "+v"(fi[4]), "+v"(fi[5]), "+v"(fi[6]), "+v"(fi[7]));
      v4u o0, o1;
#pragma unroll
      for (int i = 0; i < 4; ++i) { o0[i] = fr[i] | (fi[i] << 16); o1[i] = fr[4 + i] | (fi[4 + i] << 16); }
      *(LAS v4u*)(L + P_WB + lane * 144 + w * 32) = o0;
      *(LAS v4u*)(L + P_WB + lane * 144 + w * 32 + 16) = o1;
    }
    if constexpr (n == 1) {
#pragma unroll
      for (int i = 0; i < 2; ++i) wo[i] = *(LAS v4u*)(L + P_WB + (16 * w + 8 * i + (lane >> 3)) * 144 + (lane & 7) * 16);
    }
    if constexpr (n >= 2) bstore_sc1(wo[n - 2], rs, (64 * q + 16 * w + 8 * (n - 2) + (lane >> 3)) * WS_ + (lane & 7) * 16, OFF_WT + t1 * 128);
  };
  auto fill = [&](auto STEP) {
    const v4u sv = {SENT, SENT, SENT, SENT};
    if constexpr (STEP.v < 7) bstore_sc1(sv, rs, tid * 16, OFF_H + b * 28672 + STEP.v * 4096);
    else if (tid < 16) bstore_sc1(sv, rs, tid * 16, OFF_ST + b * 256);
  };
  asm volatile("s_nop 12\n\ts_waitcnt vmcnt(32)" : "+v"(aZ) : "v"(touch), "v"(touch2) : "memory");
  PST(3);
  BAR();
  PST(4);
  {
    f32x16 aD, aT;
    v4u fo = {0u, 0u, 0u, 0u};
    {
      LAS unsigned char* da = L + P_DL + r * 1040 + h * 16;
      const v4u t0 = *(LAS v4u*)(da + ft * 64), t1_ = *(LAS v4u*)(da + ft * 64 + 32);
      v4u a[4];
#pragma unroll
      for (int i = 0; i < 4; ++i) a[i] = *(LAS v4u*)(da + i * 32);
      mfma_vv<true>(aT, t0, I[0]);
      mfma_vv<false>(aT, t1_, I[1]);
      sfor<0, 32>([&](auto KS) {
        constexpr int ks = KS.v;
        mfma_vv<ks == 0>(aD, a[ks & 3], F[ks]);
        SB();
        if constexpr (ks + 4 < 32) a[ks & 3] = *(LAS v4u*)(da + (ks + 4) * 32);
        if constexpr (ks < 9) z_out(KS);
        if constexpr (ks == 7) stat(IC<0>{});
        if constexpr (ks >= 9 && ks < 23) stat(IC<ks - 8>{});
        if constexpr (ks >= 23 && ks < 27) f_out(IC<ks - 23>{}, aT, 1.f, fo, lane * 16, OFF_W3 + (ft * 112 + 2 * tg) * 1024);
        SB();
      });
    }
    PST(5);
    vmwait<5>();
    filt(IC<0>{});
    BAR();
    PST(6);
    f32x16 aG0, aG1;
    const int q4 = (lane & 15) >> 2;
    LAS unsigned char* ba = L + P_GL + (8 * h + q4) * 128 + (4 * (q4 >> 1) + 2 * ((lane >> 4) & 1) + ((lane & 3) >> 1)) * 16 + 8 * (lane & 1);
    LAS unsigned char* bb = L + P_GL + (8 * h + q4) * 128 + (4 * (1 - (q4 >> 1)) + 2 * ((lane >> 4) & 1) + ((lane & 3) >> 1)) * 16 + 8 * (lane & 1);
    v4u b0[2], b1[2];
#pragma unroll
    for (int i = 0; i < 2; ++i) { b0[i] = tr2(ba + i * 2048, 512); b1[i] = tr2(bb + i * 2048, 512); }
    sfor<0, 32>([&](auto KS) {
      constexpr int ks = KS.v;
      mfma_vv<ks == 0>(aG0, F[ks], b0[ks & 1]);
      SB();
      if constexpr (ks + 2 < 32) b0[ks & 1] = tr2(ba + (ks + 2) * 2048, 512);
      if constexpr (ks == 0 || ks == 2 || ks == 3) filt(IC<(ks == 0 ? 1 : ks)>{});
      if constexpr (ks >= 4 && ks < 8) f_out(IC<ks - 4>{}, aD, lsc, fo, lane * 16, OFF_W2 + (ft * 112 + 2 * tg) * 1024);
      if constexpr (ks == 17) publish(rs, 0, b, tag, w, lane);
      if constexpr (ks >= 18 && ks < 26) fill(IC<ks - 18>{});
      SB();
      mfma_vv<ks == 0>(aG1, F[ks], b1[ks & 1]);
      SB();
      if constexpr (ks + 2 < 32) b1[ks & 1] = tr2(bb + (ks + 2) * 2048, 512);
      if constexpr (ks == 16) { vmwait<0>(); asm volatile("s_barrier" ::: "memory"); }
      SB();
    });
    PST(7);
    asm volatile("s_nop 12" : "+v"(aG0), "+v"(aG1));
    if (ft == 0 && h == 0) { aG0[0] = 0.f; aG1[0] = 0.f; aG0[1] *= 0.70710678118654752f; aG1[1] *= 0.70710678118654752f; }
    const int g0 = 4 * cb + (r >> 4), g1 = g0 + 2, slot = ((r & 15) + 16 * up + 32 * h) * 16;
    const int o0 = ((g0 / 7) * 8 + g0 % 7) * 32768 + slot, o1 = ((g1 / 7) * 8 + g1 % 7) * 32768 + slot;
#pragma unroll
    for (int s = 0; s < 2; ++s) {
      bstore_sc1(pack8(aG0, s, 0.0625f), rsh, o0, OFF_W1 + (2 * ft + s) * 1024);
      bstore_sc1(pack8(aG1, s, 0.0625f), rsh, o1, OFF_W1 + (2 * ft + s) * 1024);
    }
  }
  PST(8);
}

struct Tile {
  int rb, fs, pi, nb;
  DI Tile(int c) { rb = 4 * (c & 3) + (c >> 6); fs = 8 * ((c >> 2) & 1) + ((c >> 3) & 7); pi = fs >> 3; nb = fs & 7; }
};

template <int layer>
DI void column(LAS unsigned char* L, rsrc_t rs, const v4u& srd, v4u (&W)[64], unsigned long long tag) {
  int tid = threadIdx.x;
  asm volatile("" : "+v"(tid));
  const int c = opaque(blockIdx.x);
  const int lane = tid & 63, w = __builtin_amdgcn_readfirstlane(tid >> 6);
  WSrc ws;
  ws.srd = srd; ws.srd1 = srd;
  if (w == 3 && layer == 0) ws.srd1.z = 0u;
  ws.lane16 = lane * 16; ws.base = OFF_W1 + (Tile(c).fs * 8 + 2 * w) * 32768;
  unsigned wf[8];
  const int wfvo = (16 * w + (lane & 15) + 64 * 4 * (lane >> 4)) * 4;
  auto filter_in = [&]() {
    asm volatile("buffer_load_dword %0, %8, %9, %10 offen sc1\n\tbuffer_load_dword %1, %8, %9, %10 offen offset:256 sc1\n\t"
                 "buffer_load_dword %2, %8, %9, %10 offen offset:512 sc1\n\tbuffer_load_dword %3, %8, %9, %10 offen offset:768 sc1\n\t"
                 "buffer_load_dword %4, %8, %9, %11 offen sc1\n\tbuffer_load_dword %5, %8, %9, %11 offen offset:256 sc1\n\t"
                 "buffer_load_dword %6, %8, %9, %11 offen offset:512 sc1\n\tbuffer_load_dword %7, %8, %9, %11 offen offset:768 sc1"
                 : "=&v"(wf[0]), "=&v"(wf[1]), "=&v"(wf[2]), "=&v"(wf[3]), "=&v"(wf[4]), "=&v"(wf[5]), "=&v"(wf[6]), "=&v"(wf[7])
                 : "v"(wfvo), "s"(srd), "s"(OFF_WT + c * WS_), "s"(OFF_WT + c * WS_ + 4096));
  };
  float rstd[8], fr[8], fi[8];
  auto filter_unpack = [&]() {
    asm volatile("" : "+v"(wf[0]), "+v"(wf[1]), "+v"(wf[2]), "+v"(wf[3]), "+v"(wf[4]), "+v"(wf[5]), "+v"(wf[6]), "+v"(wf[7]));
#pragma unroll
    for (int e = 0; e < 8; ++e) { fr[e] = blo(wf[e]) * (1.f / 2048.f); fi[e] = bhi(wf[e]) * (1.f / 2048.f); pinf(fr[e]); pinf(fi[e]); }
  };
  STAMP(0);
  CSTAMP(22);
  v4u zr, zi, sa, sb;
  const int first = is_zero(c);
  ColConst k;
  if constexpr (layer == 0) {
    v4u fl[2];
    flags_load(fl, srd, 0, c, lane);
    SB();
    col_consts<false>(k, w, lane, W, ws);
    SB();
    vmwait<0>();
    BAR();
    publish(rs, 1, c, tag, w, lane);
    flags_wait(fl, srd, 0, c, lane, tag);
    if (c == 0 && tid == 0) __builtin_amdgcn_raw_buffer_store_b64(v2u{(unsigned)tag, (unsigned)(tag >> 32)}, rs, OFF_CTL, 0, SC1);
    ldx4<0>(zr, srd, tid * 16, OFF_ZA + (2 * c + first) * ZS); ldx4<0>(zi, srd, tid * 16, OFF_ZA + (2 * c + 1 - first) * ZS);
    ldx4<0>(sa, srd, tid * 16, OFF_R0); ldx4<0>(sb, srd, tid * 16, OFF_R0 + 4096);
    filter_in();
    __builtin_amdgcn_s_sleep(DELAY_B);
    flags_load(fl, srd, 1, c, lane);
    STAMP(1);
    vmwait<0>();
    pin(zr); pin(zi); pin(sa); pin(sb);
    filter_unpack();
    flags_wait(fl, srd, 1, c, lane, tag);
    load_ws<0, 6>(W, ws);
  } else {
    filter_in();
    SB();
    col_consts<true>(k, w, lane, W, ws);
    SB();
    vmwait<16>();
    filter_unpack();
    STAMP(1);
    __builtin_amdgcn_s_sleep(DELAY_C);
    unsigned spin = 0;
    bool again;
    do {
      RETRY(28);
      v4u st[16];
      ldx4<0>(zr, srd, tid * 16, OFF_ZA + (2 * c + first) * ZS); ldx4<0>(zi, srd, tid * 16, OFF_ZA + (2 * c + 1 - first) * ZS);
      sfor<0, 16>([&](auto I) { ldx4<0>(st[I.v], srd, tid * 16, OFF_ST + I.v * 4096); });
      vmwait<0>();
      pin(zr); pin(zi);
#pragma unroll
      for (int i = 0; i < 16; ++i) pin(st[i]);
      f32x4 a = __builtin_bit_cast(f32x4, st[0]), bq = __builtin_bit_cast(f32x4, st[1]);
#pragma unroll
      for (int n = 1; n < 8; ++n) { a += __builtin_bit_cast(f32x4, st[2 * n]); bq += __builtin_bit_cast(f32x4, st[2 * n + 1]); }
      sa = __builtin_bit_cast(v4u, a); sb = __builtin_bit_cast(v4u, bq);
      const f32x4 t = a + bq;
      const float u = (t.x + t.y) + (t.z + t.w);
      unsigned m = 0u;
      if (__builtin_expect(__builtin_amdgcn_ballot_w64(u != u) != 0ull, 0)) {
#pragma unroll
        for (int i = 0; i < 16; ++i) m = mxacc(m, st[i]);
      }
      again = (__builtin_amdgcn_ballot_w64(m == SENT) | bit0_set(tagacc(tagacc(0u, zr, 0u), zi, 0u))) != 0ull;
    } while (__builtin_expect(again, 0) && ++spin < SPIN_MAX);
  }
  constexpr int W0 = layer == 0 ? 6 : 16;
  load_ws<W0, W0 + 2>(W, ws);
#pragma unroll
  for (int e = 0; e < 4; ++e) {
    rstd[e] = __builtin_amdgcn_rsqf(__builtin_fmaf(asf(sa[e]), 1.f / 512.f, 1e-5f));
    rstd[4 + e] = __builtin_amdgcn_rsqf(__builtin_fmaf(asf(sb[e]), 1.f / 512.f, 1e-5f));
  }
  load_ws<W0 + 2, W0 + 4>(W, ws);
  STAMP(2);
  col_compute<W0 + 4>(L, rs, k, fr, fi, zr, zi, rstd, c, w, lane, layer == 0 ? OFF_ZA : OFF_ZB, W, ws);
  STAMP(3);
  CSTAMP(23);
  load_ws<W0 + 34, 64 - NW_LATE>(W, ws);
  if constexpr (layer == 1) __builtin_amdgcn_s_sleep(DELAY_A);
}

__global__ void __launch_bounds__(256, 1) k_main(Params p) {
  extern __shared__ __attribute__((aligned(16))) unsigned char smem[];
  LAS unsigned char* L = (LAS unsigned char*)smem;
  const rsrc_t rs = __builtin_amdgcn_make_buffer_rsrc((void*)p.ws, 0, WS_BYTES, 0x00020000);
  const v4u srd = make_srd(p.ws, WS_BYTES);
  unsigned long long tag = *(const unsigned long long*)(p.ws + OFF_CTL) + 0x9E3779B97F4A7C15ull;
  tag = (tag ^ (tag >> 30)) * 0xBF58476D1CE4E5B9ull;
  tag = (tag ^ (tag >> 27)) * 0x94D049BB133111EBull;
  tag ^= tag >> 31;
  prep(L, p, rs, tag);
  v4u W[64];
  column<0>(L, rs, srd, W, tag);
#pragma unroll 1
  for (int layer = 0; layer < 2; ++layer) {
    int tid = threadIdx.x;
    asm volatile("" : "+v"(tid));
    const int c = opaque(blockIdx.x);
    const int lane = tid & 63, w = __builtin_amdgcn_readfirstlane(tid >> 6);
    const Tile t(c);
    WSrc ws;
    ws.srd = srd; ws.srd1 = srd;
    if (w == 3 && layer == 0) ws.srd1.z = 0u;
    ws.lane16 = lane * 16; ws.base = OFF_W1 + (t.fs * 8 + 2 * w) * 32768;
    ffn(L, rs, ws, p, W, layer, c, t.rb, t.fs, t.pi, t.nb, w, lane);
    STAMP(18);
    if (layer == 0) column<1>(L, rs, srd, W, tag);
  }
}

extern "C" void kernel_launch(void* const* d_in, const int* in_sizes, int n_in, void* d_out, int out_size, void* d_ws, size_t ws_size,
                              hipStream_t stream) {
  Params p;
  p.x = (const u16*)d_in[0]; p.wr = (const u16*)d_in[1]; p.wi = (const u16*)d_in[2];
  p.wg = (const u16*)d_in[4]; p.wu = (const u16*)d_in[5]; p.wd = (const u16*)d_in[6];
  p.out = (u16*)d_out; p.ws = (unsigned char*)d_ws;
  (void)hipFuncSetAttribute((const void*)k_main, hipFuncAttributeMaxDynamicSharedMemorySize, M_LDS);
  k_main<<<256, 256, M_LDS, stream>>>(p);
}
```

```cpp
#include <hip/hip_runtime.h>
#include <hip/hip_bf16.h>

#define LAS __attribute__((address_space(3)))
#define DI __device__ __forceinline__
typedef unsigned short u16;
typedef unsigned v4u __attribute__((ext_vector_type(4)));
typedef unsigned v2u __attribute__((ext_vector_type(2)));
typedef float f32x2 __attribute__((ext_vector_type(2)));
typedef float f32x4 __attribute__((ext_vector_type(4)));
typedef float f32x16 __attribute__((ext_vector_type(16)));
typedef short s16x4 __attribute__((ext_vector_type(4)));
typedef __bf16 bf16x2 __attribute__((ext_vector_type(2)));
typedef __bf16 bf16x8 __attribute__((ext_vector_type(8)));
typedef __amdgpu_buffer_rsrc_t rsrc_t;

constexpr int MB = 1 << 20;
constexpr int ZS = 4224, XS = 1152, WS_ = 8320;
constexpr int OFF_ZA = 0, OFF_ZB = 9 * MB / 4;
constexpr int OFF_WT = 9 * MB;
constexpr int OFF_W1 = 12 * MB;
constexpr int OFF_W2 = 16 * MB;
constexpr int OFF_W3 = 18 * MB;
constexpr int OFF_H = 20 * MB;
constexpr int OFF_X3 = 36 * MB;
constexpr int OFF_R0 = 39 * MB;
constexpr int OFF_ST = 39 * MB + 65536;
constexpr int OFF_CTL = 39 * MB + 2 * 65536;
constexpr int OFF_FL = OFF_CTL + 4096;
constexpr int WS_BYTES = 40 * MB;
constexpr int SC1 = 16;
constexpr unsigned SENT = 0xFFFFFFFFu;
constexpr unsigned SPIN_MAX = 1u << 16;

struct Params {
  const u16* x; const u16* wr; const u16* wi; const u16* wg; const u16* wu; const u16* wd;
  u16* out; unsigned char* ws;
};

DI unsigned pk(float a, float b) { f32x2 v = {a, b}; return __builtin_bit_cast(unsigned, __builtin_convertvector(v, bf16x2)); }
DI float asf(unsigned u) { return __builtin_bit_cast(float, u); }
DI unsigned asu(float f) { return __builtin_bit_cast(unsigned, f); }
DI bf16x2 asb2(unsigned u) { return __builtin_bit_cast(bf16x2, u); }
template <int V> struct IC { static constexpr int v = V; };
template <int I, int N, class Fn> DI void sfor(Fn&& fn) { if constexpr (I < N) { fn(IC<I>{}); sfor<I + 1, N>(fn); } }
DI int opaque(int x) { asm volatile("" : "+s"(x)); return x; }
DI int is_zero(int x) { int n; asm volatile("s_min_u32 %0, %1, 1" : "=s"(n) : "s"(x) : "scc"); return 1 - n; }
DI float blo(unsigned u) { return __builtin_bit_cast(float, u << 16); }
DI float bhi(unsigned u) { return __builtin_bit_cast(float, u & 0xFFFF0000u); }
DI float rbf(float a) { return blo(pk(a, a)); }
DI void pk_tag(float a0, float b0, float a1, float b1, unsigned tag, unsigned& d0, unsigned& d1) {
  const unsigned c = pk(b0, b1), up = (tag ^ 1u) << 16;
  d0 = (__builtin_amdgcn_perm(c, asu(a0) + up, 0x05040302u) & ~1u) | tag;
  d1 = (__builtin_amdgcn_perm(c, asu(a1) + up, 0x07060302u) & ~1u) | tag;
}
DI unsigned nosent(unsigned u) { return u < 0xFFFFFFFEu ? u : 0xFFFFFFFEu; }
DI v4u nosent4(v4u v) { v.x = nosent(v.x); v.y = nosent(v.y); v.z = nosent(v.z); v.w = nosent(v.w); return v; }
DI unsigned umax(unsigned a, unsigned b) { return a > b ? a : b; }
DI unsigned mx4(v4u v) { return umax(umax(v.x, v.y), umax(v.z, v.w)); }
DI bool any_sent(unsigned m) { return __builtin_amdgcn_ballot_w64(m == SENT) != 0ull; }
DI f32x16 mfma32(v4u a, v4u b, f32x16 c) {
  return __builtin_amdgcn_mfma_f32_32x32x16_bf16(__builtin_bit_cast(bf16x8, a), __builtin_bit_cast(bf16x8, b), c, 0, 0, 0);
}
DI f32x4 mfma16(v4u a, v4u b, f32x4 c) {
  return __builtin_amdgcn_mfma_f32_16x16x32_bf16(__builtin_bit_cast(bf16x8, a), __builtin_bit_cast(bf16x8, b), c, 0, 0, 0);
}
template <bool FIRST, bool WA> DI void mfma_w(f32x16& acc, const v4u& wt, const v4u& x) {
  if constexpr (FIRST && WA) asm volatile("v_mfma_f32_32x32x16_bf16 %0, %1, %2, 0" : "=&v"(acc) : "a"(wt), "v"(x));
  if constexpr (FIRST && !WA) asm volatile("v_mfma_f32_32x32x16_bf16 %0, %2, %1, 0" : "=&v"(acc) : "a"(wt), "v"(x));
  if constexpr (!FIRST && WA) asm volatile("v_mfma_f32_32x32x16_bf16 %0, %1, %2, %0" : "+v"(acc) : "a"(wt), "v"(x));
  if constexpr (!FIRST && !WA) asm volatile("v_mfma_f32_32x32x16_bf16 %0, %2, %1, %0" : "+v"(acc) : "a"(wt), "v"(x));
}
template <bool FIRST> DI void mfma_v(f32x16& acc, const v4u& wt, const v4u& x) {
  if constexpr (FIRST) asm volatile("v_mfma_f32_32x32x16_bf16 %0, %2, %1, 0" : "=&v"(acc) : "v"(wt), "v"(x));
  else asm volatile("v_mfma_f32_32x32x16_bf16 %0, %2, %1, %0" : "+v"(acc) : "v"(wt), "v"(x));
}
template <bool FIRST> DI void mfma_vv(f32x16& acc, const v4u& a, const v4u& b) {
  if constexpr (FIRST) asm volatile("v_mfma_f32_32x32x16_bf16 %0, %1, %2, 0" : "=&v"(acc) : "v"(a), "v"(b));
  else asm volatile("v_mfma_f32_32x32x16_bf16 %0, %1, %2, %0" : "+v"(acc) : "v"(a), "v"(b));
}
template <int IMM> DI void vload(v4u& dst, const v4u& srd, int lane16, int so) {
  asm volatile("buffer_load_dwordx4 %0, %1, %2, %3 offen offset:%4 sc1" : "=v"(dst) : "v"(lane16), "s"(srd), "s"(so), "i"(IMM));
}
template <int IMM, bool REUSE> DI void wload(v4u& dst, const v4u& srd, int lane16, int so) {
  if constexpr (REUSE) asm volatile("buffer_load_dwordx4 %0, %1, %2, %3 offen offset:%4 sc1" : "+a"(dst) : "v"(lane16), "s"(srd), "s"(so), "i"(IMM));
  else asm volatile("buffer_load_dwordx4 %0, %1, %2, %3 offen offset:%4 sc1" : "=a"(dst) : "v"(lane16), "s"(srd), "s"(so), "i"(IMM));
}
DI void mfma_done(f32x16& a, f32x16& b, f32x16& c, f32x16& d) { asm volatile("s_nop 12" : "+v"(a), "+v"(b), "+v"(c), "+v"(d)); }
DI v2u trread(LAS unsigned char* p) { return __builtin_bit_cast(v2u, __builtin_amdgcn_ds_read_tr16_b64_v4i16((LAS s16x4*)p)); }
DI v4u tr2(LAS unsigned char* p, int d) { v2u a = trread(p), b = trread(p + d); return v4u{a.x, a.y, b.x, b.y}; }
DI v4u bload(rsrc_t rs, int voff, int soff) { return __builtin_amdgcn_raw_buffer_load_b128(rs, voff, soff, 0); }
DI v4u bload_sc1(rsrc_t rs, int voff, int soff) { return __builtin_amdgcn_raw_buffer_load_b128(rs, voff, soff, SC1); }
DI void bstore(v4u v, rsrc_t rs, int voff, int soff) { __builtin_amdgcn_raw_buffer_store_b128(v, rs, voff + soff, 0, 0); }
DI void bstore_sc1(v4u v, rsrc_t rs, int voff, int soff) { __builtin_amdgcn_raw_buffer_store_b128(v, rs, voff + soff, 0, SC1); }
template <int I> DI v4u fload(rsrc_t rs, int lane16, int base) { return bload(rs, lane16 + (I & 3) * 1024, base + (I >> 2) * 4096); }
template <int I> DI v4u fload_sc1(rsrc_t rs, int lane16, int base) { return bload_sc1(rs, lane16 + (I & 3) * 1024, base + (I >> 2) * 4096); }
#define SB() __builtin_amdgcn_sched_barrier(0)
#define MEMFENCE() asm volatile("" ::: "memory")
#define BAR() asm volatile("s_waitcnt lgkmcnt(0)\n\ts_barrier" ::: "memory")
template <int N> DI void vmwait() { asm volatile("s_waitcnt vmcnt(%0)" :: "i"(N) : "memory"); }
DI void pin(v4u& a) { asm volatile("" : "+v"(a)); }
DI f32x16 zero16() { f32x16 z; for (int i = 0; i < 16; ++i) z[i] = 0.f; return z; }
DI v4u pack8(const f32x16& a, int s, float sc) {
  return v4u{pk(a[8 * s] * sc, a[8 * s + 1] * sc), pk(a[8 * s + 2] * sc, a[8 * s + 3] * sc),
             pk(a[8 * s + 4] * sc, a[8 * s + 5] * sc), pk(a[8 * s + 6] * sc, a[8 * s + 7] * sc)};
}
DI void widen(const f32x16& a, float sc, v4u& o0, v4u& o1) {
  unsigned d[8];
#pragma unroll
  for (int i = 0; i < 8; ++i) d[i] = pk(a[2 * i] * sc, a[2 * i + 1] * sc);
  auto r0 = __builtin_amdgcn_permlane32_swap(d[0], d[2], false, false);
  auto r1 = __builtin_amdgcn_permlane32_swap(d[1], d[3], false, false);
  auto r2 = __builtin_amdgcn_permlane32_swap(d[4], d[6], false, false);
  auto r3 = __builtin_amdgcn_permlane32_swap(d[5], d[7], false, false);
  o0 = v4u{r0[0], r1[0], r0[1], r1[1]};
  o1 = v4u{r2[0], r3[0], r2[1], r3[1]};
}
template <int CTRL> DI float dppf(float v) {
  return __builtin_bit_cast(float, __builtin_amdgcn_update_dpp(0, __builtin_bit_cast(int, v), CTRL, 0xF, 0xF, true));
}
DI float sum32(float v) {
  v += dppf<0xB1>(v); v += dppf<0x4E>(v); v += dppf<0x141>(v); v += dppf<0x140>(v);
  unsigned u = __builtin_bit_cast(unsigned, v);
  auto r = __builtin_amdgcn_permlane16_swap(u, u, false, false);
  return __builtin_bit_cast(float, (unsigned)r[0]) + __builtin_bit_cast(float, (unsigned)r[1]);
}
DI int stat_of(int p) { return ((p >> 2) & 1) * 4096 + (p >> 3) * 16 + (p & 3) * 4; }
DI int token_of(int p) {
  const int l = (p >> 3) & 63, e = p & 7;
  return 32 * (16 * (p >> 9) + 4 * (l >> 4) + (e & 3)) + (l & 15) + 16 * (e >> 2);
}
DI v4u make_srd(const void* base, unsigned bytes) { return v4u{(unsigned)(size_t)base, (unsigned)((size_t)base >> 32) & 0xFFFFu, bytes, 0x00020000u}; }
DI void glds_in(const v4u& srd, int voff, int soff, unsigned lds) {
  asm volatile("s_mov_b32 m0, %3\n\ts_nop 0\n\tbuffer_load_dwordx4 %0, %1, %2 offen lds" :: "v"(voff), "s"(srd), "s"(soff), "s"(lds) : "memory");
}
template <int IMM> DI void ld2u16(unsigned& a, unsigned& b, int voff, const v4u& sa, const v4u& sb, int soff) {
  asm volatile("buffer_load_ushort %0, %2, %3, %5 offen offset:%6\n\tbuffer_load_ushort %1, %2, %4, %5 offen offset:%6"
               : "=&v"(a), "=&v"(b) : "v"(voff), "s"(sa), "s"(sb), "s"(soff), "i"(IMM));
}
DI v4u cosfrag(float a0, float step, int off) {
  float v[8];
#pragma unroll
  for (int j = 0; j < 8; ++j) v[j] = __builtin_amdgcn_cosf(__builtin_fmaf(step, (float)(off + j), a0));
  return v4u{pk(v[0], v[1]), pk(v[2], v[3]), pk(v[4], v[5]), pk(v[6], v[7])};
}

constexpr int M_Z = 0;
constexpr int M_RB = 40960;
constexpr int M_TB = 81920;
constexpr int M_S1 = 131072;
constexpr int M_S2 = M_S1 + 2048;
constexpr int M_C1 = M_S2 + 1024;
constexpr int M_C2 = M_C1 + 2 * 4608;
constexpr int M_LDS = M_C2 + 2 * 4608;
#define STAMP(i)
#define PST(i)
#define KSTAMP(i)
#define CSTAMP(i)
#define RETRY(i)
#define DELAY_A 6
#define DELAY_B 4
#define DELAY_C 2

template <int IMM> DI void ldx4(v4u& dst, const v4u& srd, int voff, int soff) {
  asm volatile("buffer_load_dwordx4 %0, %1, %2, %3 offen offset:%4 sc1" : "=v"(dst) : "v"(voff), "s"(srd), "s"(soff), "i"(IMM));
}
struct WSrc { v4u srd, srd1; int lane16, base; };
template <int N> DI void load_w(v4u (&W)[64], const WSrc& s) {
  wload<((N >> 1) & 3) * 1024, false>(W[N], (N & 1) ? s.srd1 : s.srd, s.lane16, s.base + (N & 1) * 32768 + (N >> 3) * 4096);
}
template <int LO, int HI> DI void load_ws(v4u (&W)[64], const WSrc& s) { SB(); sfor<LO, HI>([&](auto J) { load_w<J.v>(W, s); }); SB(); }
DI unsigned mxacc(unsigned m, const v4u& v) { return umax(umax(umax(umax(m, v.x), v.y), v.z), v.w); }
DI unsigned tagacc(unsigned m, const v4u& v, unsigned tag) { return m | ((v.x ^ tag) | (v.y ^ tag)) | ((v.z ^ tag) | (v.w ^ tag)); }
DI unsigned andacc(unsigned m, const v4u& v) { return (m & v.x & v.y) & (v.z & v.w); }
DI unsigned long long bit0_set(unsigned m) { return __builtin_amdgcn_ballot_w64((m & 1u) != 0u); }
DI unsigned long long bit0_clear(unsigned m) { return __builtin_amdgcn_ballot_w64((m & 1u) == 0u); }
DI void pins(unsigned long long& k) { asm volatile("" : "+s"(k)); }
DI void pinf(float& a) { asm volatile("" : "+v"(a)); }
DI void pinu(unsigned& a) { asm volatile("" : "+v"(a)); }

DI void publish(rsrc_t rs, int which, int c, unsigned long long tag, int w, int lane) {
  if (w == 0 && lane < 8)
    __builtin_amdgcn_raw_buffer_store_b64(v2u{(unsigned)tag, (unsigned)(tag >> 32)}, rs, OFF_FL + which * 16384 + lane * 2048 + c * 8, 0, SC1);
}
DI void flags_load(v4u (&f)[2], const v4u& srd, int which, int c, int lane) {
  ldx4<0>(f[0], srd, lane * 16, OFF_FL + which * 16384 + (c & 7) * 2048);
  ldx4<1024>(f[1], srd, lane * 16, OFF_FL + which * 16384 + (c & 7) * 2048);
}
DI void flags_wait(v4u (&f)[2], const v4u& srd, int which, int c, int lane, unsigned long long tag) {
  const unsigned lo = (unsigned)tag, hi = (unsigned)(tag >> 32);
  auto missing = [&]() {
    pin(f[0]); pin(f[1]);
    const unsigned d = ((f[0].x ^ lo) | (f[0].y ^ hi) | (f[0].z ^ lo)) | ((f[0].w ^ hi) | (f[1].x ^ lo) | (f[1].y ^ hi)) | ((f[1].z ^ lo) | (f[1].w ^ hi));
    return __builtin_amdgcn_ballot_w64(d != 0u) != 0ull;
  };
  if (__builtin_expect(missing(), 0)) {
    unsigned spin = 0;
    do { __builtin_amdgcn_s_sleep(2); flags_load(f, srd, which, c, lane); vmwait<0>(); } while (missing() && ++spin < SPIN_MAX);
  }
}

struct ColConst { v4u C1[2], S1[2], N1[2], C2[2], S2[2], N2[2]; float tc[8], ts[8]; };

template <bool LOADS> DI void col_consts(ColConst& k, int w, int lane, v4u (&W)[64], const WSrc& ws) {
  const int g = lane >> 4, r = lane & 15;
  const float k1 = (float)(16 * w + r);
  const unsigned neg = 0x80008000u;
  sfor<0, 2>([&](auto CS) {
    constexpr int cs = CS.v;
    const float st = k1 * (1.f / 64.f), a0 = st * (float)(8 * g);
    if constexpr (LOADS) load_ws<3 * cs, 3 * cs + 2>(W, ws);
    k.C1[cs] = cosfrag(a0, st, 32 * cs);
    pin(k.C1[cs]);
    if constexpr (LOADS) load_ws<3 * cs + 2, 3 * cs + 3>(W, ws);
    k.S1[cs] = cosfrag(a0 - 0.25f, st, 32 * cs);
    k.N1[cs] = k.S1[cs] ^ neg;
    pin(k.S1[cs]); pin(k.N1[cs]);
  });
  sfor<0, 2>([&](auto T) {
    constexpr int t = T.v;
    const float st = (float)(16 * t + r) * (1.f / 32.f), a0 = st * (float)(4 * g);
    float c[8], s[8];
    if constexpr (LOADS) load_ws<6 + 3 * t, 8 + 3 * t>(W, ws);
#pragma unroll
    for (int j = 0; j < 8; ++j) c[j] = __builtin_amdgcn_cosf(__builtin_fmaf(st, (float)(16 * (j >> 2) + (j & 3)), a0));
    k.C2[t] = v4u{pk(c[0], c[1]), pk(c[2], c[3]), pk(c[4], c[5]), pk(c[6], c[7])};
    pin(k.C2[t]);
    if constexpr (LOADS) load_ws<8 + 3 * t, 9 + 3 * t>(W, ws);
#pragma unroll
    for (int j = 0; j < 8; ++j) s[j] = __builtin_amdgcn_sinf(__builtin_fmaf(st, (float)(16 * (j >> 2) + (j & 3)), a0));
    k.S2[t] = v4u{pk(s[0], s[1]), pk(s[2], s[3]), pk(s[4], s[5]), pk(s[6], s[7])};
    k.N2[t] = k.S2[t] ^ neg;
    pin(k.S2[t]); pin(k.N2[t]);
  });
  sfor<0, 2>([&](auto T) {
    constexpr int t = T.v;
    if constexpr (LOADS) load_ws<12 + 2 * t, 14 + 2 * t>(W, ws);
#pragma unroll
    for (int e = 4 * t; e < 4 * t + 4; ++e) {
      const float a = k1 * (1.f / 2048.f) * (float)(16 * (e >> 2) + 4 * g + (e & 3));
      k.tc[e] = __builtin_amdgcn_cosf(a); k.ts[e] = __builtin_amdgcn_sinf(a);
      pinf(k.tc[e]); pinf(k.ts[e]);
    }
  });
}

template <bool FIRST> DI void m16(f32x4& acc, const v4u& a, const v4u& b) {
  SB();
  if constexpr (FIRST) asm volatile("v_mfma_f32_16x16x32_bf16 %0, %1, %2, 0" : "=&v"(acc) : "v"(a), "v"(b));
  else asm volatile("v_mfma_f32_16x16x32_bf16 %0, %1, %2, %0" : "+v"(acc) : "v"(a), "v"(b));
  SB();
}
DI void m16_done(f32x4& a, f32x4& b) { asm volatile("s_nop 7" : "+v"(a), "+v"(b)); }
DI void m16_ready(v4u& a, v4u& b) { asm volatile("s_nop 1" : "+v"(a), "+v"(b)); }
template <int PART> DI float cmul(float a, float b, float c, float s) {
  float r = PART == 0 ? __builtin_fmaf(a, c, -(b * s)) : __builtin_fmaf(a, s, b * c);
  pinf(r);
  return r;
}

template <int WB>
DI void col_compute(LAS unsigned char* L, rsrc_t rs, const ColConst& k, const float (&fr)[8], const float (&fi)[8], v4u zr, v4u zi,
                    const float (&rstd)[8], int c, int w, int lane, int out_off, v4u (&W)[64], const WSrc& ws) {
  const int g = lane >> 4, r = lane & 15;
  const int first = is_zero(c);
  const float f0 = first ? 0.f : 1.f;
  float xr[8], xi[8];
#pragma unroll
  for (int i = 0; i < 4; ++i) { xr[2 * i] = blo(zr[i]); xr[2 * i + 1] = bhi(zr[i]); xi[2 * i] = blo(zi[i]); xi[2 * i + 1] = bhi(zi[i]); }
#pragma unroll
  for (int t = 0; t < 2; ++t) {
    const f32x4 rv = {rstd[4 * t], rstd[4 * t + 1], rstd[4 * t + 2], rstd[4 * t + 3]};
    const f32x4 a = f32x4{xr[4 * t], xr[4 * t + 1], xr[4 * t + 2], xr[4 * t + 3]} * rv;
    const f32x4 bq = f32x4{xi[4 * t], xi[4 * t + 1], xi[4 * t + 2], xi[4 * t + 3]} * (rv * f0);
    LAS unsigned char* d = L + M_C1 + (r + 16 * t) * 144 + (16 * w + 4 * g) * 2;
    *(LAS v2u*)d = v2u{pk(a[0], a[1]), pk(a[2], a[3])};
    *(LAS v2u*)(d + 4608) = v2u{pk(bq[0], bq[1]), pk(bq[2], bq[3])};
  }
  load_ws<WB, WB + 3>(W, ws);
  BAR();
  f32x4 dr[2], di[2];
  float ur[8], ui[8];
  {
    v4u ar[2][2], ai[2][2];
#pragma unroll
    for (int t = 0; t < 2; ++t)
#pragma unroll
      for (int cs = 0; cs < 2; ++cs) {
        LAS unsigned char* s = L + M_C1 + (r + 16 * t) * 144 + (32 * cs + 8 * g) * 2;
        ar[t][cs] = *(LAS v4u*)s; ai[t][cs] = *(LAS v4u*)(s + 4608);
      }
    load_ws<WB + 3, WB + 5>(W, ws);
    sfor<0, 16>([&](auto N) {
      constexpr int n = N.v, t = n >> 3, cs = (n >> 2) & 1, m = n & 3;
      if constexpr (m == 0) m16<cs == 0>(dr[t], ar[t][cs], k.C1[cs]);
      if constexpr (m == 1) m16<cs == 0>(di[t], ai[t][cs], k.C1[cs]);
      if constexpr (m == 2) m16<false>(dr[t], ai[t][cs], k.S1[cs]);
      if constexpr (m == 3) m16<false>(di[t], ar[t][cs], k.N1[cs]);
      if constexpr (m == 3) load_w<WB + 5 + (n >> 2)>(W, ws);
      if constexpr (n >= 9) {
        constexpr int e = (n - 9) >> 1;
        if constexpr (n & 1) ur[e] = cmul<0>(dr[0][e], di[0][e], k.tc[e], -k.ts[e]);
        else ui[e] = cmul<1>(dr[0][e], di[0][e], k.tc[e], -k.ts[e]);
      }
    });
    ui[3] = cmul<1>(dr[0][3], di[0][3], k.tc[3], -k.ts[3]);
  }
  v4u br, bi;
  br.x = pk(ur[0], ur[1]); br.y = pk(ur[2], ur[3]); bi.x = pk(ui[0], ui[1]); bi.y = pk(ui[2], ui[3]);
  SB();
  m16_done(dr[1], di[1]);
  load_ws<WB + 9, WB + 11>(W, ws);
#pragma unroll
  for (int e = 4; e < 8; ++e) {
    ur[e] = dr[1][e - 4] * k.tc[e] + di[1][e - 4] * k.ts[e]; ui[e] = di[1][e - 4] * k.tc[e] - dr[1][e - 4] * k.ts[e];
  }
  br.z = pk(ur[4], ur[5]); br.w = pk(ur[6], ur[7]); bi.z = pk(ui[4], ui[5]); bi.w = pk(ui[6], ui[7]);
  m16_ready(br, bi);
  sfor<0, 8>([&](auto N) {
    constexpr int n = N.v, t = n >> 2, m = n & 3;
    if constexpr (m == 0) m16<true>(dr[t], k.C2[t], br);
    if constexpr (m == 1) m16<true>(di[t], k.C2[t], bi);
    if constexpr (m == 2) m16<false>(dr[t], k.S2[t], bi);
    if constexpr (m == 3) m16<false>(di[t], k.N2[t], br);
    if constexpr (m == 3) load_w<WB + 11 + t>(W, ws);
    if constexpr (n == 5) ur[0] = cmul<0>(dr[0][0], di[0][0], fr[0], fi[0]);
    if constexpr (n == 6) ui[0] = cmul<1>(dr[0][0], di[0][0], fr[0], fi[0]);
  });
#pragma unroll
  for (int e = 1; e < 4; ++e) { ur[e] = dr[0][e] * fr[e] - di[0][e] * fi[e]; ui[e] = dr[0][e] * fi[e] + di[0][e] * fr[e]; }
  br.x = pk(ur[0], ur[1]); br.y = pk(ur[2], ur[3]); bi.x = pk(ui[0], ui[1]); bi.y = pk(ui[2], ui[3]);
  SB();
  m16_done(dr[1], di[1]);
  load_ws<WB + 13, WB + 15>(W, ws);
#pragma unroll
  for (int e = 4; e < 8; ++e) { ur[e] = dr[1][e - 4] * fr[e] - di[1][e - 4] * fi[e]; ui[e] = dr[1][e - 4] * fi[e] + di[1][e - 4] * fr[e]; }
  br.z = pk(ur[4], ur[5]); br.w = pk(ur[6], ur[7]); bi.z = pk(ui[4], ui[5]); bi.w = pk(ui[6], ui[7]);
  m16_ready(br, bi);
  sfor<0, 8>([&](auto N) {
    constexpr int n = N.v, t = n >> 2, m = n & 3;
    if constexpr (m == 0) m16<true>(dr[t], k.C2[t], br);
    if constexpr (m == 1) m16<true>(di[t], k.C2[t], bi);
    if constexpr (m == 2) m16<false>(dr[t], k.N2[t], bi);
    if constexpr (m == 3) m16<false>(di[t], k.S2[t], br);
    if constexpr (m == 3) load_w<WB + 15 + t>(W, ws);
    if constexpr (n == 5) ur[0] = cmul<0>(dr[0][0], di[0][0], k.tc[0], k.ts[0]);
    if constexpr (n == 6) ui[0] = cmul<1>(dr[0][0], di[0][0], k.tc[0], k.ts[0]);
  });
#pragma unroll
  for (int t = 0; t < 2; ++t) {
    if (t == 1) { SB(); m16_done(dr[1], di[1]); load_ws<WB + 17, WB + 19>(W, ws); }
#pragma unroll
    for (int i = (t == 0 ? 1 : 0); i < 4; ++i) {
      const int e = 4 * t + i;
      ur[e] = dr[t][i] * k.tc[e] - di[t][i] * k.ts[e]; ui[e] = di[t][i] * k.tc[e] + dr[t][i] * k.ts[e];
    }
    LAS unsigned char* d = L + M_C2 + (16 * w + r) * 72 + (16 * t + 4 * g) * 2;
    *(LAS v2u*)d = v2u{pk(ur[4 * t], ur[4 * t + 1]), pk(ur[4 * t + 2], ur[4 * t + 3])};
    *(LAS v2u*)(d + 4608) = v2u{pk(ui[4 * t], ui[4 * t + 1]), pk(ui[4 * t + 2], ui[4 * t + 3])};
  }
  load_ws<WB + 19, WB + 21>(W, ws);
  BAR();
  unsigned od[8];
  {
    v4u tr_[2][2], ti_[2][2];
#pragma unroll
    for (int t = 0; t < 2; ++t)
#pragma unroll
      for (int cs = 0; cs < 2; ++cs) {
        LAS unsigned char* s = L + M_C2 + (32 * cs + 8 * g + (r >> 2)) * 72 + (16 * t + 4 * (r & 3)) * 2;
        tr_[t][cs] = tr2(s, 4 * 72); ti_[t][cs] = tr2(s + 4608, 4 * 72);
      }
    load_ws<WB + 21, WB + 23>(W, ws);
    sfor<0, 16>([&](auto N) {
      constexpr int n = N.v, t = n >> 3, cs = (n >> 2) & 1, m = n & 3;
      if constexpr (m == 0) m16<cs == 0>(dr[t], k.C1[cs], tr_[t][cs]);
      if constexpr (m == 1) m16<cs == 0>(di[t], k.C1[cs], ti_[t][cs]);
      if constexpr (m == 2) m16<false>(dr[t], k.N1[cs], ti_[t][cs]);
      if constexpr (m == 3) m16<false>(di[t], k.S1[cs], tr_[t][cs]);
      if constexpr (m == 3) load_w<WB + 23 + (n >> 2)>(W, ws);
      if constexpr (n >= 9 && n < 13) {
        constexpr int e = n - 9;
        xr[e] += dr[0][e]; xi[e] = __builtin_fmaf(di[0][e], f0, xi[e]);
        pinf(xr[e]); pinf(xi[e]);
      }
      if constexpr (n == 14) { pk_tag(xr[0], xr[1], xr[2], xr[3], 1u, od[0], od[1]); pinu(od[0]); pinu(od[1]); }
    });
    pk_tag(xi[0], xi[1], xi[2], xi[3], 1u, od[4], od[5]);
    SB();
    m16_done(dr[1], di[1]);
#pragma unroll
    for (int e = 4; e < 8; ++e) { xr[e] += dr[1][e - 4]; xi[e] = __builtin_fmaf(di[1][e - 4], f0, xi[e]); }
    pk_tag(xr[4], xr[5], xr[6], xr[7], 1u, od[2], od[3]);
    pk_tag(xi[4], xi[5], xi[6], xi[7], 1u, od[6], od[7]);
  }
  const int vo = (w * 64 + lane) * 16;
  bstore_sc1(v4u{od[0], od[1], od[2], od[3]}, rs, vo, out_off + (2 * c + first) * ZS);
  bstore_sc1(v4u{od[4], od[5], od[6], od[7]}, rs, vo, out_off + (2 * c + 1 - first) * ZS);
  load_ws<WB + 27, WB + 30>(W, ws);
}

constexpr int NW_LATE = 8;
constexpr int NV_EARLY = 16;
constexpr int ZG[6] = {0, 2, 4, 8, 16, 32};
constexpr int ZR[5] = {-1, 1, 4, 10, 21};
constexpr int z_read_at(int ks) {
  for (int G = 1; G < 5; ++G) if (ks >= ZR[G] && 2 * (ks - ZR[G]) < ZG[G + 1] - ZG[G]) return ZG[G] + 2 * (ks - ZR[G]);
  return -1;
}
constexpr int z_first(int j) { for (int G = 0; G < 5; ++G) if (ZG[G] == j) return 1; return 0; }
constexpr int z_decide_at(int ks) { for (int G = 1; G < 5; ++G) if (ks == ZR[G] + (ZG[G + 1] - ZG[G]) / 2) return G; return -1; }
constexpr int z_requested(int ks) { return ks < 8 ? 9 + ks : ks < 16 ? 2 * ks + 1 : 32; }
constexpr int z_younger(int j, int ks) {
  int n = z_requested(ks) - j - 1;
  for (int k = 0; k < ks; ++k) if ((k < 8 && j < 16 && k >= j - 8) || k >= 16) ++n;
  return n;
}
struct ZSrc { v4u srd; int vo, so; unsigned lds; };
template <int J> DI void z_issue(const ZSrc& z, int so) {
  asm volatile("s_add_u32 m0, %0, %4\n\ts_nop 0\n\tbuffer_load_dwordx4 %1, %2, %3 offen sc1 lds"
               :: "s"(z.lds), "v"(z.vo), "s"(z.srd), "s"(so + (J & 15) * 32 * ZS + (J >> 4) * 128), "i"((J >> 4) * 65536 + (J & 15) * 4096)
               : "memory", "scc");
}
template <int J> DI v4u z_back(LAS unsigned char* zown) { return *(LAS v4u*)(zown + (J >> 4) * 65536 + (J & 15) * 4096); }
template <int G, int KS> DI void z_fix(unsigned long long bad, const ZSrc& z, LAS unsigned char* zown, LAS unsigned char* L, int layer, int w) {
  if (__builtin_expect(bad != 0ull, 0)) {
    unsigned spin = 0, m;
    vmwait<0>();
    do {
      RETRY(G == 0 ? 24 : 25);
      const int zo = opaque(z.so);
      sfor<ZG[G], z_requested(KS)>([&](auto I) { z_issue<I.v>(z, zo); });
      vmwait<0>();
      m = 1u;
      sfor<ZG[G], ZG[G + 1]>([&](auto I) { m = andacc(m, z_back<I.v>(zown)); });
    } while (bit0_clear(m) != 0ull && ++spin < SPIN_MAX);
  }
}

#define HA_START 14
#define HB_START 6
#define H_ROOM 16
constexpr int NSTR = 56 - NV_EARLY + 58;
struct Stream { int kind[NSTR], idx[NSTR], end[89]; };
constexpr Stream make_stream() {
  Stream s{};
  int kind[NSTR] = {}, idx[NSTR] = {}, rel[NSTR] = {}, need[NSTR] = {};
  bool done[NSTR] = {};
  int n = 0;
  for (int i = NV_EARLY; i < 56; ++i) { kind[n] = 0; idx[n] = i; rel[n] = (i - NV_EARLY) >> 1; need[n] = i >> 1; ++n; }
  for (int i = 0; i < 28; ++i) { kind[n] = 1; idx[n] = i; rel[n] = HA_START; need[n] = i; ++n; }
  for (int i = 0; i < 28; ++i) { kind[n] = 2; idx[n] = i; rel[n] = 33 + HB_START; need[n] = 28 + i; ++n; }
  for (int i = 0; i < 2; ++i) { kind[n] = 3; idx[n] = i; rel[n] = 33 + HB_START; need[n] = 60; ++n; }
  int e = 0, held = 0;
  for (int sl = 0; sl < 89; ++sl) {
    int cap = sl < 8 ? 1 : sl < 32 ? 2 - (sl % 4 == 3) : sl == 32 ? 6 : (sl - 33) % 7 == 6 ? 0 : 2;
    for (; cap > 0; --cap) {
      int best = -1;
      const int live = held - (sl < 33 ? 0 : sl - 32), room = sl < 32 ? H_ROOM : H_ROOM + 6;
      for (int i = 0; i < NSTR; ++i)
        if (!done[i] && rel[i] <= sl && (kind[i] == 0 || live < room) && (best < 0 || need[i] < need[best])) best = i;
      if (best < 0) break;
      if (kind[best] != 0) ++held;
      done[best] = true; s.kind[e] = kind[best]; s.idx[e] = idx[best]; ++e;
    }
    s.end[sl] = e;
  }
  return s;
}
constexpr Stream STR = make_stream();
static_assert(STR.end[88] == NSTR, "every load is issued");
constexpr int str_pos(int kind, int idx) { for (int i = 0; i < NSTR; ++i) if (STR.kind[i] == kind && STR.idx[i] == idx) return i; return -1; }
constexpr int str_count(int kind, int upto) { int n = 0; for (int i = 0; i < upto; ++i) if (STR.kind[i] == kind) ++n; return n; }
struct Down {
  int vbase, hbase, xvo, xso;
  DI int blk(int g) const { return g * 4096; }
};
template <int SLOT, int PART> DI void stream_issue(v4u (&W)[64], v4u (&hA)[28], v4u (&hB)[28], v4u (&x3)[2], const v4u& srd, int lane16, const Down& d) {
  constexpr int lo = SLOT == 0 ? 0 : STR.end[SLOT - 1], hi = STR.end[SLOT];
  sfor<(PART == 1 ? lo + 1 : lo), (PART == 0 && hi > lo ? lo + 1 : hi)>([&](auto I) {
    constexpr int k = STR.kind[I.v], i = STR.idx[I.v];
    if constexpr (k == 0) wload<((i >> 1) & 3) * 1024, true>(W[i - NV_EARLY], srd, lane16, d.vbase + (i & 1) * 112 * 1024 + d.blk(i >> 3));
    if constexpr (k == 1) ldx4<(i & 3) * 1024>(hA[i], srd, lane16, d.hbase + d.blk(i >> 2));
    if constexpr (k == 2) ldx4<(i & 3) * 1024>(hB[i], srd, lane16, d.hbase + 224 * 1024 + d.blk(i >> 2));
    if constexpr (k == 3) ldx4<0>(x3[i], srd, d.xvo, d.xso + i * 64 * XS);
  });
}
constexpr int dn_need(int g) {
  const int H = g / 28, i = g % 28;
  const int ph = str_pos(1 + H, i), pv = H == 0 && 2 * i >= NV_EARLY ? str_pos(0, 2 * i + 1) : 0;
  return ph > pv ? ph : pv;
}
template <int G, int ISSUED> DI void h_acc(v4u (&hA)[28], v4u (&hB)[28], unsigned& m, unsigned tag) {
  static_assert(dn_need(G) < ISSUED, "requested in time");
  vmwait<ISSUED - 1 - dn_need(G)>();
  v4u& x = G < 28 ? hA[G % 28] : hB[G % 28];
  pin(x);
  m = tagacc(m, x, tag);
  pinu(m);
}
template <int G, int ISSUED> DI void h_decide(v4u (&hA)[28], v4u (&hB)[28], unsigned long long bad, const v4u& srd, int lane16, const Down& d,
                                              LAS unsigned char* L, int layer, int w) {
  constexpr int H = G / 28, LO = G % 28;
  v4u (&hh)[28] = H == 0 ? hA : hB;
  if (__builtin_expect(bad != 0ull, 0)) {
    unsigned spin = 0, m;
    vmwait<0>();
    do {
      RETRY(26 + H);
      const int bo = opaque(d.hbase);
      sfor<LO, str_count(1 + H, ISSUED)>([&](auto I) { ldx4<(I.v & 3) * 1024>(hh[I.v], srd, lane16, bo + H * 224 * 1024 + d.blk(I.v >> 2)); });
      vmwait<0>();
      m = 0;
#pragma unroll
      for (int i = LO; i < LO + 7; ++i) { pin(hh[i]); m = tagacc(m, hh[i], layer); }
    } while (bit0_set(m) != 0ull && ++spin < SPIN_MAX);
  }
}

DI void sumsq1(float& ss, unsigned a) {
  const float x = blo(a), y = bhi(a);
  asm volatile("v_fmac_f32 %0, %1, %1\n\tv_fmac_f32 %0, %2, %2" : "+v"(ss) : "v"(x), "v"(y));
}
DI float mulf(float a, float b) { float r; asm volatile("v_mul_f32 %0, %1, %2" : "=v"(r) : "v"(a), "v"(b)); return r; }
DI void addf4(f32x4& a, const f32x4& b) {
  float x = a.x, y = a.y, z = a.z, t = a.w;
  asm volatile("v_add_f32 %0, %0, %4\n\tv_add_f32 %1, %1, %5\n\tv_add_f32 %2, %2, %6\n\tv_add_f32 %3, %3, %7"
               : "+v"(x), "+v"(y), "+v"(z), "+v"(t) : "v"(b.x), "v"(b.y), "v"(b.z), "v"(b.w));
  a = f32x4{x, y, z, t};
}
DI float relu(float a) { const int i = __builtin_bit_cast(int, a); return __builtin_bit_cast(float, i > 0 ? i : 0); }
DI void h_piece(const f32x16& a, unsigned tag, rsrc_t rs, int lane, int so) {
  unsigned d[4];
#pragma unroll
  for (int i = 0; i < 2; ++i)
    pk_tag(relu(a[4 * i]) * a[4 * i + 8], relu(a[4 * i + 1]) * a[4 * i + 9], relu(a[4 * i + 2]) * a[4 * i + 10], relu(a[4 * i + 3]) * a[4 * i + 11], tag,
           d[2 * i], d[2 * i + 1]);
  bstore_sc1(v4u{d[0], d[1], d[2], d[3]}, rs, lane * 16, so);
}
DI void x3_piece(const f32x16& a, rsrc_t rs, int lane, int so) {
  v4u o0, o1;
  widen(a, 1.f, o0, o1);
  bstore_sc1(nosent4(o0), rs, (lane & 31) * XS + (lane >> 5) * 16, so);
  bstore_sc1(nosent4(o1), rs, (lane & 31) * XS + (lane >> 5) * 16, so + 32);
}

DI void ffn(LAS unsigned char* L, rsrc_t rs, const WSrc& ws, const Params& p, v4u (&W)[64], int layer, int c, int rb, int fs, int pi, int nb,
            int w, int lane) {
  const v4u& srd = ws.srd;
  constexpr int hoff = OFF_H;
  const int h = lane >> 5, r = lane & 31, q = (lane & 15) >> 2, lane16 = lane * 16;
  ZSrc z;
  z.srd = srd;
  z.vo = (8 * w + (lane >> 3)) * ZS + (((lane & 7) ^ ((lane >> 2) & 4)) * 16);
  z.so = OFF_ZA + layer * (OFF_ZB - OFF_ZA) + rb * 256;
  z.lds = (unsigned)(size_t)L + M_Z + w * 1024;
  STAMP(4);
  sfor<0, 8>([&](auto I) { z_issue<I.v>(z, z.so); });
  SB();
  LAS unsigned char* zown = L + M_Z + w * 1024 + lane16;
  LAS unsigned char* zr[2];
#pragma unroll
  for (int s = 0; s < 2; ++s)
    zr[s] = L + M_Z + (4 * h + q) * 128 + (4 * (s ^ (q >> 1)) + 2 * ((lane >> 4) & 1) + ((lane & 3) >> 1)) * 16 + 8 * (lane & 1);
  const unsigned dcmask = (w == 0 && h == 0) ? 0xFFFF0000u : 0xFFFFFFFFu;
  const int pt = 4 * rb + pi;
  Down dn;
  dn.vbase = OFF_W2 + layer * (OFF_W3 - OFF_W2) + (nb * 2 * 112 + 28 * w) * 1024;
  dn.hbase = hoff + (pt * 112 + 28 * w) * 1024;
  dn.xvo = (lane & 31) * XS + h * 16;
  dn.xso = OFF_W1 + layer * (OFF_X3 - OFF_W1) + pt * 32 * XS + nb * 128 + w * 32;
  const int ks0 = fs * 7 + 2 * w;
  const bool two = w < 3;
  const bool idft = opaque(w + layer) == 4;
  const rsrc_t rs2 = __builtin_amdgcn_make_buffer_rsrc((void*)p.ws, 0, two ? WS_BYTES : 0, 0x00020000);
  v4u hA[28], hB[28], x3[2], res[2], Ve[NV_EARLY];
  unsigned s0, s1, s2, s3;
  asm volatile("v_mov_b32 %0, -1\n\tv_mov_b32 %1, -1\n\tv_mov_b32 %2, -1\n\tv_mov_b32 %3, -1" : "=v"(s0), "=v"(s1), "=v"(s2), "=v"(s3));
  const v4u sv = {s0, s1, s2, s3};
  const rsrc_t rsf = __builtin_amdgcn_make_buffer_rsrc((void*)p.ws, 0, layer == 0 ? WS_BYTES : 0, 0x00020000);
  const int fo_z = (w * 64 + lane) * 16 + OFF_ZB + 2 * c * ZS,
            fo_x = (w * 16 + (lane >> 2)) * XS + (lane & 3) * 16 + OFF_X3 + 128 * rb * XS + fs * 64;
  STAMP(5);
  vmwait<z_younger(1, -1)>();
  z_fix<0, -1>(bit0_clear(andacc(andacc(1u, z_back<0>(zown)), z_back<1>(zown))), z, zown, L, layer, w);
  STAMP(6);
  BAR();
  STAMP(7);
  CSTAMP(20);
  f32x16 acc[2][2][2];
  float ss[2] = {0.f, 0.f};
  sfor<0, 2>([&](auto HH) {
    constexpr int H = HH.v;
    v4u bf[2][2], sq[2], rbk[2];
    unsigned hq[4];
    float hp[4];
    unsigned zm;
    unsigned long long bad;
    LAS unsigned char* zb[2] = {zr[0] + H * 65536, zr[1] + H * 65536};
    asm volatile("" : "+v"(zb[0]), "+v"(zb[1]));
    LAS unsigned char* zq[2];
    if constexpr (H == 1) { zq[0] = (pi ? zr[1] : zr[0]) + w * 16384; zq[1] = zq[0] + 65536; asm volatile("" : "+v"(zq[0]), "+v"(zq[1])); }
#pragma unroll
    for (int s = 0; s < 2; ++s) bf[0][s] = tr2(zb[s], 1024);
    sfor<0, 32>([&](auto KS) {
      constexpr int ks = KS.v;
      constexpr bool edge = H == 0 && (ks == 3 || ks == 7 || ks == 15);
      constexpr int e = ks >> 1, es = e & 1, et = e >> 1;
      constexpr int sr = H == 1 && ks >= 8 && (ks - 8) % 3 < 2 ? 2 * ((ks - 8) / 3) + (ks - 8) % 3 : -1;
      constexpr int sn = H == 1 && ks >= 9 && (ks - 9) % 3 < 2 ? 2 * ((ks - 9) / 3) + (ks - 9) % 3 : -1;
      constexpr int rd = H == 0 ? z_read_at(ks) : -1, rp = H == 0 && ks > 0 ? z_read_at(ks - 1) : -1, dec = H == 0 ? z_decide_at(ks) : -1;
      mfma_w<ks == 0, true>(acc[H][0][0], W[2 * ks], bf[ks & 1][0]);
      SB();
      if constexpr (ks + 1 < 32 && !edge) bf[(ks + 1) & 1][0] = tr2(zb[0] + (ks + 1) * 2048, 1024);
      if constexpr (rp >= 0) { zm = andacc(z_first(rp) ? 1u : zm, rbk[0]); pinu(zm); }
      if constexpr (H == 1 && ks < 8) hp[0] = mulf(relu(acc[0][et][es][4 * (ks & 1)]), acc[0][et][es][4 * (ks & 1) + 8]);
      if constexpr (H == 1 && ks < 8) hp[1] = mulf(relu(acc[0][et][es][4 * (ks & 1) + 1]), acc[0][et][es][4 * (ks & 1) + 9]);
      if constexpr (sn >= 0) sumsq1(ss[sn & 1], sn < 2 ? sq[sn & 1].x & dcmask : sq[sn & 1].x);
      SB();
      mfma_w<ks == 0, true>(acc[H][1][0], W[2 * ks + 1], bf[ks & 1][0]);
      SB();
      if constexpr (H == 0 && ks < 16) z_issue<(ks < 8 ? 8 + ks : 2 * ks)>(z, z.so);
      if constexpr (H == 1 && ks < 8) hp[2] = mulf(relu(acc[0][et][es][4 * (ks & 1) + 2]), acc[0][et][es][4 * (ks & 1) + 10]);
      if constexpr (H == 1 && ks < 8) hp[3] = mulf(relu(acc[0][et][es][4 * (ks & 1) + 3]), acc[0][et][es][4 * (ks & 1) + 11]);
      if constexpr (sn >= 0) sumsq1(ss[sn & 1], sq[sn & 1].y);
      if constexpr (H == 1 && ks > 0) stream_issue<ks - 1, 1>(W, hA, hB, x3, srd, lane16, dn);
      if constexpr (H == 0 && ks >= 16 && (ks & 3) == 0) {
        constexpr int i = (ks - 16) / 4;
        if constexpr (i < 2) bstore_sc1(sv, rsf, fo_x, i * 64 * XS);
        else bstore_sc1(~sv, rsf, fo_z, (i - 2) * ZS);
      }
      SB();
      mfma_w<ks == 0, true>(acc[H][0][1], W[2 * ks], bf[ks & 1][1]);
      SB();
      if constexpr (ks + 1 < 32 && !edge) bf[(ks + 1) & 1][1] = tr2(zb[1] + (ks + 1) * 2048, 1024);
      if constexpr (rp >= 0) { zm = andacc(zm, rbk[1]); pinu(zm); }
      if constexpr (dec >= 0) { bad = bit0_clear(zm); pins(bad); }
      if constexpr (H == 1 && ks < 8) { pk_tag(hp[0], hp[1], hp[2], hp[3], layer, hq[2 * (ks & 1)], hq[2 * (ks & 1) + 1]); pinu(hq[2 * (ks & 1)]); pinu(hq[2 * (ks & 1) + 1]); }
      if constexpr (sn >= 0) sumsq1(ss[sn & 1], sq[sn & 1].z);
      SB();
      mfma_w<ks == 0, true>(acc[H][1][1], W[2 * ks + 1], bf[ks & 1][1]);
      SB();
      if constexpr (dec >= 0) { pins(bad); z_fix<dec, ks>(bad, z, zown, L, layer, w); }
      if constexpr (rd >= 0) { vmwait<z_younger(rd + 1, ks)>(); rbk[0] = z_back<rd>(zown); rbk[1] = z_back<rd + 1>(zown); }
      if constexpr (sn >= 0) sumsq1(ss[sn & 1], sq[sn & 1].w);
      if constexpr (sr >= 0) sq[sr & 1] = tr2(zq[sr & 1] + (sr >> 1) * 2048, 1024);
      if constexpr (H == 0 && ks < NW_LATE) load_w<64 - NW_LATE + ks>(W, ws);
      if constexpr (H == 0 && ks >= 8 && ks < 16) z_issue<2 * ks + 1>(z, z.so);
      if constexpr (H == 0 && ks >= 16) {
        constexpr int n = ks - 16;
        vload<((n >> 1) & 3) * 1024>(Ve[n], srd, lane16, dn.vbase + (n & 1) * 112 * 1024 + dn.blk(n >> 3));
      }
      if constexpr (H == 1) {
        if constexpr (ks < 8 && (ks & 1)) {
          bstore_sc1(v4u{hq[0], hq[1], hq[2], hq[3]}, et == 0 ? rs : rs2, lane16, hoff + ((4 * rb + es) * 112 + ks0 + et) * 1024);
          if constexpr (et == 1) { if (idft) x3_piece(acc[0][1][es], rs, lane, OFF_X3 + (128 * rb + 32 * es) * XS + fs * 64); }
        }
        stream_issue<ks, (ks == 31 ? 2 : 0)>(W, hA, hB, x3, srd, lane16, dn);
      }
      if constexpr (edge) {
        BAR();
#pragma unroll
        for (int s = 0; s < 2; ++s) bf[(ks + 1) & 1][s] = tr2(zb[s] + (ks + 1) * 2048, 1024);
      }
      SB();
    });
    STAMP(8 + 2 * H);
    if constexpr (H == 0) BAR();
    STAMP(9 + 2 * H);
  });
  mfma_done(acc[1][0][0], acc[1][0][1], acc[1][1][0], acc[1][1][1]);
#pragma unroll
  for (int s = 0; s < 2; ++s) h_piece(acc[1][0][s], layer, rs, lane, hoff + ((4 * rb + 2 + s) * 112 + ks0) * 1024);
  if (two) {
#pragma unroll
    for (int s = 0; s < 2; ++s) h_piece(acc[1][1][s], layer, rs, lane, hoff + ((4 * rb + 2 + s) * 112 + ks0 + 1) * 1024);
  } else if (idft) {
#pragma unroll
    for (int s = 0; s < 2; ++s) x3_piece(acc[1][1][s], rs, lane, OFF_X3 + (128 * rb + 64 + 32 * s) * XS + fs * 64);
  }
  SB();
  stream_issue<32, 2>(W, hA, hB, x3, srd, lane16, dn);
  {
    auto x = __builtin_amdgcn_permlane32_swap(asu(ss[0]), asu(ss[1]), false, false);
    *(LAS float*)(L + M_S1 + w * 256 + lane * 4) = asf(x[0]) + asf(x[1]);
  }
  const int row = 16 * w + (lane >> 2);
  {
    const int n = nb * 64 + row;
    LAS unsigned char* ra = L + M_Z + n * 128 + (((4 * pi + (lane & 3)) ^ ((n & 2) << 1)) * 16);
    res[0] = *(LAS v4u*)ra; res[1] = *(LAS v4u*)(ra + 65536);
  }
  STAMP(12);
  CSTAMP(21);
  unsigned hm = 0;
  sfor<0, 7>([&](auto G) { h_acc<G.v, STR.end[32]>(hA, hB, hm, layer); });
  h_decide<0, STR.end[32]>(hA, hB, bit0_set(hm), srd, lane16, dn, L, layer, w);
  hm = 0;
  BAR();
  STAMP(13);
  LAS unsigned char* rw = L + M_Z + w * 9216 + r * 144 + h * 16;
  LAS unsigned char* rr = L + M_Z + row * 144 + (lane & 3) * 32;
  f32x16 dac[2][2];
  f32x4 fa[2][2], tq[2];
  auto red_write = [&](auto HH, auto K) {
    constexpr int H = HH.v, t = K.v >> 2, g = K.v & 3;
    *(LAS f32x4*)(rw + H * M_RB + t * 32 * 144 + g * 32) = f32x4{dac[H][t][4 * g], dac[H][t][4 * g + 1], dac[H][t][4 * g + 2], dac[H][t][4 * g + 3]};
  };
  sfor<0, 56>([&](auto GG) {
    constexpr int g = GG.v, H = g / 28, i = g % 28;
    v4u& hx = H == 0 ? hA[i] : hB[i];
    unsigned long long bad = 0ull;
    if constexpr (2 * i < NV_EARLY) mfma_v<i == 0>(dac[H][0], Ve[2 * i], hx);
    else mfma_w<false, false>(dac[H][0], W[2 * i - NV_EARLY], hx);
    SB();
    if constexpr (g + 7 < 56) h_acc<g + 7, STR.end[32 + g]>(hA, hB, hm, layer);
    if constexpr (g + 7 < 56 && g % 7 == 6) { bad = bit0_set(hm); pins(bad); hm = 0; }
    if constexpr (g == 1) {
      LAS float* sp = (LAS float*)(L + M_S1 + lane * 4);
      *(LAS float*)(L + M_S1 + 1024 + w * 256 + lane * 4) =
          __builtin_amdgcn_rcpf(__builtin_fmaf((sp[0] + sp[64]) + (sp[128] + sp[192]), 1.f / 512.f, 1e-5f));
    }
    if constexpr (g >= 51 && g <= 53) addf4(fa[0][0], tq[0]);
    SB();
    if constexpr (2 * i < NV_EARLY) mfma_v<i == 0>(dac[H][1], Ve[2 * i + 1], hx);
    else mfma_w<false, false>(dac[H][1], W[2 * i + 1 - NV_EARLY], hx);
    SB();
    if constexpr (g + 7 < 56 && g % 7 == 6) { pins(bad); h_decide<g + 1, STR.end[32 + g]>(hA, hB, bad, srd, lane16, dn, L, layer, w); }
    stream_issue<33 + g, 2>(W, hA, hB, x3, srd, lane16, dn);
    if constexpr (g >= 30 && g <= 46 && g % 2 == 0 && g != 34) red_write(IC<0>{}, IC<(g < 34 ? (g - 30) / 2 : (g - 32) / 2)>{});
    if constexpr (g == 47) BAR();
    if constexpr (g == 49) { fa[0][0] = *(LAS f32x4*)rr; fa[0][1] = *(LAS f32x4*)(rr + 16); }
    if constexpr (g >= 51 && g <= 53) addf4(fa[0][1], tq[1]);
    if constexpr (g >= 50 && g <= 52) { tq[0] = *(LAS f32x4*)(rr + (g - 49) * 9216); tq[1] = *(LAS f32x4*)(rr + (g - 49) * 9216 + 16); }
    SB();
    if constexpr (g == 27) STAMP(14);
  });
  STAMP(15);
  asm volatile("s_nop 12" : "+v"(dac[1][0]), "+v"(dac[1][1]));
  sfor<0, 8>([&](auto K) { red_write(IC<1>{}, K); });
  vmwait<0>();
  pin(x3[0]); pin(x3[1]);
  if (__builtin_expect(layer == 1 && any_sent(mxacc(mxacc(0u, x3[0]), x3[1])), 0)) {
    unsigned spin = 0;
    do {
      RETRY(29);
      ldx4<0>(x3[0], srd, dn.xvo, dn.xso); ldx4<0>(x3[1], srd, dn.xvo, dn.xso + 64 * XS);
      vmwait<0>();
      pin(x3[0]); pin(x3[1]);
    } while (any_sent(mxacc(mxacc(0u, x3[0]), x3[1])) && ++spin < SPIN_MAX);
  }
  STAMP(16);
  BAR();
  STAMP(17);
  fa[1][0] = *(LAS f32x4*)(rr + M_RB); fa[1][1] = *(LAS f32x4*)(rr + M_RB + 16);
#pragma unroll
  for (int ww = 1; ww < 4; ++ww) { fa[1][0] += *(LAS f32x4*)(rr + M_RB + ww * 9216); fa[1][1] += *(LAS f32x4*)(rr + M_RB + ww * 9216 + 16); }
  f32x4 sc[2][2];
#pragma unroll
  for (int H = 0; H < 2; ++H)
#pragma unroll
    for (int x = 0; x < 2; ++x) sc[H][x] = *(LAS f32x4*)(L + M_S1 + 1024 + w * 256 + H * 128 + (lane & 3) * 32 + x * 16);
  if (layer == 0) {
    const bool dc = nb == 0 && row == 0;
#pragma unroll
    for (int H = 0; H < 2; ++H) {
      unsigned o[4];
      float sq[8];
#pragma unroll
      for (int x = 0; x < 2; ++x)
        pk_tag(__builtin_fmaf(fa[H][x][0], sc[H][x][0], blo(res[H][2 * x])), __builtin_fmaf(fa[H][x][1], sc[H][x][1], bhi(res[H][2 * x])),
               __builtin_fmaf(fa[H][x][2], sc[H][x][2], blo(res[H][2 * x + 1])), __builtin_fmaf(fa[H][x][3], sc[H][x][3], bhi(res[H][2 * x + 1])), 0u,
               o[2 * x], o[2 * x + 1]);
      bstore_sc1(v4u{o[0], o[1], o[2], o[3]}, rs, row * ZS + (lane & 3) * 16, OFF_ZA + nb * 64 * ZS + pt * 64 + H * 128);
#pragma unroll
      for (int j = 0; j < 4; ++j) {
        const unsigned u = dc ? 0u : o[j];
        sq[2 * j] = blo(u) * blo(u); sq[2 * j + 1] = bhi(u) * bhi(u);
      }
      float t4[4], t2[2];
#pragma unroll
      for (int j = 0; j < 4; ++j) {
        auto x = __builtin_amdgcn_permlane32_swap(asu(sq[j]), asu(sq[j + 4]), false, false);
        t4[j] = asf(x[0]) + asf(x[1]);
      }
#pragma unroll
      for (int j = 0; j < 2; ++j) {
        auto x = __builtin_amdgcn_permlane16_swap(asu(t4[j]), asu(t4[j + 2]), false, false);
        t2[j] = asf(x[0]) + asf(x[1]);
        t2[j] += dppf<0x124>(t2[j]);
        t2[j] += dppf<0x128>(t2[j]);
      }
      *(LAS f32x2*)(L + M_S2 + w * 256 + H * 128 + (8 * (lane & 3) + 4 * h + 2 * ((lane >> 4) & 1)) * 4) = f32x2{t2[0], t2[1]};
    }
    BAR();
    if (w == 0) {
      LAS float* sp = (LAS float*)(L + M_S2 + lane * 4);
      const float t = (sp[0] + sp[64]) + (sp[128] + sp[192]);
      __builtin_amdgcn_raw_buffer_store_b32(nosent(asu(t)), rs, stat_of(h * 64 + r), OFF_ST + nb * 8192 + pt * 64, SC1);
    }
  } else {
    LAS unsigned char* tb = L + M_TB + w * 2560;
#pragma unroll
    for (int H = 0; H < 2; ++H) {
      const f32x4 a = fa[H][0] * sc[H][0], bq = fa[H][1] * sc[H][1];
      *(LAS v4u*)(tb + H * 1280 + (lane >> 2) * 80 + (lane & 3) * 16) = v4u{pk(a.x, a.y), pk(a.z, a.w), pk(bq.x, bq.y), pk(bq.z, bq.w)};
    }
#pragma unroll
    for (int H = 0; H < 2; ++H) {
      const v4u y = tr2(tb + H * 1280 + (8 * h + q) * 80 + ((lane >> 4) & 1) * 32 + (lane & 3) * 8, 4 * 80);
      v4u o;
#pragma unroll
      for (int j = 0; j < 4; ++j) o[j] = pk(blo(x3[H][j]) + blo(y[j]), bhi(x3[H][j]) + bhi(y[j]));
      const int tok = token_of(32 * (pt + 2 * H) + r);
      *(v4u*)(p.out + (size_t)tok * 512 + nb * 64 + 16 * w + 8 * h) = o;
    }
  }
  SB();
}

constexpr int CODE_BYTES = 44 * 1024;
constexpr int P_XL = 0, P_GL = 33280, P_DL = P_GL + 65536, P_ZB = P_DL + 33280, P_WB = P_ZB + 4 * 2560;
static_assert(P_WB + 64 * 144 <= M_C2 + 2 * 4608, "fits");

struct Late { v4u d[2], t[2]; int vo, so; rsrc_t rs; };
DI void prep(LAS unsigned char* L, const Params& p, rsrc_t rs, const v4u& srd, unsigned long long tag, unsigned touch0, Late& late) {
  const unsigned Lb = (unsigned)(size_t)L;
  const int tid = threadIdx.x, lane = tid & 63, w = __builtin_amdgcn_readfirstlane(tid >> 6);
  const int b = blockIdx.x, xcd = b & 7, q = (b >> 3) & 3, tgi = b >> 5;
  const int t1 = xcd * 8 + tgi, tg = xcd * 7 + tgi;
  const bool heavy = tgi < 7;
  const int up = tg >= 28, cb = tg - 28 * up;
  const int h = lane >> 5, r = lane & 31, ft = 4 * q + w;
  PST(0);
  {
    const v4u sx = make_srd(p.x, 2048u * 1024u);
    const int tok0 = token_of(32 * t1 + 2 * w);
#pragma unroll
    for (int i = 0; i < 8; ++i) {
      const int a = ((i >> 1) + q) & 3;
      glds_in(sx, lane * 16, (tok0 + a + 32 * (i & 1)) * 1024, Lb + P_XL + (8 * a + 2 * w + (i & 1)) * 1040);
    }
  }
  const unsigned hm = opaque((tgi - 7) >> 31);
  v4u sd = make_srd(p.wd, 1792u * 1024u & hm), sg = make_srd(up ? p.wu : p.wg, 512u * 3584u & hm);
  v4u sr = make_srd(p.wr, 2048u * 514u), si = make_srd(p.wi, 2048u * 514u);
  asm volatile("" : "+s"(sg));
  unsigned touch, touch2;
  {
    v4u st = w == 2 ? sd : w == 3 ? sr : sg;
    asm volatile("" : "+s"(st));
    const int so = w == 2 ? (32 * tg + 8 * q) * 1024 : w == 3 ? (32 * t1 * 257 + 64 * q) * 2 : (128 * q + 64 * w) * 3584 + 128 * cb;
    const int vo = lane * (w == 2 ? 128 : w == 3 ? 0 : 3584) + (w == 3 ? r * 514 : 0);
    asm volatile("buffer_load_ushort %0, %1, %2, %3 offen" : "=v"(touch) : "v"(vo), "s"(st), "s"(so));
    asm volatile("buffer_load_ushort %0, %1, %2, %3 offen" : "=v"(touch2) : "v"(r * 514), "s"(si), "s"((32 * t1 * 257 + 64 * q) * 2));
  }
  SB();
  const int feat = 32 * ft + r;
  const float step = (feat == 0 ? 0.f : feat == 1 ? 256.f : (float)(feat >> 1)) * (1.f / 512.f);
  const float ph = (feat > 1 && (feat & 1)) ? 0.25f : 0.f;
  const float a0 = ph + step * (float)(8 * h);
  const unsigned sgn = (feat > 1 && (feat & 2)) ? 0x80008000u : 0u;
  auto fill = [&](int i) { bstore_sc1(v4u{SENT, SENT, SENT, SENT}, rs, tid * 16, OFF_H + b * 28672 + i * 4096); };
  v4u F[32];
  sfor<0, 16>([&](auto KS) {
    constexpr int ks = KS.v;
    F[ks] = cosfrag(a0, step, 16 * ks); F[ks + 16] = F[ks] ^ sgn;
    pin(F[ks]); pin(F[ks + 16]);
    if constexpr (ks % 5 == 2) { SB(); fill(ks / 5); SB(); }
  });
  const float lsc = feat < 2 ? 0.04419417382415922f : 0.0625f;
  v4u I[2];
#pragma unroll
  for (int s = 0; s < 2; ++s) {
    const int e = r - 16 * s - 8 * h;
    const unsigned one = (e & 1) ? 0x3F800000u : 0x00003F80u;
    I[s] = v4u{(e >> 1) == 0 ? one : 0u, (e >> 1) == 1 ? one : 0u, (e >> 1) == 2 ? one : 0u, (e >> 1) == 3 ? one : 0u};
  }
  pin(I[0]); pin(I[1]);
  const int gvo = (lane >> 3) * 3584 + (((lane & 7) ^ ((lane >> 2) & 4)) * 16);
  const int fcol = 64 * q + lane, fvo = (fcol == 0 ? 256 : fcol) * 2, fso = (32 * t1 + 8 * w) * 514;
  if (!heavy) {
#pragma unroll
    for (int i = 0; i < 2; ++i) {
      const int dt = 2 * xcd + i;
      const float b0 = __builtin_amdgcn_fractf(__builtin_fmaf(step, (float)(32 * dt), a0));
      f32x16 aI;
      v4u c0 = cosfrag(b0, step, 0), c1 = cosfrag(b0, step, 16);
      m16_ready(c0, c1);
      mfma_vv<true>(aI, c0, I[0]);
      mfma_vv<false>(aI, c1, I[1]);
      asm volatile("s_nop 12" : "+v"(aI));
      if (ft == 0 && h == 0) { aI[0] *= 0.70710678118654752f; aI[1] *= 0.70710678118654752f; }
#pragma unroll
      for (int s = 0; s < 2; ++s) bstore_sc1(pack8(aI, s, 0.0625f), rs, lane * 16, OFF_W1 + (dt * 8 + 7) * 32768 + (2 * ft + s) * 1024);
    }
  }
  const rsrc_t rsh = __builtin_amdgcn_make_buffer_rsrc((void*)p.ws, 0, WS_BYTES & hm, 0x00020000);
  f32x16 aZ;
  unsigned fr[8], fi[8];
  v4u zo[2], su[2], wo[2];
  unsigned zd[8];
  float sv_[16], ssum;
  auto z_out = [&](auto STEP) {
    constexpr int n = STEP.v;
    LAS unsigned char* zb = L + P_ZB + w * 2560;
    if constexpr (n < 4) {
      pk_tag(aZ[4 * n] * lsc, aZ[4 * n + 1] * lsc, aZ[4 * n + 2] * lsc, aZ[4 * n + 3] * lsc, 0u, zd[2 * n], zd[2 * n + 1]);
      pinu(zd[2 * n]); pinu(zd[2 * n + 1]);
    }
    if constexpr (n == 4 || n == 5) {
      auto r0 = __builtin_amdgcn_permlane32_swap(zd[4 * (n - 4)], zd[4 * (n - 4) + 2], false, false);
      auto r1 = __builtin_amdgcn_permlane32_swap(zd[4 * (n - 4) + 1], zd[4 * (n - 4) + 3], false, false);
      *(LAS v4u*)(zb + r * 80 + h * 16 + 32 * (n - 4)) = v4u{r0[0], r1[0], r0[1], r1[1]};
    }
    if constexpr (n == 6) { zo[0] = *(LAS v4u*)(zb + (lane >> 2) * 80 + (lane & 3) * 16); zo[1] = *(LAS v4u*)(zb + (16 + (lane >> 2)) * 80 + (lane & 3) * 16); }
    if constexpr (n == 7 || n == 8) bstore_sc1(zo[n - 7], rs, (32 * ft + 16 * (n - 7) + (lane >> 2)) * ZS + (lane & 3) * 16, OFF_ZA + t1 * 64);
  };
  auto stat = [&](auto STEP) {
    constexpr int n = STEP.v;
    const int row = 8 * q + 2 * w + h;
    if constexpr (n == 0) { su[0] = *(LAS v4u*)(L + P_XL + row * 1040 + r * 32); su[1] = *(LAS v4u*)(L + P_XL + row * 1040 + r * 32 + 16); ssum = 0.f; }
    if constexpr (n >= 1 && n <= 4) {
      constexpr int k = n - 1;
      sv_[4 * k] = blo(su[k >> 1][2 * (k & 1)]); sv_[4 * k + 1] = bhi(su[k >> 1][2 * (k & 1)]);
      sv_[4 * k + 2] = blo(su[k >> 1][2 * (k & 1) + 1]); sv_[4 * k + 3] = bhi(su[k >> 1][2 * (k & 1) + 1]);
      ssum += (sv_[4 * k] + sv_[4 * k + 1]) + (sv_[4 * k + 2] + sv_[4 * k + 3]);
    }
    if constexpr (n == 5) { const float m = sum32(ssum) * (1.f / 512.f); ssum = m; }
    if constexpr (n >= 6 && n <= 9) {
#pragma unroll
      for (int i = 4 * (n - 6); i < 4 * (n - 6) + 4; ++i) { sv_[i] -= ssum; pinf(sv_[i]); }
    }
    if constexpr (n == 10) ssum = 0.f;
    if constexpr (n >= 10 && n <= 12) {
#pragma unroll
      for (int i = 6 * (n - 10); i < 6 * (n - 10) + 6 && i < 16; ++i) ssum = __builtin_fmaf(sv_[i], sv_[i], ssum);
    }
    if constexpr (n == 13) ssum = sum32(ssum);
    if constexpr (n == 14) { if (r == 0) __builtin_amdgcn_raw_buffer_store_b32(asu(ssum), rs, stat_of(32 * t1 + row), OFF_R0, SC1); }
    pinf(ssum);
  };
  auto f_out = [&](auto N, const f32x16& a, float sc, v4u& o, int vo, int so) {
    constexpr int k = 8 * (N.v >> 1) + 4 * (N.v & 1);
    o[2 * (N.v & 1)] = pk(a[k] * sc, a[k + 1] * sc); o[2 * (N.v & 1) + 1] = pk(a[k + 2] * sc, a[k + 3] * sc);
    if constexpr (N.v & 1) bstore_sc1(o, rsh, vo, so + (N.v >> 1) * 1024); else pin(o);
  };
  auto filt = [&](auto STEP) {
    constexpr int n = STEP.v;
    if constexpr (n == 0) {
      asm volatile("" : "+v"(fr[0]), "+v"(fr[1]), "+v"(fr[2]), "+v"(fr[3]), "+v"(fr[4]), "+v"(fr[5]), "+v"(fr[6]), "+v"(fr[7]),
                        "+v"(fi[0]), "+v"(fi[1]), "+v"(fi[2]), "+v"(fi[3]), "+v"(fi[4]), "+v"(fi[5]), "+v"(fi[6]), "+v"(fi[7]));
      v4u o0, o1;
#pragma unroll
      for (int i = 0; i < 4; ++i) { o0[i] = fr[i] | (fi[i] << 16); o1[i] = fr[4 + i] | (fi[4 + i] << 16); }
      *(LAS v4u*)(L + P_WB + lane * 144 + w * 32) = o0;
      *(LAS v4u*)(L + P_WB + lane * 144 + w * 32 + 16) = o1;
    }
    if constexpr (n == 1) {
#pragma unroll
      for (int i = 0; i < 2; ++i) wo[i] = *(LAS v4u*)(L + P_WB + (16 * w + 8 * i + (lane >> 3)) * 144 + (lane & 7) * 16);
    }
    if constexpr (n >= 2) bstore_sc1(wo[n - 2], rs, (64 * q + 16 * w + 8 * (n - 2) + (lane >> 3)) * WS_ + (lane & 7) * 16, OFF_WT + t1 * 128);
  };
  auto gu = [&](int i) {
    const int blk = 16 * (((i >> 2) + q) & 3) + 4 * w + (i & 3);
    glds_in(sg, gvo, blk * 8 * 3584 + cb * 128, Lb + P_GL + blk * 1024);
  };
#pragma unroll
  for (int i = 0; i < 6; ++i) gu(i);
  fill(3);
  PST(1);
  vmwait<12>();
  BAR();
  PST(2);
  {
    LAS unsigned char* xa = L + P_XL + r * 1040 + h * 16;
    v4u a[4];
#pragma unroll
    for (int i = 0; i < 4; ++i) a[i] = *(LAS v4u*)(xa + i * 32);
    sfor<0, 32>([&](auto KS) {
      constexpr int ks = KS.v;
      mfma_vv<ks == 0>(aZ, a[ks & 3], F[ks]);
      SB();
      if constexpr (ks + 4 < 32) a[ks & 3] = *(LAS v4u*)(xa + (ks + 4) * 32);
      if constexpr (ks < 20 && !(ks & 1)) gu(6 + ks / 2);
      if constexpr (ks >= 20 && ks < 28) ld2u16<(ks - 20) * 514>(fr[ks - 20], fi[ks - 20], fvo, sr, si, fso);
      if constexpr ((ks & 1) && ks < 30) stat(IC<(ks >> 1)>{});
      if constexpr (ks == 8 || ks == 19 || ks == 30) fill(4 + ks / 11);
      SB();
    });
  }
  asm volatile("s_nop 12\n\ts_waitcnt vmcnt(16)" : "+v"(aZ) : "v"(touch), "v"(touch2), "v"(touch0) : "memory");
  PST(3);
  BAR();
  PST(4);
  f32x16 aG0, aG1;
  {
    const int q4 = (lane & 15) >> 2;
    LAS unsigned char* ba = L + P_GL + (8 * h + q4) * 128 + (4 * (q4 >> 1) + 2 * ((lane >> 4) & 1) + ((lane & 3) >> 1)) * 16 + 8 * (lane & 1);
    LAS unsigned char* bb = L + P_GL + (8 * h + q4) * 128 + (4 * (1 - (q4 >> 1)) + 2 * ((lane >> 4) & 1) + ((lane & 3) >> 1)) * 16 + 8 * (lane & 1);
    v4u b0[2], b1[2];
#pragma unroll
    for (int i = 0; i < 2; ++i) { b0[i] = tr2(ba + i * 2048, 512); b1[i] = tr2(bb + i * 2048, 512); }
    sfor<0, 32>([&](auto KS) {
      constexpr int ks = KS.v;
      mfma_vv<ks == 0>(aG0, F[ks], b0[ks & 1]);
      SB();
      if constexpr (ks + 2 < 32) b0[ks & 1] = tr2(ba + (ks + 2) * 2048, 512);
      if constexpr (ks < 9) z_out(KS);
      if constexpr (ks >= 11 && ks < 14) filt(IC<ks - 10>{});
      if constexpr (ks == 25) publish(rs, 0, b, tag, w, lane);
      SB();
      mfma_vv<ks == 0>(aG1, F[ks], b1[ks & 1]);
      SB();
      if constexpr (ks + 2 < 32) b1[ks & 1] = tr2(bb + (ks + 2) * 2048, 512);
      if constexpr (ks == 9) { vmwait<8>(); filt(IC<0>{}); }
      if constexpr (ks >= 1 && ks < 9) {
        const int row = 8 * ((((ks - 1) >> 1) + q) & 3) + 2 * w + ((ks - 1) & 1);
        glds_in(sd, lane * 16, (32 * tg + row) * 1024, Lb + P_DL + row * 1040);
      }
      if constexpr (ks == 10) BAR();
      if constexpr (ks == 24) { vmwait<0>(); asm volatile("s_barrier" ::: "memory"); }
      SB();
    });
  }
  PST(5);
  f32x16 aD, aT;
  v4u fl[2];
  {
    const int g0 = 4 * cb + (r >> 4), g1 = g0 + 2, slot = ((r & 15) + 16 * up + 32 * h) * 16;
    const int o0 = ((g0 / 7) * 8 + g0 % 7) * 32768 + slot, o1 = ((g1 / 7) * 8 + g1 % 7) * 32768 + slot;
    v4u fo = {0u, 0u, 0u, 0u};
    LAS unsigned char* da = L + P_DL + r * 1040 + h * 16;
    const v4u t0 = *(LAS v4u*)(da + ft * 64), t1_ = *(LAS v4u*)(da + ft * 64 + 32);
    v4u a[4];
#pragma unroll
    for (int i = 0; i < 4; ++i) a[i] = *(LAS v4u*)(da + i * 32);
    mfma_vv<true>(aT, t0, I[0]);
    mfma_vv<false>(aT, t1_, I[1]);
    sfor<0, 32>([&](auto KS) {
      constexpr int ks = KS.v;
      mfma_vv<ks == 0>(aD, a[ks & 3], F[ks]);
      SB();
      if constexpr (ks + 4 < 32) a[ks & 3] = *(LAS v4u*)(da + (ks + 4) * 32);
      if constexpr (ks == 0) {
        const bool edge = ft == 0 && h == 0;
        aG0[0] = edge ? 0.f : aG0[0]; aG1[0] = edge ? 0.f : aG1[0];
        aG0[1] *= edge ? 0.70710678118654752f : 1.f; aG1[1] *= edge ? 0.70710678118654752f : 1.f;
      }
      if constexpr (ks >= 1 && ks < 5) f_out(IC<ks - 1>{}, aG0, 0.0625f, fo, o0, OFF_W1 + 2 * ft * 1024);
      if constexpr (ks >= 5 && ks < 9) f_out(IC<ks - 5>{}, aG1, 0.0625f, fo, o1, OFF_W1 + 2 * ft * 1024);
      if constexpr (ks == 10 || ks == 12) { late.t[ks == 12] = pack8(aT, ks == 12, 1.f); pin(late.t[ks == 12]); }
      if constexpr (ks == 22) flags_load(fl, srd, 0, b, lane);
      SB();
    });
  }
  PST(6);
  asm volatile("s_nop 12\n\ts_waitcnt vmcnt(0)\n\ts_barrier" : "+v"(aD) :: "memory");
  publish(rs, 1, b, tag, w, lane);
  late.d[0] = pack8(aD, 0, lsc); late.d[1] = pack8(aD, 1, lsc);
  late.vo = lane * 16; late.so = (ft * 112 + 2 * tg) * 1024; late.rs = rsh;
  PST(7);
  flags_wait(fl, srd, 0, b, lane, tag);
  PST(8);
}

struct Tile {
  int rb, fs, pi, nb;
  DI Tile(int c) { rb = 4 * (c & 3) + (c >> 6); fs = 8 * ((c >> 2) & 1) + ((c >> 3) & 7); pi = fs >> 3; nb = fs & 7; }
};

template <int layer>
DI void column(LAS unsigned char* L, rsrc_t rs, const v4u& srd, v4u (&W)[64], unsigned long long tag, const Late& late) {
  int tid = threadIdx.x;
  asm volatile("" : "+v"(tid));
  const int c = opaque(blockIdx.x);
  const int lane = tid & 63, w = __builtin_amdgcn_readfirstlane(tid >> 6);
  WSrc ws;
  ws.srd = srd; ws.srd1 = srd;
  if (w == 3 && layer == 0) ws.srd1.z = 0u;
  ws.lane16 = lane * 16; ws.base = OFF_W1 + (Tile(c).fs * 8 + 2 * w) * 32768;
  unsigned wf[8];
  const int wfvo = (16 * w + (lane & 15) + 64 * 4 * (lane >> 4)) * 4;
  auto filter_in = [&]() {
    asm volatile("buffer_load_dword %0, %8, %9, %10 offen sc1\n\tbuffer_load_dword %1, %8, %9, %10 offen offset:256 sc1\n\t"
                 "buffer_load_dword %2, %8, %9, %10 offen offset:512 sc1\n\tbuffer_load_dword %3, %8, %9, %10 offen offset:768 sc1\n\t"
                 "buffer_load_dword %4, %8, %9, %11 offen sc1\n\tbuffer_load_dword %5, %8, %9, %11 offen offset:256 sc1\n\t"
                 "buffer_load_dword %6, %8, %9, %11 offen offset:512 sc1\n\tbuffer_load_dword %7, %8, %9, %11 offen offset:768 sc1"
                 : "=&v"(wf[0]), "=&v"(wf[1]), "=&v"(wf[2]), "=&v"(wf[3]), "=&v"(wf[4]), "=&v"(wf[5]), "=&v"(wf[6]), "=&v"(wf[7])
                 : "v"(wfvo), "s"(srd), "s"(OFF_WT + c * WS_), "s"(OFF_WT + c * WS_ + 4096));
  };
  float rstd[8], fr[8], fi[8];
  auto filter_unpack = [&]() {
    asm volatile("" : "+v"(wf[0]), "+v"(wf[1]), "+v"(wf[2]), "+v"(wf[3]), "+v"(wf[4]), "+v"(wf[5]), "+v"(wf[6]), "+v"(wf[7]));
#pragma unroll
    for (int e = 0; e < 8; ++e) { fr[e] = blo(wf[e]) * (1.f / 2048.f); fi[e] = bhi(wf[e]) * (1.f / 2048.f); pinf(fr[e]); pinf(fi[e]); }
  };
  STAMP(0);
  CSTAMP(22);
  v4u zr, zi, sa, sb;
  const int first = is_zero(c);
  ColConst k;
  if constexpr (layer == 0) {
    v4u fl[2];
    ldx4<0>(zr, srd, tid * 16, OFF_ZA + (2 * c + first) * ZS); ldx4<0>(zi, srd, tid * 16, OFF_ZA + (2 * c + 1 - first) * ZS);
    ldx4<0>(sa, srd, tid * 16, OFF_R0); ldx4<0>(sb, srd, tid * 16, OFF_R0 + 4096);
    filter_in();
    SB();
#pragma unroll
    for (int i = 0; i < 2; ++i) {
      bstore_sc1(late.d[i], late.rs, late.vo, OFF_W2 + late.so + i * 1024);
      bstore_sc1(late.t[i], late.rs, late.vo, OFF_W3 + late.so + i * 1024);
    }
    if (tid < 16) bstore_sc1(v4u{SENT, SENT, SENT, SENT}, rs, tid * 16, OFF_ST + c * 256);
    if (c == 0 && tid == 0) __builtin_amdgcn_raw_buffer_store_b64(v2u{(unsigned)tag, (unsigned)(tag >> 32)}, rs, OFF_CTL, 0, SC1);
    SB();
    col_consts<false>(k, w, lane, W, ws);
    SB();
    flags_load(fl, srd, 1, c, lane);
    STAMP(1);
    vmwait<2>();
    pin(zr); pin(zi); pin(sa); pin(sb);
    filter_unpack();
    vmwait<0>();
    flags_wait(fl, srd, 1, c, lane, tag);
    load_ws<0, 6>(W, ws);
  } else {
    filter_in();
    SB();
    col_consts<true>(k, w, lane, W, ws);
    SB();
    vmwait<16>();
    filter_unpack();
    STAMP(1);
    __builtin_amdgcn_s_sleep(DELAY_C);
    unsigned spin = 0;
    bool again;
    do {
      RETRY(28);
      v4u st[16];
      ldx4<0>(zr, srd, tid * 16, OFF_ZA + (2 * c + first) * ZS); ldx4<0>(zi, srd, tid * 16, OFF_ZA + (2 * c + 1 - first) * ZS);
      sfor<0, 16>([&](auto I) { ldx4<0>(st[I.v], srd, tid * 16, OFF_ST + I.v * 4096); });
      vmwait<0>();
      pin(zr); pin(zi);
#pragma unroll
      for (int i = 0; i < 16; ++i) pin(st[i]);
      f32x4 a = __builtin_bit_cast(f32x4, st[0]), bq = __builtin_bit_cast(f32x4, st[1]);
#pragma unroll
      for (int n = 1; n < 8; ++n) { a += __builtin_bit_cast(f32x4, st[2 * n]); bq += __builtin_bit_cast(f32x4, st[2 * n + 1]); }
      sa = __builtin_bit_cast(v4u, a); sb = __builtin_bit_cast(v4u, bq);
      const f32x4 t = a + bq;
      const float u = (t.x + t.y) + (t.z + t.w);
      unsigned m = 0u;
      if (__builtin_expect(__builtin_amdgcn_ballot_w64(u != u) != 0ull, 0)) {
#pragma unroll
        for (int i = 0; i < 16; ++i) m = mxacc(m, st[i]);
      }
      again = (__builtin_amdgcn_ballot_w64(m == SENT) | bit0_set(tagacc(tagacc(0u, zr, 0u), zi, 0u))) != 0ull;
    } while (__builtin_expect(again, 0) && ++spin < SPIN_MAX);
  }
  constexpr int W0 = layer == 0 ? 6 : 16;
  load_ws<W0, W0 + 2>(W, ws);
#pragma unroll
  for (int e = 0; e < 4; ++e) {
    rstd[e] = __builtin_amdgcn_rsqf(__builtin_fmaf(asf(sa[e]), 1.f / 512.f, 1e-5f));
    rstd[4 + e] = __builtin_amdgcn_rsqf(__builtin_fmaf(asf(sb[e]), 1.f / 512.f, 1e-5f));
  }
  load_ws<W0 + 2, W0 + 4>(W, ws);
  STAMP(2);
  col_compute<W0 + 4>(L, rs, k, fr, fi, zr, zi, rstd, c, w, lane, layer == 0 ? OFF_ZA : OFF_ZB, W, ws);
  STAMP(3);
  CSTAMP(23);
  load_ws<W0 + 34, 64 - NW_LATE>(W, ws);
  if constexpr (layer == 1) __builtin_amdgcn_s_sleep(DELAY_A);
}

__global__ void __launch_bounds__(256, 1) k_main(Params p) {
  extern __shared__ __attribute__((aligned(16))) unsigned char smem[];
  LAS unsigned char* L = (LAS unsigned char*)smem;
  const rsrc_t rs = __builtin_amdgcn_make_buffer_rsrc((void*)p.ws, 0, WS_BYTES, 0x00020000);
  const v4u srd = make_srd(p.ws, WS_BYTES);
  unsigned touch0;
  {
    unsigned lo, hi;
    asm volatile("s_getpc_b64 s[20:21]\n\ts_add_u32 %0, s20, _Z6k_main6Params@rel32@lo+4\n\ts_addc_u32 %1, s21, _Z6k_main6Params@rel32@hi+12"
                 : "=s"(lo), "=s"(hi) :: "s20", "s21", "scc");
    const int off = threadIdx.x * 128 + (threadIdx.x >= 128 ? (blockIdx.x & 8) * 2048 : 0);
    asm volatile("global_load_dword %0, %1, %2" : "=v"(touch0) : "v"(off < CODE_BYTES ? off : 0), "s"(((unsigned long long)hi << 32) | lo));
    SB();
  }
  unsigned long long tag = *(const unsigned long long*)(p.ws + OFF_CTL) + 0x9E3779B97F4A7C15ull;
  tag = (tag ^ (tag >> 30)) * 0xBF58476D1CE4E5B9ull;
  tag = (tag ^ (tag >> 27)) * 0x94D049BB133111EBull;
  tag ^= tag >> 31;
  Late late;
  prep(L, p, rs, srd, tag, touch0, late);
  v4u W[64];
  column<0>(L, rs, srd, W, tag, late);
#pragma unroll 1
  for (int layer = 0; layer < 2; ++layer) {
    int tid = threadIdx.x;
    asm volatile("" : "+v"(tid));
    const int c = opaque(blockIdx.x);
    const int lane = tid & 63, w = __builtin_amdgcn_readfirstlane(tid >> 6);
    const Tile t(c);
    WSrc ws;
    ws.srd = srd; ws.srd1 = srd;
    if (w == 3 && layer == 0) ws.srd1.z = 0u;
    ws.lane16 = lane * 16; ws.base = OFF_W1 + (t.fs * 8 + 2 * w) * 32768;
    ffn(L, rs, ws, p, W, layer, c, t.rb, t.fs, t.pi, t.nb, w, lane);
    STAMP(18);
    if (layer == 0) column<1>(L, rs, srd, W, tag, late);
  }
}

extern "C" void kernel_launch(void* const* d_in, const int* in_sizes, int n_in, void* d_out, int out_size, void* d_ws, size_t ws_size,
                              hipStream_t stream) {
  Params p;
  p.x = (const u16*)d_in[0]; p.wr = (const u16*)d_in[1]; p.wi = (const u16*)d_in[2];
  p.wg = (const u16*)d_in[4]; p.wu = (const u16*)d_in[5]; p.wd = (const u16*)d_in[6];
  p.out = (u16*)d_out; p.ws = (unsigned char*)d_ws;
  (void)hipFuncSetAttribute((const void*)k_main, hipFuncAttributeMaxDynamicSharedMemorySize, M_LDS);
  k_main<<<256, 256, M_LDS, stream>>>(p);
}
```

```cpp
#include <hip/hip_runtime.h>
#include <hip/hip_bf16.h>

#define LAS __attribute__((address_space(3)))
#define DI __device__ __forceinline__
typedef unsigned short u16;
typedef unsigned v4u __attribute__((ext_vector_type(4)));
typedef unsigned v2u __attribute__((ext_vector_type(2)));
typedef float f32x2 __attribute__((ext_vector_type(2)));
typedef float f32x4 __attribute__((ext_vector_type(4)));
typedef float f32x16 __attribute__((ext_vector_type(16)));
typedef short s16x4 __attribute__((ext_vector_type(4)));
typedef __bf16 bf16x2 __attribute__((ext_vector_type(2)));
typedef __bf16 bf16x8 __attribute__((ext_vector_type(8)));
typedef __amdgpu_buffer_rsrc_t rsrc_t;

constexpr int MB = 1 << 20;
constexpr int ZS = 4224;
constexpr int OFF_ZA = 0;
constexpr int OFF_ZB = 9 * MB / 4;
constexpr int OFF_W1 = 12 * MB;
constexpr int OFF_W2 = 16 * MB;
constexpr int OFF_W3 = 18 * MB;
constexpr int OFF_H = 20 * MB;
constexpr int OFF_R0 = 39 * MB;
constexpr int OFF_ST = 39 * MB + 65536;
constexpr int OFF_CTL = 39 * MB + 2 * 65536;
constexpr int OFF_FL = OFF_CTL + 4096;
constexpr int WS_BYTES = 40 * MB;
constexpr int SC1 = 16;
constexpr unsigned SENT = 0xFFFFFFFFu;
constexpr unsigned SPIN_MAX = 1u << 16;

struct Params {
  const u16* x; const u16* wr; const u16* wi; const u16* wg; const u16* wu; const u16* wd;
  u16* out; unsigned char* ws;
};

DI unsigned pk(float a, float b) { f32x2 v = {a, b}; return __builtin_bit_cast(unsigned, __builtin_convertvector(v, bf16x2)); }
DI float asf(unsigned u) { return __builtin_bit_cast(float, u); }
DI unsigned asu(float f) { return __builtin_bit_cast(unsigned, f); }
DI bf16x2 asb2(unsigned u) { return __builtin_bit_cast(bf16x2, u); }
template <int V> struct IC { static constexpr int v = V; };
template <int I, int N, class Fn> DI void sfor(Fn&& fn) { if constexpr (I < N) { fn(IC<I>{}); sfor<I + 1, N>(fn); } }
DI int opaque(int x) { asm volatile("" : "+s"(x)); return x; }
DI int lane_id() { int l; asm volatile("v_mbcnt_lo_u32_b32 %0, -1, 0\n\tv_mbcnt_hi_u32_b32 %0, -1, %0" : "=v"(l)); return l; }
DI int is_zero(int x) { int n; asm volatile("s_min_u32 %0, %1, 1" : "=s"(n) : "s"(x) : "scc"); return 1 - n; }
DI float blo(unsigned u) { return __builtin_bit_cast(float, u << 16); }
DI float bhi(unsigned u) { return __builtin_bit_cast(float, u & 0xFFFF0000u); }
DI float rbf(float a) { return blo(pk(a, a)); }
DI void pk_tag(float a0, float b0, float a1, float b1, unsigned tag, unsigned& d0, unsigned& d1) {
  const unsigned c = pk(b0, b1), up = (tag ^ 1u) << 16;
  d0 = (__builtin_amdgcn_perm(c, asu(a0) + up, 0x05040302u) & ~1u) | tag;
  d1 = (__builtin_amdgcn_perm(c, asu(a1) + up, 0x07060302u) & ~1u) | tag;
}
DI unsigned nosent(unsigned u) { return u < 0xFFFFFFFEu ? u : 0xFFFFFFFEu; }
DI unsigned umax(unsigned a, unsigned b) { return a > b ? a : b; }
DI unsigned mx4(v4u v) { return umax(umax(v.x, v.y), umax(v.z, v.w)); }
DI bool any_sent(unsigned m) { return __builtin_amdgcn_ballot_w64(m == SENT) != 0ull; }
DI f32x16 mfma32(v4u a, v4u b, f32x16 c) {
  return __builtin_amdgcn_mfma_f32_32x32x16_bf16(__builtin_bit_cast(bf16x8, a), __builtin_bit_cast(bf16x8, b), c, 0, 0, 0);
}
DI f32x4 mfma16(v4u a, v4u b, f32x4 c) {
  return __builtin_amdgcn_mfma_f32_16x16x32_bf16(__builtin_bit_cast(bf16x8, a), __builtin_bit_cast(bf16x8, b), c, 0, 0, 0);
}
template <bool FIRST, bool WA> DI void mfma_w(f32x16& acc, const v4u& wt, const v4u& x) {
  if constexpr (FIRST && WA) asm volatile("v_mfma_f32_32x32x16_bf16 %0, %1, %2, 0" : "=&v"(acc) : "a"(wt), "v"(x));
  if constexpr (FIRST && !WA) asm volatile("v_mfma_f32_32x32x16_bf16 %0, %2, %1, 0" : "=&v"(acc) : "a"(wt), "v"(x));
  if constexpr (!FIRST && WA) asm volatile("v_mfma_f32_32x32x16_bf16 %0, %1, %2, %0" : "+v"(acc) : "a"(wt), "v"(x));
  if constexpr (!FIRST && !WA) asm volatile("v_mfma_f32_32x32x16_bf16 %0, %2, %1, %0" : "+v"(acc) : "a"(wt), "v"(x));
}
template <bool FIRST> DI void mfma_v(f32x16& acc, const v4u& wt, const v4u& x) {
  if constexpr (FIRST) asm volatile("v_mfma_f32_32x32x16_bf16 %0, %2, %1, 0" : "=&v"(acc) : "v"(wt), "v"(x));
  else asm volatile("v_mfma_f32_32x32x16_bf16 %0, %2, %1, %0" : "+v"(acc) : "v"(wt), "v"(x));
}
template <bool FIRST> DI void mfma_vv(f32x16& acc, const v4u& a, const v4u& b) {
  if constexpr (FIRST) asm volatile("v_mfma_f32_32x32x16_bf16 %0, %1, %2, 0" : "=&v"(acc) : "v"(a), "v"(b));
  else asm volatile("v_mfma_f32_32x32x16_bf16 %0, %1, %2, %0" : "+v"(acc) : "v"(a), "v"(b));
}
template <int IMM> DI void vload(v4u& dst, const v4u& srd, int lane16, int so) {
  asm volatile("buffer_load_dwordx4 %0, %1, %2, %3 offen offset:%4 sc1" : "=v"(dst) : "v"(lane16), "s"(srd), "s"(so), "i"(IMM));
}
template <int IMM, bool REUSE> DI void wload(v4u& dst, const v4u& srd, int lane16, int so) {
  if constexpr (REUSE) asm volatile("buffer_load_dwordx4 %0, %1, %2, %3 offen offset:%4 sc1" : "+a"(dst) : "v"(lane16), "s"(srd), "s"(so), "i"(IMM));
  else asm volatile("buffer_load_dwordx4 %0, %1, %2, %3 offen offset:%4 sc1" : "=a"(dst) : "v"(lane16), "s"(srd), "s"(so), "i"(IMM));
}
DI void mfma_done(f32x16& a, f32x16& b, f32x16& c, f32x16& d) { asm volatile("s_nop 12" : "+v"(a), "+v"(b), "+v"(c), "+v"(d)); }
DI v2u trread(LAS unsigned char* p) { return __builtin_bit_cast(v2u, __builtin_amdgcn_ds_read_tr16_b64_v4i16((LAS s16x4*)p)); }
DI v4u tr2(LAS unsigned char* p, int d) { v2u a = trread(p), b = trread(p + d); return v4u{a.x, a.y, b.x, b.y}; }
DI v4u bload(rsrc_t rs, int voff, int soff) { return __builtin_amdgcn_raw_buffer_load_b128(rs, voff, soff, 0); }
DI v4u bload_sc1(rsrc_t rs, int voff, int soff) { return __builtin_amdgcn_raw_buffer_load_b128(rs, voff, soff, SC1); }
DI void bstore(v4u v, rsrc_t rs, int voff, int soff) { __builtin_amdgcn_raw_buffer_store_b128(v, rs, voff + soff, 0, 0); }
DI void bstore_sc1(v4u v, rsrc_t rs, int voff, int soff) { __builtin_amdgcn_raw_buffer_store_b128(v, rs, voff + soff, 0, SC1); }
template <int I> DI v4u fload(rsrc_t rs, int lane16, int base) { return bload(rs, lane16 + (I & 3) * 1024, base + (I >> 2) * 4096); }
template <int I> DI v4u fload_sc1(rsrc_t rs, int lane16, int base) { return bload_sc1(rs, lane16 + (I & 3) * 1024, base + (I >> 2) * 4096); }
#define SB() __builtin_amdgcn_sched_barrier(0)
#define MEMFENCE() asm volatile("" ::: "memory")
#define BAR() asm volatile("s_waitcnt lgkmcnt(0)\n\ts_barrier" ::: "memory")
template <int N> DI void vmwait() { asm volatile("s_waitcnt vmcnt(%0)" :: "i"(N) : "memory"); }
DI void pin(v4u& a) { asm volatile("" : "+v"(a)); }
DI f32x16 zero16() { f32x16 z; for (int i = 0; i < 16; ++i) z[i] = 0.f; return z; }
DI v4u pack8(const f32x16& a, int s) {
  return v4u{pk(a[8 * s], a[8 * s + 1]), pk(a[8 * s + 2], a[8 * s + 3]), pk(a[8 * s + 4], a[8 * s + 5]), pk(a[8 * s + 6], a[8 * s + 7])};
}
template <int CTRL> DI float dppf(float v) {
  return __builtin_bit_cast(float, __builtin_amdgcn_update_dpp(0, __builtin_bit_cast(int, v), CTRL, 0xF, 0xF, true));
}
DI float sum32(float v) {
  v += dppf<0xB1>(v); v += dppf<0x4E>(v); v += dppf<0x141>(v); v += dppf<0x140>(v);
  unsigned u = __builtin_bit_cast(unsigned, v);
  auto r = __builtin_amdgcn_permlane16_swap(u, u, false, false);
  return __builtin_bit_cast(float, (unsigned)r[0]) + __builtin_bit_cast(float, (unsigned)r[1]);
}
DI int stat_of(int p) { return ((p >> 2) & 1) * 4096 + (p >> 3) * 16 + (p & 3) * 4; }
DI int token_of(int p) {
  const int l = (p >> 3) & 63, e = p & 7;
  return 32 * (16 * (p >> 9) + 4 * (l >> 4) + (e & 3)) + (l & 15) + 16 * (e >> 2);
}
DI v4u make_srd(const void* base, unsigned bytes) { return v4u{(unsigned)(size_t)base, (unsigned)((size_t)base >> 32) & 0xFFFFu, bytes, 0x00020000u}; }
template <int SI, int LI> DI void glds_in(const v4u& srd, int voff, int soff, unsigned lds) {
  int t;
  asm volatile("s_add_u32 m0, %4, %6\n\ts_add_u32 %0, %3, %5\n\tbuffer_load_dwordx4 %1, %2, %0 offen lds"
               : "=&s"(t) : "v"(voff), "s"(srd), "s"(soff), "s"(lds), "i"(SI), "i"(LI) : "memory", "scc");
}
template <int IMM> DI void ld2u16(unsigned& a, unsigned& b, int voff, const v4u& sa, const v4u& sb, int soff) {
  asm volatile("buffer_load_ushort %0, %2, %3, %5 offen offset:%6\n\tbuffer_load_ushort %1, %2, %4, %5 offen offset:%6"
               : "=&v"(a), "=&v"(b) : "v"(voff), "s"(sa), "s"(sb), "s"(soff), "i"(IMM));
}
DI v4u cosfrag(float a0, float step, int off) {
  float v[8];
#pragma unroll
  for (int j = 0; j < 8; ++j) v[j] = __builtin_amdgcn_cosf(__builtin_fmaf(step, (float)(off + j), a0));
  return v4u{pk(v[0], v[1]), pk(v[2], v[3]), pk(v[4], v[5]), pk(v[6], v[7])};
}

constexpr int M_Z = 0;
constexpr int M_RB = 40960;
constexpr int M_TB = 81920;
constexpr int M_S1 = 131072;
constexpr int M_S2 = M_S1 + 2048;
constexpr int M_C1 = M_S2 + 1024;
constexpr int M_C2 = M_C1 + 2 * 4608;
constexpr int M_LDS = M_C2 + 2 * 4608;
#define STAMP(i)
#define PST(i)
#define PSK(i)
#define KSTAMP(i)
#define CSTAMP(i)
#define RETRY(i)
#define DELAY_B 1
#define DELAY_C 1

template <int IMM> DI void ldx4(v4u& dst, const v4u& srd, int voff, int soff) {
  asm volatile("buffer_load_dwordx4 %0, %1, %2, %3 offen offset:%4 sc1" : "=v"(dst) : "v"(voff), "s"(srd), "s"(soff), "i"(IMM));
}
struct WSrc { v4u srd, srd1; int lane16, base; };
constexpr int NW_COL = 52;
template <int N, bool COL = true> DI void load_w(v4u (&W)[64], const WSrc& s) {
  if constexpr (!COL || N < NW_COL) wload<((N >> 1) & 3) * 1024, false>(W[N], (N & 1) ? s.srd1 : s.srd, s.lane16, s.base + (N & 1) * 32768 + (N >> 3) * 4096);
}
template <int LO, int HI> DI void load_ws(v4u (&W)[64], const WSrc& s) { SB(); sfor<LO, HI>([&](auto J) { load_w<J.v>(W, s); }); SB(); }
DI unsigned mxacc(unsigned m, const v4u& v) { return umax(umax(umax(umax(m, v.x), v.y), v.z), v.w); }
DI unsigned tagacc(unsigned m, const v4u& v, unsigned tag) { return m | ((v.x ^ tag) | (v.y ^ tag)) | ((v.z ^ tag) | (v.w ^ tag)); }
DI unsigned andacc(unsigned m, const v4u& v) { return (m & v.x & v.y) & (v.z & v.w); }
DI unsigned long long bit0_set(unsigned m) { return __builtin_amdgcn_ballot_w64((m & 1u) != 0u); }
DI unsigned long long bit0_clear(unsigned m) { return __builtin_amdgcn_ballot_w64((m & 1u) == 0u); }
DI void pins(unsigned long long& k) { asm volatile("" : "+s"(k)); }
DI void pinf(float& a) { asm volatile("" : "+v"(a)); }
DI void pinu(unsigned& a) { asm volatile("" : "+v"(a)); }

DI void publish(rsrc_t rs, int which, int c, unsigned long long tag, int w, int lane) {
  if (w == 0 && lane < 8)
    __builtin_amdgcn_raw_buffer_store_b64(v2u{(unsigned)tag, (unsigned)(tag >> 32)}, rs, OFF_FL + which * 16384 + lane * 2048 + c * 8, 0, SC1);
}
DI void flags_load(v4u (&f)[2], const v4u& srd, int which, int c, int lane) {
  ldx4<0>(f[0], srd, lane * 16, OFF_FL + which * 16384 + (c & 7) * 2048);
  ldx4<1024>(f[1], srd, lane * 16, OFF_FL + which * 16384 + (c & 7) * 2048);
}
DI void flags_wait(v4u (&f)[2], const v4u& srd, int which, int c, int lane, unsigned long long tag) {
  const unsigned lo = (unsigned)tag, hi = (unsigned)(tag >> 32);
  auto missing = [&]() {
    pin(f[0]); pin(f[1]);
    const unsigned d = ((f[0].x ^ lo) | (f[0].y ^ hi) | (f[0].z ^ lo)) | ((f[0].w ^ hi) | (f[1].x ^ lo) | (f[1].y ^ hi)) | ((f[1].z ^ lo) | (f[1].w ^ hi));
    return __builtin_amdgcn_ballot_w64(d != 0u) != 0ull;
  };
  if (__builtin_expect(missing(), 0)) {
    unsigned spin = 0;
    do { __builtin_amdgcn_s_sleep(2); flags_load(f, srd, which, c, lane); vmwait<0>(); } while (missing() && ++spin < SPIN_MAX);
  }
}

struct ColConst { v4u C1[2], S1[2], N1[2], C2[2], S2[2], N2[2]; float tc[8], ts[8]; };

template <class Mid> DI void col_consts(ColConst& k, int w, int lane, Mid&& mid) {
  const int g = lane >> 4, r = lane & 15;
  const float k1 = (float)(16 * w + r);
  const unsigned neg = 0x80008000u;
  sfor<0, 2>([&](auto CS) {
    constexpr int cs = CS.v;
    const float st = k1 * (1.f / 64.f), a0 = st * (float)(8 * g);
    k.C1[cs] = cosfrag(a0, st, 32 * cs);
    pin(k.C1[cs]);
    k.S1[cs] = cosfrag(a0 - 0.25f, st, 32 * cs);
    k.N1[cs] = k.S1[cs] ^ neg;
    pin(k.S1[cs]); pin(k.N1[cs]);
  });
  sfor<0, 2>([&](auto T) {
    constexpr int t = T.v;
    const float st = (float)(16 * t + r) * (1.f / 32.f), a0 = st * (float)(4 * g);
    float c[8], s[8];
#pragma unroll
    for (int j = 0; j < 8; ++j) c[j] = __builtin_amdgcn_cosf(__builtin_fmaf(st, (float)(16 * (j >> 2) + (j & 3)), a0));
    k.C2[t] = v4u{pk(c[0], c[1]), pk(c[2], c[3]), pk(c[4], c[5]), pk(c[6], c[7])};
    pin(k.C2[t]);
#pragma unroll
    for (int j = 0; j < 8; ++j) s[j] = __builtin_amdgcn_sinf(__builtin_fmaf(st, (float)(16 * (j >> 2) + (j & 3)), a0));
    k.S2[t] = v4u{pk(s[0], s[1]), pk(s[2], s[3]), pk(s[4], s[5]), pk(s[6], s[7])};
    k.N2[t] = k.S2[t] ^ neg;
    pin(k.S2[t]); pin(k.N2[t]);
  });
  SB();
  mid();
  SB();
  sfor<0, 2>([&](auto T) {
    constexpr int t = T.v;
#pragma unroll
    for (int e = 4 * t; e < 4 * t + 4; ++e) {
      const float a = k1 * (1.f / 2048.f) * (float)(16 * (e >> 2) + 4 * g + (e & 3));
      k.tc[e] = __builtin_amdgcn_cosf(a); k.ts[e] = __builtin_amdgcn_sinf(a);
      pinf(k.tc[e]); pinf(k.ts[e]);
    }
  });
}

template <bool FIRST> DI void m16(f32x4& acc, const v4u& a, const v4u& b) {
  SB();
  if constexpr (FIRST) asm volatile("v_mfma_f32_16x16x32_bf16 %0, %1, %2, 0" : "=&v"(acc) : "v"(a), "v"(b));
  else asm volatile("v_mfma_f32_16x16x32_bf16 %0, %1, %2, %0" : "+v"(acc) : "v"(a), "v"(b));
  SB();
}
DI void m16_done(f32x4& a, f32x4& b) { asm volatile("s_nop 7" : "+v"(a), "+v"(b)); }
DI void m16_ready(v4u& a, v4u& b) { asm volatile("s_nop 1" : "+v"(a), "+v"(b)); }
template <int PART> DI float cmul(float a, float b, float c, float s) {
  float r = PART == 0 ? __builtin_fmaf(a, c, -(b * s)) : __builtin_fmaf(a, s, b * c);
  pinf(r);
  return r;
}

template <int WB>
DI void col_compute(LAS unsigned char* L, rsrc_t rs, const ColConst& k, const float (&fr)[8], const float (&fi)[8], v4u zr, v4u zi,
                    const float (&rstd)[8], int c, int w, int lane, int out_off, v4u (&W)[64], const WSrc& ws) {
  const int g = lane >> 4, r = lane & 15;
  const int first = is_zero(c);
  const float f0 = first ? 0.f : 1.f;
  float xr[8], xi[8];
#pragma unroll
  for (int i = 0; i < 4; ++i) { xr[2 * i] = blo(zr[i]); xr[2 * i + 1] = bhi(zr[i]); xi[2 * i] = blo(zi[i]); xi[2 * i + 1] = bhi(zi[i]); }
#pragma unroll
  for (int t = 0; t < 2; ++t) {
    const f32x4 rv = {rstd[4 * t], rstd[4 * t + 1], rstd[4 * t + 2], rstd[4 * t + 3]};
    const f32x4 a = f32x4{xr[4 * t], xr[4 * t + 1], xr[4 * t + 2], xr[4 * t + 3]} * rv;
    const f32x4 bq = f32x4{xi[4 * t], xi[4 * t + 1], xi[4 * t + 2], xi[4 * t + 3]} * (rv * f0);
    LAS unsigned char* d = L + M_C1 + (r + 16 * t) * 144 + (16 * w + 4 * g) * 2;
    *(LAS v2u*)d = v2u{pk(a[0], a[1]), pk(a[2], a[3])};
    *(LAS v2u*)(d + 4608) = v2u{pk(bq[0], bq[1]), pk(bq[2], bq[3])};
  }
  load_ws<WB, WB + 3>(W, ws);
  BAR();
  f32x4 dr[2], di[2];
  float ur[8], ui[8];
  {
    v4u ar[2][2], ai[2][2];
#pragma unroll
    for (int t = 0; t < 2; ++t)
#pragma unroll
      for (int cs = 0; cs < 2; ++cs) {
        LAS unsigned char* s = L + M_C1 + (r + 16 * t) * 144 + (32 * cs + 8 * g) * 2;
        ar[t][cs] = *(LAS v4u*)s; ai[t][cs] = *(LAS v4u*)(s + 4608);
      }
    load_ws<WB + 3, WB + 5>(W, ws);
    sfor<0, 16>([&](auto N) {
      constexpr int n = N.v, t = n >> 3, cs = (n >> 2) & 1, m = n & 3;
      if constexpr (m == 0) m16<cs == 0>(dr[t], ar[t][cs], k.C1[cs]);
      if constexpr (m == 1) m16<cs == 0>(di[t], ai[t][cs], k.C1[cs]);
      if constexpr (m == 2) m16<false>(dr[t], ai[t][cs], k.S1[cs]);
      if constexpr (m == 3) m16<false>(di[t], ar[t][cs], k.N1[cs]);
      if constexpr (m == 3) load_w<WB + 5 + (n >> 2)>(W, ws);
      if constexpr (n >= 9) {
        constexpr int e = (n - 9) >> 1;
        if constexpr (n & 1) ur[e] = cmul<0>(dr[0][e], di[0][e], k.tc[e], -k.ts[e]);
        else ui[e] = cmul<1>(dr[0][e], di[0][e], k.tc[e], -k.ts[e]);
      }
    });
    ui[3] = cmul<1>(dr[0][3], di[0][3], k.tc[3], -k.ts[3]);
  }
  v4u br, bi;
  br.x = pk(ur[0], ur[1]); br.y = pk(ur[2], ur[3]); bi.x = pk(ui[0], ui[1]); bi.y = pk(ui[2], ui[3]);
  SB();
  m16_done(dr[1], di[1]);
  load_ws<WB + 9, WB + 11>(W, ws);
#pragma unroll
  for (int e = 4; e < 8; ++e) {
    ur[e] = dr[1][e - 4] * k.tc[e] + di[1][e - 4] * k.ts[e]; ui[e] = di[1][e - 4] * k.tc[e] - dr[1][e - 4] * k.ts[e];
  }
  br.z = pk(ur[4], ur[5]); br.w = pk(ur[6], ur[7]); bi.z = pk(ui[4], ui[5]); bi.w = pk(ui[6], ui[7]);
  m16_ready(br, bi);
  sfor<0, 8>([&](auto N) {
    constexpr int n = N.v, t = n >> 2, m = n & 3;
    if constexpr (m == 0) m16<true>(dr[t], k.C2[t], br);
    if constexpr (m == 1) m16<true>(di[t], k.C2[t], bi);
    if constexpr (m == 2) m16<false>(dr[t], k.S2[t], bi);
    if constexpr (m == 3) m16<false>(di[t], k.N2[t], br);
    if constexpr (m == 3) load_w<WB + 11 + t>(W, ws);
    if constexpr (n == 5) ur[0] = cmul<0>(dr[0][0], di[0][0], fr[0], fi[0]);
    if constexpr (n == 6) ui[0] = cmul<1>(dr[0][0], di[0][0], fr[0], fi[0]);
  });
#pragma unroll
  for (int e = 1; e < 4; ++e) { ur[e] = dr[0][e] * fr[e] - di[0][e] * fi[e]; ui[e] = dr[0][e] * fi[e] + di[0][e] * fr[e]; }
  br.x = pk(ur[0], ur[1]); br.y = pk(ur[2], ur[3]); bi.x = pk(ui[0], ui[1]); bi.y = pk(ui[2], ui[3]);
  SB();
  m16_done(dr[1], di[1]);
  load_ws<WB + 13, WB + 15>(W, ws);
#pragma unroll
  for (int e = 4; e < 8; ++e) { ur[e] = dr[1][e - 4] * fr[e] - di[1][e - 4] * fi[e]; ui[e] = dr[1][e - 4] * fi[e] + di[1][e - 4] * fr[e]; }
  br.z = pk(ur[4], ur[5]); br.w = pk(ur[6], ur[7]); bi.z = pk(ui[4], ui[5]); bi.w = pk(ui[6], ui[7]);
  m16_ready(br, bi);
  sfor<0, 8>([&](auto N) {
    constexpr int n = N.v, t = n >> 2, m = n & 3;
    if constexpr (m == 0) m16<true>(dr[t], k.C2[t], br);
    if constexpr (m == 1) m16<true>(di[t], k.C2[t], bi);
    if constexpr (m == 2) m16<false>(dr[t], k.N2[t], bi);
    if constexpr (m == 3) m16<false>(di[t], k.S2[t], br);
    if constexpr (m == 3) load_w<WB + 15 + t>(W, ws);
    if constexpr (n == 5) ur[0] = cmul<0>(dr[0][0], di[0][0], k.tc[0], k.ts[0]);
    if constexpr (n == 6) ui[0] = cmul<1>(dr[0][0], di[0][0], k.tc[0], k.ts[0]);
  });
#pragma unroll
  for (int t = 0; t < 2; ++t) {
    if (t == 1) { SB(); m16_done(dr[1], di[1]); load_ws<WB + 17, WB + 19>(W, ws); }
#pragma unroll
    for (int i = (t == 0 ? 1 : 0); i < 4; ++i) {
      const int e = 4 * t + i;
      ur[e] = dr[t][i] * k.tc[e] - di[t][i] * k.ts[e]; ui[e] = di[t][i] * k.tc[e] + dr[t][i] * k.ts[e];
    }
    LAS unsigned char* d = L + M_C2 + (16 * w + r) * 72 + (16 * t + 4 * g) * 2;
    *(LAS v2u*)d = v2u{pk(ur[4 * t], ur[4 * t + 1]), pk(ur[4 * t + 2], ur[4 * t + 3])};
    *(LAS v2u*)(d + 4608) = v2u{pk(ui[4 * t], ui[4 * t + 1]), pk(ui[4 * t + 2], ui[4 * t + 3])};
  }
  load_ws<WB + 19, WB + 21>(W, ws);
  BAR();
  unsigned od[8];
  {
    v4u tr_[2][2], ti_[2][2];
#pragma unroll
    for (int t = 0; t < 2; ++t)
#pragma unroll
      for (int cs = 0; cs < 2; ++cs) {
        LAS unsigned char* s = L + M_C2 + (32 * cs + 8 * g + (r >> 2)) * 72 + (16 * t + 4 * (r & 3)) * 2;
        tr_[t][cs] = tr2(s, 4 * 72); ti_[t][cs] = tr2(s + 4608, 4 * 72);
      }
    load_ws<WB + 21, WB + 23>(W, ws);
    sfor<0, 16>([&](auto N) {
      constexpr int n = N.v, t = n >> 3, cs = (n >> 2) & 1, m = n & 3;
      if constexpr (m == 0) m16<cs == 0>(dr[t], k.C1[cs], tr_[t][cs]);
      if constexpr (m == 1) m16<cs == 0>(di[t], k.C1[cs], ti_[t][cs]);
      if constexpr (m == 2) m16<false>(dr[t], k.N1[cs], ti_[t][cs]);
      if constexpr (m == 3) m16<false>(di[t], k.S1[cs], tr_[t][cs]);
      if constexpr (m == 3) load_w<WB + 23 + (n >> 2)>(W, ws);
      if constexpr (n >= 9 && n < 13) {
        constexpr int e = n - 9;
        xr[e] += dr[0][e]; xi[e] = __builtin_fmaf(di[0][e], f0, xi[e]);
        pinf(xr[e]); pinf(xi[e]);
      }
      if constexpr (n == 14) { pk_tag(xr[0], xr[1], xr[2], xr[3], 1u, od[0], od[1]); pinu(od[0]); pinu(od[1]); }
    });
    pk_tag(xi[0], xi[1], xi[2], xi[3], 1u, od[4], od[5]);
    SB();
    m16_done(dr[1], di[1]);
#pragma unroll
    for (int e = 4; e < 8; ++e) { xr[e] += dr[1][e - 4]; xi[e] = __builtin_fmaf(di[1][e - 4], f0, xi[e]); }
    pk_tag(xr[4], xr[5], xr[6], xr[7], 1u, od[2], od[3]);
    pk_tag(xi[4], xi[5], xi[6], xi[7], 1u, od[6], od[7]);
  }
  const int vo = (w * 64 + lane) * 16;
  bstore_sc1(v4u{od[0], od[1], od[2], od[3]}, rs, vo, out_off + (2 * c + first) * ZS);
  bstore_sc1(v4u{od[4], od[5], od[6], od[7]}, rs, vo, out_off + (2 * c + 1 - first) * ZS);
  load_ws<WB + 27, WB + 30>(W, ws);
}

constexpr int NW_LATE = 12;
constexpr int NV_EARLY = 16;
static_assert(NW_COL + NW_LATE == 64, "");
constexpr int ZG[5] = {0, 4, 8, 16, 32};
constexpr int ZR[4] = {-1, 2, 9, 22};
constexpr int z_read_at(int ks) {
  for (int G = 1; G < 4; ++G) if (ks >= ZR[G] && 2 * (ks - ZR[G]) < ZG[G + 1] - ZG[G]) return ZG[G] + 2 * (ks - ZR[G]);
  return -1;
}
constexpr int z_first(int j) { for (int G = 0; G < 4; ++G) if (ZG[G] == j) return 1; return 0; }
constexpr int z_decide_at(int ks) { for (int G = 1; G < 4; ++G) if (ks == ZR[G] + (ZG[G + 1] - ZG[G]) / 2) return G; return -1; }
constexpr int z_requested(int ks) { return ks < 23 ? 9 + ks : 32; }
constexpr int z_younger(int j, int ks) {
  int n = z_requested(ks) - j - 1;
  for (int k = 0; k < ks && k < 14 + NV_EARLY; ++k) if (k >= j - 8 && (k < NW_LATE || k >= 14)) ++n;
  return n;
}
constexpr int sq_at(int ks, int g) {
  const int o6[5] = {0, 0, 2, 5, 6}, o4[5] = {0, 1, 3, 3, 4};
  return ks < 8 ? 0 : ks < 24 ? 6 * (ks - 8) + o6[g] : 96 + 4 * (ks - 24) + o4[g];
}
struct ZSrc { v4u srd; int vo, so; unsigned lds; };
template <int J> DI void z_issue(const ZSrc& z, int so) {
  int t;
  asm volatile("s_add_u32 m0, %1, %5\n\ts_add_u32 %0, %4, %6\n\tbuffer_load_dwordx4 %2, %3, %0 offen sc1 lds"
               : "=&s"(t) : "s"(z.lds), "v"(z.vo), "s"(z.srd), "s"(so), "i"((J >> 4) * 65536 + (J & 15) * 4096), "i"((J & 15) * 32 * ZS + (J >> 4) * 128)
               : "memory", "scc");
}
template <int J> DI v4u z_back(LAS unsigned char* zown) { return *(LAS v4u*)(zown + (J >> 4) * 65536 + (J & 15) * 4096); }
template <int G, int KS> DI void z_fix(unsigned long long bad, const ZSrc& z, LAS unsigned char* zown, LAS unsigned char* L, int layer, int w) {
  if (__builtin_expect(bad != 0ull, 0)) {
    unsigned spin = 0, m;
    vmwait<0>();
    do {
      RETRY(G == 0 ? 24 : 25);
      const int zo = opaque(z.so);
      sfor<ZG[G], ZG[G + 1]>([&](auto I) { z_issue<I.v>(z, zo); });
      vmwait<0>();
      m = 1u;
      sfor<ZG[G], ZG[G + 1]>([&](auto I) { m = andacc(m, z_back<I.v>(zown)); });
    } while (bit0_clear(m) != 0ull && ++spin < SPIN_MAX);
    sfor<ZG[G + 1], z_requested(KS)>([&](auto I) { z_issue<I.v>(z, opaque(z.so)); });
    if constexpr (G > 0) vmwait<0>();
  }
}

#define HA_START 14
#define HB_START 8
#define H_ROOM 23
#define H_ROOM0 18
constexpr int NSTR = 56 - NV_EARLY + 58;
struct Stream { int kind[NSTR], idx[NSTR], end[89]; };
constexpr Stream make_stream() {
  Stream s{};
  int kind[NSTR] = {}, idx[NSTR] = {}, rel[NSTR] = {}, need[NSTR] = {};
  bool done[NSTR] = {};
  int n = 0;
  for (int i = NV_EARLY; i < 56; ++i) { kind[n] = 0; idx[n] = i; rel[n] = (i - NV_EARLY) >> 1; need[n] = i >> 1; ++n; }
  for (int i = 0; i < 28; ++i) { kind[n] = 1; idx[n] = i; rel[n] = HA_START; need[n] = i; ++n; }
  for (int i = 0; i < 28; ++i) { kind[n] = 2; idx[n] = i; rel[n] = 33 + HB_START; need[n] = 28 + i; ++n; }
  for (int i = 0; i < 2; ++i) { kind[n] = 3; idx[n] = i; rel[n] = 33 + HB_START; need[n] = 60; ++n; }
  int e = 0, held = 0;
  for (int sl = 0; sl < 89; ++sl) {
    int cap = sl < 8 ? 1 + (sl & 1) : sl < 32 ? 2 : sl == 32 ? 4 : (sl - 33) % 7 == 6 ? 0 : 2;
    for (; cap > 0; --cap) {
      int best = -1;
      const int live = held - (sl < 33 ? 0 : sl - 32), room = sl < 33 ? H_ROOM0 : H_ROOM;
      for (int i = 0; i < NSTR; ++i)
        if (!done[i] && rel[i] <= sl && (kind[i] == 0 || live < room) && (best < 0 || need[i] < need[best])) best = i;
      if (best < 0) break;
      if (kind[best] != 0) ++held;
      done[best] = true; s.kind[e] = kind[best]; s.idx[e] = idx[best]; ++e;
    }
    s.end[sl] = e;
  }
  return s;
}
constexpr Stream STR = make_stream();
static_assert(STR.end[82] == NSTR, "every load is issued");
constexpr int vslot(int n) { return n - NV_EARLY; }
constexpr int str_pos(int kind, int idx) { for (int i = 0; i < NSTR; ++i) if (STR.kind[i] == kind && STR.idx[i] == idx) return i; return -1; }
constexpr int str_count(int kind, int upto) { int n = 0; for (int i = 0; i < upto; ++i) if (STR.kind[i] == kind) ++n; return n; }
struct Down {
  int vbase, hbase, xso;
  DI int blk(int g) const { return g * 4096; }
};
template <int LO, int HI> DI void stream_range(v4u (&W)[64], v4u (&hA)[28], v4u (&hB)[28], v4u (&x3)[2], const v4u& srd, int lane16, const Down& d) {
  sfor<LO, HI>([&](auto I) {
    constexpr int k = STR.kind[I.v], i = STR.idx[I.v];
    if constexpr (k == 0) wload<((i >> 1) & 3) * 1024, true>(W[vslot(i)], srd, lane16, d.vbase + (i & 1) * 112 * 1024 + d.blk(i >> 3));
    if constexpr (k == 1) ldx4<(i & 3) * 1024>(hA[i], srd, lane16, d.hbase + d.blk(i >> 2));
    if constexpr (k == 2) ldx4<(i & 3) * 1024>(hB[i], srd, lane16, d.hbase + 224 * 1024 + d.blk(i >> 2));
    if constexpr (k == 3) ldx4<0>(x3[i], srd, lane16, d.xso + i * 16 * ZS);
  });
}
template <int SLOT, int PART> DI void stream_issue(v4u (&W)[64], v4u (&hA)[28], v4u (&hB)[28], v4u (&x3)[2], const v4u& srd, int lane16, const Down& d) {
  constexpr int lo = SLOT == 0 ? 0 : STR.end[SLOT - 1], hi = STR.end[SLOT];
  stream_range<(PART == 1 ? lo + 1 : lo), (PART == 0 && hi > lo ? lo + 1 : hi)>(W, hA, hB, x3, srd, lane16, d);
}
constexpr int dn_need(int g) {
  const int H = g / 28, i = g % 28;
  const int ph = str_pos(1 + H, i), pv = H == 0 && 2 * i >= NV_EARLY ? str_pos(0, 2 * i + 1) : 0;
  return ph > pv ? ph : pv;
}
template <int G, int ISSUED> DI void h_acc(v4u (&hA)[28], v4u (&hB)[28], unsigned& m, unsigned tag) {
  static_assert(dn_need(G) < ISSUED, "requested in time");
  const v4u& x = G < 28 ? hA[G % 28] : hB[G % 28];
  unsigned t0, t1;
  asm volatile("s_waitcnt vmcnt(%8)\n\tv_bitop3_b32 %1, %3, %4, %7 bitop3:0x7e\n\tv_bitop3_b32 %2, %5, %6, %7 bitop3:0x7e\n\tv_or3_b32 %0, %0, %1, %2"
               : "+v"(m), "=&v"(t0), "=&v"(t1) : "v"(x.x), "v"(x.y), "v"(x.z), "v"(x.w), "s"(tag),
                 "i"(ISSUED - 1 - dn_need(G) + (G >= 7 && dn_need(G) < STR.end[32] ? 4 : 0)));
}
DI void and_acc(unsigned& m, const v4u& x) {
  asm volatile("v_bitop3_b32 %0, %0, %1, %2 bitop3:0x80\n\tv_bitop3_b32 %0, %0, %3, %4 bitop3:0x80" : "+v"(m) : "v"(x.x), "v"(x.y), "v"(x.z), "v"(x.w));
}
template <int G, int ISSUED> DI void h_decide(v4u (&hA)[28], v4u (&hB)[28], unsigned long long bad, const v4u& srd, int lane16, const Down& d,
                                              LAS unsigned char* L, int layer, int w) {
  constexpr int H = G / 28, LO = G % 28;
  v4u (&hh)[28] = H == 0 ? hA : hB;
  if (__builtin_expect(bad != 0ull, 0)) {
    unsigned spin = 0, m;
    vmwait<0>();
    do {
      RETRY(26 + H);
      const int bo = opaque(d.hbase);
      sfor<LO, LO + 7>([&](auto I) { ldx4<(I.v & 3) * 1024>(hh[I.v], srd, lane16, bo + H * 224 * 1024 + d.blk(I.v >> 2)); });
      vmwait<0>();
      m = 0;
#pragma unroll
      for (int i = LO; i < LO + 7; ++i) { pin(hh[i]); m = tagacc(m, hh[i], layer); }
    } while (bit0_set(m) != 0ull && ++spin < SPIN_MAX);
  }
}

DI void sumsq1(float& ss, float x) { asm volatile("v_fmac_f32 %0, %1, %1" : "+v"(ss) : "v"(x)); }
DI float mulf(float a, float b) { float r; asm volatile("v_mul_f32 %0, %1, %2" : "=v"(r) : "v"(a), "v"(b)); return r; }
DI void addf4(f32x4& a, const f32x4& b) {
  float x = a.x, y = a.y, z = a.z, t = a.w;
  asm volatile("v_add_f32 %0, %0, %4\n\tv_add_f32 %1, %1, %5\n\tv_add_f32 %2, %2, %6\n\tv_add_f32 %3, %3, %7"
               : "+v"(x), "+v"(y), "+v"(z), "+v"(t) : "v"(b.x), "v"(b.y), "v"(b.z), "v"(b.w));
  a = f32x4{x, y, z, t};
}
DI float relu(float a) { const int i = __builtin_bit_cast(int, a); return __builtin_bit_cast(float, i > 0 ? i : 0); }
template <bool HI> DI unsigned h_low(unsigned c, float a, unsigned tag) {
  return (__builtin_amdgcn_perm(c, asu(a), HI ? 0x07060302u : 0x05040302u) & ~1u) | tag;
}
DI v4u h_pack(const f32x16& a, unsigned tag) {
  v4u d;
#pragma unroll
  for (int i = 0; i < 2; ++i) {
    const unsigned c = pk(relu(a[4 * i + 1]) * a[4 * i + 9], relu(a[4 * i + 3]) * a[4 * i + 11]);
    d[2 * i] = h_low<false>(c, relu(a[4 * i]) * a[4 * i + 8], tag);
    d[2 * i + 1] = h_low<true>(c, relu(a[4 * i + 2]) * a[4 * i + 10], tag);
  }
  return d;
}
DI void x3_piece(const f32x16& a, rsrc_t rs, int lane16, int tile) {
#pragma unroll
  for (int g = 0; g < 2; ++g) {
    unsigned d[4];
    pk_tag(a[8 * g], a[8 * g + 1], a[8 * g + 2], a[8 * g + 3], 1u, d[0], d[1]);
    pk_tag(a[8 * g + 4], a[8 * g + 5], a[8 * g + 6], a[8 * g + 7], 1u, d[2], d[3]);
    bstore_sc1(v4u{d[0], d[1], d[2], d[3]}, rs, lane16, OFF_ZA + (tile >> 1) * ZS + (tile & 1) * 2048 + g * 1024);
  }
}

DI void ffn(LAS unsigned char* L, rsrc_t rs, const WSrc& ws, const Params& p, v4u (&W)[64], int layer, int c, int rb, int fs, int pi, int nb,
            int w, int lane) {
  const v4u& srd = ws.srd;
  constexpr int hoff = OFF_H;
  const int h = lane >> 5, r = lane & 31, q = (lane & 15) >> 2, lane16 = lane * 16;
  ZSrc z;
  z.srd = srd;
  z.vo = (8 * w + (lane >> 3)) * ZS + (((lane & 7) ^ ((lane >> 2) & 4)) * 16);
  z.so = OFF_ZA + layer * (OFF_ZB - OFF_ZA) + rb * 256;
  z.lds = (unsigned)(size_t)L + M_Z + w * 1024;
  STAMP(4);
  sfor<0, 8>([&](auto I) { z_issue<I.v>(z, z.so); });
  SB();
  LAS unsigned char* zown = L + M_Z + w * 1024 + lane16;
  LAS unsigned char* zr[2];
#pragma unroll
  for (int s = 0; s < 2; ++s)
    zr[s] = L + M_Z + (4 * h + q) * 128 + (4 * (s ^ (q >> 1)) + 2 * ((lane >> 4) & 1) + ((lane & 3) >> 1)) * 16 + 8 * (lane & 1);
  const unsigned dcmask = (w == 0 && h == 0) ? 0xFFFF0000u : 0xFFFFFFFFu;
  const int pt = 4 * rb + pi;
  Down dn;
  dn.vbase = OFF_W2 + layer * (OFF_W3 - OFF_W2) + (nb * 2 * 112 + 28 * w) * 1024;
  dn.hbase = hoff + (pt * 112 + 28 * w) * 1024;
  dn.xso = OFF_W1 + layer * (OFF_ZA - OFF_W1) + (pt * 8 + nb) * ZS + w * 1024;
  const int ks0 = fs * 7 + 2 * w;
  const bool two = w < 3;
  const bool idft = opaque(w + layer) == 4;
  const rsrc_t rs2 = __builtin_amdgcn_make_buffer_rsrc((void*)p.ws, 0, two ? WS_BYTES : 0, 0x00020000);
  v4u hA[28], hB[28], x3[2], res[2], Ve[NV_EARLY];
  STAMP(5);
  vmwait<z_younger(3, -1)>();
  z_fix<0, -1>(bit0_clear(andacc(andacc(andacc(andacc(1u, z_back<0>(zown)), z_back<1>(zown)), z_back<2>(zown)), z_back<3>(zown))), z, zown, L, layer, w);
  STAMP(6);
  BAR();
  STAMP(7);
  CSTAMP(20);
  f32x16 acc[2][2][2];
  float ss[2] = {0.f, 0.f};
  v4u bf[2][2], sq[2];
  unsigned hm = 0;
  unsigned long long bad;
  LAS unsigned char* zb[4] = {zr[0], zr[1], zr[0] + 65536, zr[1] + 65536};
  LAS unsigned char* zq = (pi ? zr[1] : zr[0]) + w * 16384;
  LAS unsigned char* zq2 = zq + 65536;
  asm volatile("" : "+v"(zb[2]), "+v"(zb[3]), "+v"(zq), "+v"(zq2));
  auto square = [&](auto U) {
    constexpr int n = U.v >> 3, d = (U.v >> 1) & 3;
    if constexpr (U.v & 1) sumsq1(ss[U.v >> 6], bhi(sq[n & 1][d]));
    else sumsq1(ss[U.v >> 6], blo((U.v & 63) == 0 ? sq[0][0] & dcmask : sq[n & 1][d]));
  };
#pragma unroll
  for (int s = 0; s < 2; ++s) bf[0][s] = tr2(zb[s], 1024);
  {
    v4u rbk[2];
    unsigned zm = 1u;
    sfor<0, 32>([&](auto KS) {
      constexpr int ks = KS.v;
      constexpr int rd = z_read_at(ks), rp = ks > 0 ? z_read_at(ks - 1) : -1, dec = z_decide_at(ks);
      mfma_w<ks == 0, true>(acc[0][0][0], W[2 * ks], bf[ks & 1][0]);
      SB();
      if constexpr (ks < 31) { bf[(ks + 1) & 1][1] = tr2(zb[1] + (ks + 1) * 2048, 1024); SB(); bf[(ks + 1) & 1][0] = tr2(zb[0] + (ks + 1) * 2048, 1024); }
      else { bf[0][1] = tr2(zb[3], 1024); SB(); bf[0][0] = tr2(zb[2], 1024); }
      SB();
      mfma_w<ks == 0, true>(acc[0][1][0], W[2 * ks + 1], bf[ks & 1][0]);
      SB();
      if constexpr (ks < 24) z_issue<8 + ks>(z, z.so);
      if constexpr (rp >= 0) and_acc(zm, rbk[0]);
      SB();
      mfma_w<ks == 0, true>(acc[0][0][1], W[2 * ks], bf[ks & 1][1]);
      SB();
      if constexpr (rp >= 0) and_acc(zm, rbk[1]);
      if constexpr (dec >= 0) { bad = bit0_clear(zm); pins(bad); }
      SB();
      mfma_w<ks == 0, true>(acc[0][1][1], W[2 * ks + 1], bf[ks & 1][1]);
      SB();
      if constexpr (dec >= 0) { pins(bad); z_fix<dec, ks>(bad, z, zown, L, layer, w); zm = 1u; asm volatile("s_barrier" ::: "memory"); }
      if constexpr (rd >= 0) { vmwait<z_younger(rd + 1, ks)>(); rbk[0] = z_back<rd>(zown); rbk[1] = z_back<rd + 1>(zown); }
      if constexpr (ks < NW_LATE) load_w<64 - NW_LATE + ks, false>(W, ws);
      if constexpr (ks >= 14 && ks < 14 + NV_EARLY) {
        constexpr int n = ks - 14;
        vload<((n >> 1) & 3) * 1024>(Ve[n], srd, lane16, dn.vbase + (n & 1) * 112 * 1024 + dn.blk(n >> 3));
      }
      SB();
    });
  }
  STAMP(8);
  STAMP(9);
  {
    unsigned hq[4], hc;
    float hp[4];
    auto leave = [&](auto A) {
      constexpr int u = A.v / 7, k = A.v % 7, pt_ = u >> 2, ps = (u >> 1) & 1, o = 4 * (u & 1);
      if constexpr (k < 4) hp[k] = mulf(relu(acc[0][pt_][ps][o + k]), acc[0][pt_][ps][o + k + 8]);
      if constexpr (k == 4) { hc = pk(hp[1], hp[3]); pinu(hc); }
      if constexpr (k == 5) { hq[2 * (u & 1)] = h_low<false>(hc, hp[0], layer); pinu(hq[2 * (u & 1)]); }
      if constexpr (k == 6) { hq[2 * (u & 1) + 1] = h_low<true>(hc, hp[2], layer); pinu(hq[2 * (u & 1) + 1]); }
      if constexpr (k == 6 && (u & 1)) {
        bstore_sc1(v4u{hq[0], hq[1], hq[2], hq[3]}, pt_ == 0 ? rs : rs2, lane16, hoff + ((4 * rb + ps) * 112 + ks0 + pt_) * 1024);
        if constexpr (pt_ == 1) { if (idft) x3_piece(acc[0][1][ps], rs, lane16, (4 * rb + ps) * 16 + fs); }
      }
    };
    sfor<0, 32>([&](auto KS) {
      constexpr int ks = KS.v;
      auto atoms = [&](auto G) {
        constexpr int o7[5] = {0, 1, 3, 6, 7};
        if constexpr (ks < 8) sfor<7 * ks + o7[G.v], 7 * ks + o7[G.v + 1]>([&](auto A) { leave(A); });
        else sfor<sq_at(ks, G.v), sq_at(ks, G.v + 1)>([&](auto U) { square(U); });
      };
      mfma_w<ks == 0, true>(acc[1][0][0], W[2 * ks], bf[ks & 1][0]);
      SB();
      if constexpr (ks < 31) { bf[(ks + 1) & 1][1] = tr2(zb[3] + (ks + 1) * 2048, 1024); SB(); bf[(ks + 1) & 1][0] = tr2(zb[2] + (ks + 1) * 2048, 1024); }
      atoms(IC<0>{});
      SB();
      mfma_w<ks == 0, true>(acc[1][1][0], W[2 * ks + 1], bf[ks & 1][0]);
      SB();
      atoms(IC<1>{});
      if constexpr (ks > 0) stream_issue<ks - 1, 1>(W, hA, hB, x3, srd, lane16, dn);
      SB();
      mfma_w<ks == 0, true>(acc[1][0][1], W[2 * ks], bf[ks & 1][1]);
      SB();
      atoms(IC<2>{});
      if constexpr (ks >= 24 && ks < 31) h_acc<ks - 24, STR.end[ks - 1]>(hA, hB, hm, layer);
      if constexpr (ks == 31) { bad = bit0_set(hm); pins(bad); hm = 0; }
      SB();
      mfma_w<ks == 0, true>(acc[1][1][1], W[2 * ks + 1], bf[ks & 1][1]);
      SB();
      atoms(IC<3>{});
      sfor<0, 16>([&](auto N) {
        constexpr int n = N.v;
        if constexpr (n < 2 ? ks == 6 + n : sq_at(ks, 4) >= 8 * (n - 1) && sq_at(ks - 1, 4) < 8 * (n - 1))
          sq[n & 1] = tr2((n < 8 ? zq : zq2) + (n & 7) * 2048, 1024);
      });
      if constexpr (ks == 31) { pins(bad); h_decide<0, STR.end[30]>(hA, hB, bad, srd, lane16, dn, L, layer, w); }
      stream_issue<ks, (ks == 31 ? 2 : 0)>(W, hA, hB, x3, srd, lane16, dn);
      SB();
    });
  }
  STAMP(10);
  STAMP(11);
  stream_issue<32, 2>(W, hA, hB, x3, srd, lane16, dn);
  mfma_done(acc[1][0][0], acc[1][0][1], acc[1][1][0], acc[1][1][1]);
#pragma unroll
  for (int s = 0; s < 2; ++s) bstore_sc1(h_pack(acc[1][0][s], layer), rs, lane16, hoff + ((4 * rb + 2 + s) * 112 + ks0) * 1024);
#pragma unroll
  for (int s = 0; s < 2; ++s) bstore_sc1(h_pack(acc[1][1][s], layer), rs2, lane16, hoff + ((4 * rb + 2 + s) * 112 + ks0 + 1) * 1024);
  if (idft) {
#pragma unroll
    for (int s = 0; s < 2; ++s) x3_piece(acc[1][1][s], rs, lane16, (4 * rb + 2 + s) * 16 + fs);
  }
  SB();
  {
    auto x = __builtin_amdgcn_permlane32_swap(asu(ss[0]), asu(ss[1]), false, false);
    *(LAS float*)(L + M_S1 + w * 256 + lane * 4) = asf(x[0]) + asf(x[1]);
  }
  const int row = 16 * w + (lane >> 2);
  {
    const int n = nb * 64 + row;
    LAS unsigned char* ra = L + M_Z + n * 128 + (((4 * pi + (lane & 3)) ^ ((n & 2) << 1)) * 16);
    res[0] = *(LAS v4u*)ra; res[1] = *(LAS v4u*)(ra + 65536);
  }
  STAMP(12);
  CSTAMP(21);
  STAMP(13);
  LAS unsigned char* rw = L + M_Z + w * 9216 + r * 144 + h * 16;
  LAS unsigned char* rr = L + M_Z + row * 144 + (lane & 3) * 32;
  f32x16 dac[2][2];
  f32x4 fa[2][2], tq[2];
  auto red_write = [&](auto HH, auto K) {
    constexpr int H = HH.v, t = K.v >> 2, g = K.v & 3;
    *(LAS f32x4*)(rw + H * M_RB + t * 32 * 144 + g * 32) = f32x4{dac[H][t][4 * g], dac[H][t][4 * g + 1], dac[H][t][4 * g + 2], dac[H][t][4 * g + 3]};
  };
  sfor<0, 50>([&](auto GG) {
    constexpr int g = GG.v, H = g / 28, i = g % 28;
    v4u& hx = H == 0 ? hA[i] : hB[i];
    unsigned long long bad = 0ull;
    if constexpr (2 * i < NV_EARLY) mfma_v<i == 0>(dac[H][0], Ve[2 * i], hx);
    else mfma_w<false, false>(dac[H][0], W[vslot(2 * i)], hx);
    SB();
    if constexpr (g + 7 < 56) h_acc<g + 7, STR.end[32 + g]>(hA, hB, hm, layer);
    if constexpr (g + 7 < 56 && g % 7 == 6) { bad = bit0_set(hm); pins(bad); hm = 0; }
    SB();
    if constexpr (2 * i < NV_EARLY) mfma_v<i == 0>(dac[H][1], Ve[2 * i + 1], hx);
    else mfma_w<false, false>(dac[H][1], W[vslot(2 * i + 1)], hx);
    SB();
    if constexpr (g + 7 < 56 && g % 7 == 6) { pins(bad); h_decide<g + 1, STR.end[32 + g]>(hA, hB, bad, srd, lane16, dn, L, layer, w); }
    stream_issue<33 + g, 2>(W, hA, hB, x3, srd, lane16, dn);
    if constexpr (g == 9) BAR();
    if constexpr (g == 10) { LAS float* sp = (LAS float*)(L + M_S1 + lane * 4); tq[0] = f32x4{sp[0], sp[64], sp[128], sp[192]}; }
    if constexpr (g == 12) { tq[0].x = __builtin_fmaf((tq[0].x + tq[0].y) + (tq[0].z + tq[0].w), 0.5f, 1e-5f * 65536.f); asm volatile("" : "+v"(tq[0])); }
    if constexpr (g == 14) *(LAS float*)(L + M_S1 + 1024 + w * 256 + lane * 4) = __builtin_amdgcn_rcpf(tq[0].x);
    if constexpr (g >= 29 && g <= 39 && g != 34 && g != 35 && g != 36) red_write(IC<0>{}, IC<(g < 34 ? g - 29 : g - 32)>{});
    if constexpr (g == 40) BAR();
    if constexpr (g == 42) { fa[0][0] = *(LAS f32x4*)rr; fa[0][1] = *(LAS f32x4*)(rr + 16); }
    if constexpr (g >= 44 && g <= 46) { addf4(fa[0][0], tq[0]); }
    if constexpr (g >= 45 && g <= 47) { addf4(fa[0][1], tq[1]); }
    if constexpr (g >= 43 && g <= 45) tq[0] = *(LAS f32x4*)(rr + (g - 42) * 9216);
    if constexpr (g >= 44 && g <= 46) tq[1] = *(LAS f32x4*)(rr + (g - 43) * 9216 + 16);
    SB();
    if constexpr (g == 27) STAMP(14);
  });
  sfor<22, 28>([&](auto I) { mfma_w<false, false>(dac[1][0], W[vslot(2 * I.v)], hB[I.v]); });
  sfor<22, 28>([&](auto I) {
    mfma_w<false, false>(dac[1][1], W[vslot(2 * I.v + 1)], hB[I.v]);
    SB();
    if constexpr (I.v >= 23 && I.v < 27) red_write(IC<1>{}, IC<I.v - 23>{});
    SB();
  });
  STAMP(15);
  asm volatile("s_nop 12" : "+v"(dac[1][1]));
  sfor<4, 8>([&](auto K) { red_write(IC<1>{}, K); });
  vmwait<0>();
  pin(x3[0]); pin(x3[1]);
  if (__builtin_expect(layer == 1 && bit0_clear(andacc(andacc(1u, x3[0]), x3[1])) != 0ull, 0)) {
    unsigned spin = 0;
    do {
      RETRY(29);
      ldx4<0>(x3[0], srd, lane16, dn.xso); ldx4<0>(x3[1], srd, lane16, dn.xso + 16 * ZS);
      vmwait<0>();
      pin(x3[0]); pin(x3[1]);
    } while (bit0_clear(andacc(andacc(1u, x3[0]), x3[1])) != 0ull && ++spin < SPIN_MAX);
  }
  const bool dc = nb == 0 && row == 0;
  LAS unsigned char* tb = L + M_TB + w * 2560;
  auto finish = [&](auto HH) {
    constexpr int H = HH.v;
    f32x4 sc[2];
#pragma unroll
    for (int x = 0; x < 2; ++x) sc[x] = *(LAS f32x4*)(L + M_S1 + 1024 + w * 256 + H * 128 + (lane & 3) * 32 + x * 16);
    if (layer == 0) {
      unsigned o[4];
      float sq[8];
#pragma unroll
      for (int x = 0; x < 2; ++x)
        pk_tag(__builtin_fmaf(fa[H][x][0], sc[x][0], blo(res[H][2 * x])), __builtin_fmaf(fa[H][x][1], sc[x][1], bhi(res[H][2 * x])),
               __builtin_fmaf(fa[H][x][2], sc[x][2], blo(res[H][2 * x + 1])), __builtin_fmaf(fa[H][x][3], sc[x][3], bhi(res[H][2 * x + 1])), 0u,
               o[2 * x], o[2 * x + 1]);
      bstore_sc1(v4u{o[0], o[1], o[2], o[3]}, rs, row * ZS + (lane & 3) * 16, OFF_ZA + nb * 64 * ZS + pt * 64 + H * 128);
#pragma unroll
      for (int j = 0; j < 4; ++j) {
        const unsigned u = dc ? 0u : o[j];
        sq[2 * j] = blo(u) * blo(u); sq[2 * j + 1] = bhi(u) * bhi(u);
      }
      float t4[4], t2[2];
#pragma unroll
      for (int j = 0; j < 4; ++j) {
        auto x = __builtin_amdgcn_permlane32_swap(asu(sq[j]), asu(sq[j + 4]), false, false);
        t4[j] = asf(x[0]) + asf(x[1]);
      }
#pragma unroll
      for (int j = 0; j < 2; ++j) {
        auto x = __builtin_amdgcn_permlane16_swap(asu(t4[j]), asu(t4[j + 2]), false, false);
        t2[j] = asf(x[0]) + asf(x[1]);
        t2[j] += dppf<0x124>(t2[j]);
        t2[j] += dppf<0x128>(t2[j]);
      }
      *(LAS f32x2*)(L + M_S2 + w * 256 + H * 128 + (8 * (lane & 3) + 4 * h + 2 * ((lane >> 4) & 1)) * 4) = f32x2{t2[0], t2[1]};
    } else {
      const f32x4 a = fa[H][0] * sc[0], bq = fa[H][1] * sc[1];
      *(LAS v4u*)(tb + H * 1280 + (lane >> 2) * 80 + (lane & 3) * 16) = v4u{pk(a.x, a.y), pk(a.z, a.w), pk(bq.x, bq.y), pk(bq.z, bq.w)};
      const v4u y = tr2(tb + H * 1280 + (4 * h + q) * 80 + ((lane >> 4) & 1) * 32 + (lane & 3) * 8, 8 * 80);
      unsigned d[4];
#pragma unroll
      for (int j = 0; j < 4; ++j)
        d[j] = pk(__builtin_fmaf(blo(x3[H][j]), 1.f / 256.f, blo(y[j])), __builtin_fmaf(bhi(x3[H][j]), 1.f / 256.f, bhi(y[j])));
      auto r0 = __builtin_amdgcn_permlane32_swap(d[0], d[2], false, false);
      auto r1 = __builtin_amdgcn_permlane32_swap(d[1], d[3], false, false);
      const v4u o = {r0[0], r1[0], r0[1], r1[1]};
      const int tok = token_of(32 * (pt + 2 * H) + r);
      *(__attribute__((address_space(1))) v4u*)(p.out + (size_t)tok * 512 + nb * 64 + 16 * w + 8 * h) = o;
    }
  };
  finish(IC<0>{});
  STAMP(16);
  BAR();
  STAMP(17);
  f32x4 tr4[6];
  fa[1][0] = *(LAS f32x4*)(rr + M_RB); fa[1][1] = *(LAS f32x4*)(rr + M_RB + 16);
#pragma unroll
  for (int ww = 1; ww < 4; ++ww) { tr4[2 * ww - 2] = *(LAS f32x4*)(rr + M_RB + ww * 9216); tr4[2 * ww - 1] = *(LAS f32x4*)(rr + M_RB + ww * 9216 + 16); }
#pragma unroll
  for (int ww = 0; ww < 3; ++ww) { fa[1][0] += tr4[2 * ww]; fa[1][1] += tr4[2 * ww + 1]; }
  finish(IC<1>{});
  if (layer == 0) {
    BAR();
    if (w == 0) {
      LAS float* sp = (LAS float*)(L + M_S2 + lane * 4);
      const float t = (sp[0] + sp[64]) + (sp[128] + sp[192]), u = dppf<0xB1>(t);
      __builtin_amdgcn_raw_buffer_store_b32(nosent((lane & 1) ? pk(u, t) : pk(t, u)), rs, h * 128 + (r & 30) * 2, OFF_ST + nb * 4096 + pt * 64, SC1);
    }
  }
  SB();
}

constexpr int CODE_BYTES = 40 * 1024;
constexpr int P_XL = 0, P_GL = 33280, P_DL = P_GL + 65536, P_ZB = P_DL + 33280, P_WB = P_ZB + 4 * 2560;
static_assert(P_WB + 64 * 144 <= M_C2 + 2 * 4608, "fits");

constexpr int FA_KS = 21;
struct Late { v4u d[2], t[2]; int vo, so; rsrc_t rs; };
DI void prep(LAS unsigned char* L, const Params& p, rsrc_t rs, const v4u& srd, unsigned long long& tag, unsigned touch0, Late& late, int w) {
  const unsigned Lb = (unsigned)(size_t)L;
  const int lane = lane_id(), lane16 = lane * 16;
  const int b = blockIdx.x, xcd = b & 7, q = (b >> 3) & 3, tgi = b >> 5;
  const int t1 = xcd * 8 + tgi, tg = xcd * 7 + tgi;
  const bool heavy = tgi < 7;
  const int up = tg >= 28, cb = tg - 28 * up;
  const int h = lane >> 5, r = lane & 31, ft = 4 * q + w;
  int qa[4];
#pragma unroll
  for (int i = 0; i < 4; ++i) qa[i] = (i + q) & 3;
  PST(0);
  const v4u sx = make_srd(p.x, 2048u * 1024u);
  const int tok0 = token_of(32 * t1 + 2 * w);
  auto xin = [&](auto I) {
    glds_in<(I.v & 1) * 32768, (I.v & 1) * 1040>(sx, lane16, (tok0 + qa[I.v >> 1]) * 1024, Lb + P_XL + (8 * qa[I.v >> 1] + 2 * w) * 1040);
  };
  xin(IC<0>{}); xin(IC<1>{});
  SB();
  const unsigned hm = opaque((tgi - 7) >> 31);
  v4u sd = make_srd(p.wd, 1792u * 1024u & hm), sg = make_srd(up ? p.wu : p.wg, 512u * 3584u & hm);
  v4u sr = make_srd(p.wr, 2048u * 514u), si = make_srd(p.wi, 2048u * 514u);
  asm volatile("" : "+s"(sg));
  const int feat = 32 * ft + r;
  const float step = (feat == 0 ? 0.f : feat == 1 ? 256.f : (float)(feat >> 1)) * (1.f / 512.f);
  const float ph = feat < 2 ? 0.125f : (feat & 1) ? 0.25f : 0.f;
  const float a0 = ph + step * (float)(8 * h);
  const unsigned sgn = (feat > 1 && (feat & 2)) ? 0x80008000u : 0u;
  v4u sv = {SENT, SENT, SENT, SENT};
  int fo = w * 1024 + lane16 + OFF_H + b * 28672;
  pin(sv);
  auto fill = [&](int i) { bstore_sc1(sv, rs, fo, i * 4096); };
  int gvo = (lane >> 3) * 3584 + (((lane & 7) ^ ((lane >> 2) & 4)) * 16);
  int gso[4];
  unsigned gld[4];
#pragma unroll
  for (int i = 0; i < 4; ++i) { gso[i] = (16 * qa[i] + 4 * w) * 8 * 3584 + cb * 128; gld[i] = Lb + P_GL + (16 * qa[i] + 4 * w) * 1024; }
  auto gu = [&](auto I) { glds_in<(I.v & 3) * 8 * 3584, (I.v & 3) * 1024>(sg, gvo, gso[I.v >> 2], gld[I.v >> 2]); };
  const int fcol = 64 * q + lane;
  const int fvo = (fcol == 0 ? 256 : fcol) * 2, fso = (32 * t1 + 8 * w) * 514;
  unsigned fr[8], fi[8];
  v4u F[32];
  PSK(13);
  {
    float c0[8], c1[8];
    const float tw = 2.f * __builtin_amdgcn_cosf(16.f * step);
    sfor<0, 16>([&](auto KS) {
      constexpr int ks = KS.v;
      float (&c)[8] = (ks & 1) ? c1 : c0;
      float (&o)[8] = (ks & 1) ? c0 : c1;
#pragma unroll
      for (int j = 0; j < 8; ++j) {
        if constexpr (ks < 2) c[j] = __builtin_amdgcn_cosf(__builtin_fmaf(step, (float)(16 * ks + j), a0));
        else asm volatile("v_fma_f32 %0, %1, %2, -%0" : "+v"(c[j]) : "v"(tw), "v"(o[j]));
      }
      F[ks] = v4u{pk(c[0], c[1]), pk(c[2], c[3]), pk(c[4], c[5]), pk(c[6], c[7])};
      F[ks + 16] = F[ks] ^ sgn;
      pin(F[ks]); pin(F[ks + 16]);
      SB();
      if constexpr (ks < 3) { xin(IC<2 + 2 * ks>{}); xin(IC<3 + 2 * ks>{}); }
      if constexpr (ks >= 4 && !(ks & 1)) gu(IC<ks / 2 - 2>{});
      if constexpr (ks >= 8) ld2u16<(ks - 8) * 514>(fr[ks - 8], fi[ks - 8], fvo, sr, si, fso);
      if constexpr (ks == 3 || ks == 9 || ks == 15) fill(ks / 6);
      SB();
    });
  }
  PSK(14);
  v4u I[2];
#pragma unroll
  for (int s = 0; s < 2; ++s) {
    const int e = r - 16 * s - 8 * h;
    const unsigned one = (e & 1) ? 0x3F800000u : 0x00003F80u;
    I[s] = v4u{(e >> 1) == 0 ? one : 0u, (e >> 1) == 1 ? one : 0u, (e >> 1) == 2 ? one : 0u, (e >> 1) == 3 ? one : 0u};
  }
  pin(I[0]); pin(I[1]);
  if (!heavy) {
#pragma unroll
    for (int i = 0; i < 2; ++i) {
      const int dt = 2 * xcd + i;
      const float b0 = __builtin_amdgcn_fractf(__builtin_fmaf(step, (float)(32 * dt), a0));
      f32x16 aI;
      v4u c0 = cosfrag(b0, step, 0), c1 = cosfrag(b0, step, 16);
      m16_ready(c0, c1);
      mfma_vv<true>(aI, c0, I[0]);
      mfma_vv<false>(aI, c1, I[1]);
      asm volatile("s_nop 12" : "+v"(aI));
#pragma unroll
      for (int s = 0; s < 2; ++s) bstore_sc1(pack8(aI, s), rs, lane16, OFF_W1 + (dt * 8 + 7) * 32768 + (2 * ft + s) * 1024);
    }
  }
  const rsrc_t rsh = __builtin_amdgcn_make_buffer_rsrc((void*)p.ws, 0, WS_BYTES & hm, 0x00020000);
  int dso[4];
  unsigned dld[4];
#pragma unroll
  for (int i = 0; i < 4; ++i) {
    dso[i] = (32 * tg + 8 * qa[i] + 2 * w) * 1024; dld[i] = Lb + P_DL + (8 * qa[i] + 2 * w) * 1040;
    asm volatile("" : "+s"(dso[i]), "+s"(dld[i]));
  }
  const int srow = 8 * q + 2 * w + h, q4 = (lane & 15) >> 2;
  LAS unsigned char* xa = L + P_XL + r * 1040 + h * 16;
  LAS unsigned char* xs = L + P_XL + srow * 1040 + r * 32;
  int ro = stat_of(32 * t1 + 8 * q + 2 * w) + 4 * h + OFF_R0;
  asm volatile("" : "+v"(xa), "+v"(xs), "+v"(ro));
  LAS unsigned char *zbw, *zbr, *wbw, *wbr, *ba, *bb;
  int zo0, zo1, wo0, wo1, o0, o1;
  unsigned keep;
  auto addr = [&](auto STEP) {
    constexpr int n = STEP.v;
    if constexpr (n == 0) { ba = L + P_GL + (8 * h + q4) * 128 + (4 * (q4 >> 1) + 2 * ((lane >> 4) & 1) + ((lane & 3) >> 1)) * 16 + 8 * (lane & 1); asm volatile("" : "+v"(ba)); }
    if constexpr (n == 1) {
      bb = (LAS unsigned char*)((unsigned)(size_t)ba ^ 64u);
      zbw = L + P_ZB + w * 2560 + r * 80 + h * 16; asm volatile("" : "+v"(bb), "+v"(zbw));
    }
    if constexpr (n == 2) { zbr = L + P_ZB + w * 2560 + (lane >> 2) * 80 + (lane & 3) * 16; wbw = L + P_WB + lane * 144 + w * 32; asm volatile("" : "+v"(zbr), "+v"(wbw)); }
    if constexpr (n == 3) { wbr = L + P_WB + (16 * w + (lane >> 3)) * 144 + (lane & 7) * 16; asm volatile("" : "+v"(wbr)); }
    if constexpr (n == 4) { zo0 = (32 * ft + (lane >> 2)) * ZS + (lane & 3) * 16 + OFF_ZA + t1 * 64; zo1 = zo0 + 16 * ZS; asm volatile("" : "+v"(zo0), "+v"(zo1)); }
    if constexpr (n == 5) { wo0 = (2 * (64 * q + 16 * w + (lane >> 3)) + (t1 >> 5)) * ZS + (lane & 7) * 16 + OFF_ZB + (t1 & 31) * 128; wo1 = wo0 + 16 * ZS; asm volatile("" : "+v"(wo0), "+v"(wo1)); }
    if constexpr (n == 6) { keep = ft == 0 && h == 0 ? 0u : ~0u; pinu(keep); }
    if constexpr (n == 7) {
      auto tile = [&](int g) { return (g + g / 7) * 32768 + 256 * up + OFF_W1 + 2 * ft * 1024; };
      const int slot = ((r & 15) + 32 * h) * 16;
      o0 = (r < 16 ? tile(4 * cb) : tile(4 * cb + 1)) + slot; o1 = (r < 16 ? tile(4 * cb + 2) : tile(4 * cb + 3)) + slot;
      asm volatile("" : "+v"(o0), "+v"(o1));
    }
    if constexpr (n == 8) {
      tag += 0x9E3779B97F4A7C15ull;
      tag = (tag ^ (tag >> 30)) * 0xBF58476D1CE4E5B9ull;
      pins(tag);
    }
    if constexpr (n == 9) {
      tag = (tag ^ (tag >> 27)) * 0x94D049BB133111EBull;
      tag ^= tag >> 31;
      pins(tag);
    }
  };
  f32x16 aZ;
  unsigned zd[8];
  v4u zo[2], su[2], wo[2];
  float sv_[16], ssum;
  auto z_out = [&](auto STEP) {
    constexpr int n = STEP.v;
    if constexpr (n < 4) { pk_tag(aZ[4 * n], aZ[4 * n + 1], aZ[4 * n + 2], aZ[4 * n + 3], 0u, zd[2 * n], zd[2 * n + 1]); pinu(zd[2 * n]); pinu(zd[2 * n + 1]); }
    if constexpr (n == 4 || n == 5) {
      auto r0 = __builtin_amdgcn_permlane32_swap(zd[4 * (n - 4)], zd[4 * (n - 4) + 2], false, false);
      auto r1 = __builtin_amdgcn_permlane32_swap(zd[4 * (n - 4) + 1], zd[4 * (n - 4) + 3], false, false);
      *(LAS v4u*)(zbw + 32 * (n - 4)) = v4u{r0[0], r1[0], r0[1], r1[1]};
    }
    if constexpr (n == 6) { zo[0] = *(LAS v4u*)zbr; zo[1] = *(LAS v4u*)(zbr + 16 * 80); }
    if constexpr (n == 7) bstore_sc1(zo[0], rs, zo0, 0);
    if constexpr (n == 8) bstore_sc1(zo[1], rs, zo1, 0);
  };
  auto stat = [&](auto STEP) {
    constexpr int n = STEP.v;
    if constexpr (n == 0) { su[0] = *(LAS v4u*)xs; su[1] = *(LAS v4u*)(xs + 16); ssum = 0.f; }
    if constexpr (n >= 1 && n <= 8) {
      sv_[2 * n - 2] = blo(su[(n - 1) >> 2][(n - 1) & 3]); sv_[2 * n - 1] = bhi(su[(n - 1) >> 2][(n - 1) & 3]);
      ssum += sv_[2 * n - 2]; pinf(ssum); ssum += sv_[2 * n - 1];
    }
    if constexpr (n == 9) { ssum += dppf<0xB1>(ssum); ssum += dppf<0x4E>(ssum); }
    if constexpr (n == 10) { ssum += dppf<0x141>(ssum); ssum += dppf<0x140>(ssum); }
    if constexpr (n == 11) { auto x = __builtin_amdgcn_permlane16_swap(asu(ssum), asu(ssum), false, false); ssum = (asf(x[0]) + asf(x[1])) * (1.f / 512.f); }
    if constexpr (n >= 12 && n <= 15) {
#pragma unroll
      for (int i = 4 * (n - 12); i < 4 * (n - 12) + 4; ++i) { sv_[i] -= ssum; pinf(sv_[i]); }
    }
    if constexpr (n == 16) ssum = 0.f;
    if constexpr (n >= 16 && n <= 19) {
#pragma unroll
      for (int i = 4 * (n - 16); i < 4 * (n - 16) + 4; ++i) sumsq1(ssum, sv_[i]);
    }
    if constexpr (n == 20) { ssum += dppf<0xB1>(ssum); ssum += dppf<0x4E>(ssum); }
    if constexpr (n == 21) { ssum += dppf<0x141>(ssum); ssum += dppf<0x140>(ssum); }
    if constexpr (n == 22) { auto x = __builtin_amdgcn_permlane16_swap(asu(ssum), asu(ssum), false, false); ssum = asf(x[0]) + asf(x[1]); }
    if constexpr (n == 23) __builtin_amdgcn_raw_buffer_store_b32(asu(ssum), rs, ro, 0, SC1);
    pinf(ssum);
  };
  auto f_out = [&](auto N, const f32x16& a, v4u& o, int vo) {
    constexpr int k = 8 * (N.v >> 1) + 4 * (N.v & 1);
    o[2 * (N.v & 1)] = pk(N.v == 0 ? asf(asu(a[0]) & keep) : a[k], a[k + 1]); o[2 * (N.v & 1) + 1] = pk(a[k + 2], a[k + 3]);
    if constexpr (N.v & 1) bstore_sc1(o, rsh, vo, (N.v >> 1) * 1024); else pin(o);
  };
  auto filt = [&](auto STEP) {
    constexpr int n = STEP.v;
    if constexpr (n < 2) {
      asm volatile("" : "+v"(fr[4 * n]), "+v"(fr[4 * n + 1]), "+v"(fr[4 * n + 2]), "+v"(fr[4 * n + 3]), "+v"(fi[4 * n]), "+v"(fi[4 * n + 1]), "+v"(fi[4 * n + 2]), "+v"(fi[4 * n + 3]));
      v4u o;
#pragma unroll
      for (int i = 0; i < 4; ++i) o[i] = (fr[4 * n + i] & ~1u) | (fi[4 * n + i] << 16);
      *(LAS v4u*)(wbw + 16 * n) = o;
    }
    if constexpr (n == 2) { wo[0] = *(LAS v4u*)wbr; wo[1] = *(LAS v4u*)(wbr + 8 * 144); }
    if constexpr (n == 3) bstore_sc1(wo[0], rs, wo0, 0);
    if constexpr (n == 4) bstore_sc1(wo[1], rs, wo1, 0);
  };
  PSK(12);
  fill(3);
  PST(1);
  vmwait<22>();
  BAR();
  PST(2);
  {
    v4u a[4];
#pragma unroll
    for (int i = 0; i < 4; ++i) a[i] = *(LAS v4u*)(xa + i * 32);
    sfor<0, 32>([&](auto KS) {
      constexpr int ks = KS.v;
      mfma_vv<ks == 0>(aZ, a[ks & 3], F[ks]);
      SB();
      if constexpr (ks + 4 < 32) a[ks & 3] = *(LAS v4u*)(xa + (ks + 4) * 32);
      if constexpr (ks < 20 && !(ks & 1)) gu(IC<6 + ks / 2>{});
      if constexpr (ks >= 20 && ks < 28) glds_in<(ks & 1) * 1024, (ks & 1) * 1040>(sd, lane16, dso[(ks - 20) >> 1], dld[(ks - 20) >> 1]);
      if constexpr (ks == 0 || (ks >= 2 && ks < 25)) stat(IC<(ks == 0 ? 0 : ks - 1)>{});
      if constexpr (ks >= 24) addr(IC<ks - 24>{});
      if constexpr (ks == 28 || ks == 29) { if constexpr (ks == 28) vmwait<18>(); filt(IC<ks - 28>{}); }
      if constexpr (ks == 8 || ks == 18 || ks == 30) fill(4 + ks / 12);
      SB();
    });
  }
  asm volatile("s_nop 4\n\ts_waitcnt vmcnt(8)" : "+v"(aZ) : "v"(touch0) : "memory");
  PST(3);
  BAR();
  PST(4);
  f32x16 aG0, aG1;
  {
    v4u b0[2], b1[2];
#pragma unroll
    for (int i = 0; i < 2; ++i) { b0[i] = tr2(ba + i * 2048, 512); b1[i] = tr2(bb + i * 2048, 512); }
    sfor<0, 32>([&](auto KS) {
      constexpr int ks = KS.v;
      if constexpr (ks == 8 || ks == 16 || ks == 24) PSK(8 + ks / 8);
      mfma_vv<ks == 0>(aG0, F[ks], b0[ks & 1]);
      SB();
      if constexpr (ks < 9) z_out(KS);
      if constexpr (ks >= 9 && ks < 11) filt(IC<ks - 6>{});
      if constexpr (ks == 11 || ks == 12) addr(IC<ks - 3>{});
      if constexpr (ks == FA_KS + 1) publish(rs, 0, b, tag, w, lane);
      SB();
      mfma_vv<ks == 0>(aG1, F[ks], b1[ks & 1]);
      SB();
      if constexpr (ks + 2 < 32) { b1[ks & 1] = tr2(bb + (ks + 2) * 2048, 512); SB(); b0[ks & 1] = tr2(ba + (ks + 2) * 2048, 512); }
      if constexpr (ks == 6) filt(IC<2>{});
      if constexpr (ks == FA_KS) { vmwait<0>(); asm volatile("s_barrier" ::: "memory"); }
      SB();
    });
  }
  PST(5);
  f32x16 aD, aT;
  v4u fl[2];
  {
    v4u fo_ = {0u, 0u, 0u, 0u};
    LAS unsigned char* da = xa + (P_DL - P_XL);
    const v4u t0 = *(LAS v4u*)(da + ft * 64), t1_ = *(LAS v4u*)(da + ft * 64 + 32);
    v4u a[4];
#pragma unroll
    for (int i = 0; i < 4; ++i) a[i] = *(LAS v4u*)(da + i * 32);
    mfma_vv<true>(aT, t0, I[0]);
    mfma_vv<false>(aT, t1_, I[1]);
    sfor<0, 32>([&](auto KS) {
      constexpr int ks = KS.v;
      mfma_vv<ks == 0>(aD, a[ks & 3], F[ks]);
      SB();
      if constexpr (ks + 4 < 32) a[ks & 3] = *(LAS v4u*)(da + (ks + 4) * 32);
      if constexpr (ks < 4) f_out(KS, aG0, fo_, o0);
      if constexpr (ks >= 4 && ks < 8) f_out(IC<ks - 4>{}, aG1, fo_, o1);
      if constexpr (ks == 10 || ks == 12) { late.t[ks == 12] = pack8(aT, ks == 12); pin(late.t[ks == 12]); }
      if constexpr (ks == 20) flags_load(fl, srd, 0, b, lane);
      SB();
    });
  }
  PST(6);
  asm volatile("s_nop 12\n\ts_waitcnt vmcnt(0)\n\ts_barrier" : "+v"(aD) :: "memory");
  publish(rs, 1, b, tag, w, lane);
  late.d[0] = pack8(aD, 0); late.d[1] = pack8(aD, 1);
  late.vo = lane16; late.so = (ft * 112 + 2 * tg) * 1024; late.rs = rsh;
  PST(7);
  flags_wait(fl, srd, 0, b, lane, tag);
  PST(8);
}

struct Tile {
  int rb, fs, pi, nb;
  DI Tile(int c) { rb = 4 * (c & 3) + (c >> 6); fs = 8 * ((c >> 2) & 1) + ((c >> 3) & 7); pi = fs >> 3; nb = fs & 7; }
};

template <int layer>
DI void column(LAS unsigned char* L, rsrc_t rs, const v4u& srd, v4u (&W)[64], unsigned long long tag, const Late& late, int w0) {
  const int c = opaque(blockIdx.x), w = opaque(w0);
  const int lane = lane_id(), tid = 64 * w + lane;
  WSrc ws;
  ws.srd = srd; ws.srd1 = srd;
  if (w == 3 && layer == 0) ws.srd1.z = 0u;
  ws.lane16 = lane * 16; ws.base = OFF_W1 + (Tile(c).fs * 8 + 2 * w) * 32768;
  unsigned wf[8];
  const int wfvo = (16 * w + (lane & 15) + 64 * 4 * (lane >> 4)) * 4;
  auto filter_in = [&]() {
    asm volatile("buffer_load_dword %0, %8, %9, %10 offen sc1\n\tbuffer_load_dword %1, %8, %9, %10 offen offset:256 sc1\n\t"
                 "buffer_load_dword %2, %8, %9, %10 offen offset:512 sc1\n\tbuffer_load_dword %3, %8, %9, %10 offen offset:768 sc1\n\t"
                 "buffer_load_dword %4, %8, %9, %11 offen sc1\n\tbuffer_load_dword %5, %8, %9, %11 offen offset:256 sc1\n\t"
                 "buffer_load_dword %6, %8, %9, %11 offen offset:512 sc1\n\tbuffer_load_dword %7, %8, %9, %11 offen offset:768 sc1"
                 : "=&v"(wf[0]), "=&v"(wf[1]), "=&v"(wf[2]), "=&v"(wf[3]), "=&v"(wf[4]), "=&v"(wf[5]), "=&v"(wf[6]), "=&v"(wf[7])
                 : "v"(wfvo), "s"(srd), "s"(OFF_ZB + 2 * c * ZS), "s"(OFF_ZB + (2 * c + 1) * ZS));
  };
  float rstd[8], fr[8], fi[8];
  auto filter_unpack = [&]() {
    asm volatile("" : "+v"(wf[0]), "+v"(wf[1]), "+v"(wf[2]), "+v"(wf[3]), "+v"(wf[4]), "+v"(wf[5]), "+v"(wf[6]), "+v"(wf[7]));
#pragma unroll
    for (int e = 0; e < 8; ++e) { fr[e] = blo(wf[e]) * (1.f / 2048.f); fi[e] = bhi(wf[e]) * (1.f / 2048.f); pinf(fr[e]); pinf(fi[e]); }
  };
  STAMP(0);
  CSTAMP(22);
  v4u zr, zi, sa, sb;
  const int first = is_zero(c);
  ColConst k;
  if constexpr (layer == 0) {
    v4u fl[2];
    ldx4<0>(zr, srd, tid * 16, OFF_ZA + (2 * c + first) * ZS); ldx4<0>(zi, srd, tid * 16, OFF_ZA + (2 * c + 1 - first) * ZS);
    ldx4<0>(sa, srd, tid * 16, OFF_R0); ldx4<0>(sb, srd, tid * 16, OFF_R0 + 4096);
    filter_in();
    SB();
#pragma unroll
    for (int i = 0; i < 2; ++i) {
      bstore_sc1(late.d[i], late.rs, late.vo, OFF_W2 + late.so + i * 1024);
      bstore_sc1(late.t[i], late.rs, late.vo, OFF_W3 + late.so + i * 1024);
    }
    if (tid < 8) bstore_sc1(v4u{SENT, SENT, SENT, SENT}, rs, tid * 16, OFF_ST + c * 128);
    if (c == 0 && tid == 0) __builtin_amdgcn_raw_buffer_store_b64(v2u{(unsigned)tag, (unsigned)(tag >> 32)}, rs, OFF_CTL, 0, SC1);
    SB();
    col_consts(k, w, lane, [&]() {});
    SB();
    flags_load(fl, srd, 1, c, lane);
    STAMP(1);
    vmwait<2>();
    pin(zr); pin(zi); pin(sa); pin(sb);
    filter_unpack();
    vmwait<0>();
    flags_wait(fl, srd, 1, c, lane, tag);
  } else {
    filter_in();
    SB();
    v4u st[8];
    auto col_in = [&]() {
      ldx4<0>(zr, srd, tid * 16, OFF_ZA + (2 * c + first) * ZS); ldx4<0>(zi, srd, tid * 16, OFF_ZA + (2 * c + 1 - first) * ZS);
      sfor<0, 8>([&](auto I) { ldx4<0>(st[I.v], srd, tid * 16, OFF_ST + I.v * 4096); });
    };
    col_consts(k, w, lane, [&]() { __builtin_amdgcn_s_sleep(DELAY_C); col_in(); });
    SB();
    load_ws<0, 4>(W, ws);
    vmwait<14>();
    filter_unpack();
    STAMP(1);
    vmwait<4>();
    auto missing = [&]() {
      pin(zr); pin(zi);
#pragma unroll
      for (int i = 0; i < 8; ++i) pin(st[i]);
      float a[8];
      unsigned sel[2] = {0x00003F80u, 0x3F800000u};
      pinu(sel[0]); pinu(sel[1]);
#pragma unroll
      for (int e = 0; e < 8; ++e) {
        a[e] = 0.f;
#pragma unroll
        for (int n = 0; n < 8; ++n) a[e] = __builtin_amdgcn_fdot2_f32_bf16(asb2(st[n][e >> 1]), asb2(sel[e & 1]), a[e], false);
      }
      sa = v4u{asu(a[0]), asu(a[1]), asu(a[2]), asu(a[3])}; sb = v4u{asu(a[4]), asu(a[5]), asu(a[6]), asu(a[7])};
      const float u = (a[0] + a[2]) + (a[4] + a[6]);
      unsigned m = 0u;
      if (__builtin_expect(__builtin_amdgcn_ballot_w64(u != u) != 0ull, 0)) {
#pragma unroll
        for (int i = 0; i < 8; ++i) m = mxacc(m, st[i]);
      }
      return (__builtin_amdgcn_ballot_w64(m == SENT) | bit0_set(tagacc(tagacc(0u, zr, 0u), zi, 0u))) != 0ull;
    };
    if (__builtin_expect(missing(), 0)) {
      unsigned spin = 0;
      do { RETRY(28); col_in(); vmwait<0>(); } while (missing() && ++spin < SPIN_MAX);
    }
  }
  constexpr int W0 = 4;
  if constexpr (layer == 0) load_ws<0, 4>(W, ws);
  load_ws<W0, W0 + 2>(W, ws);
#pragma unroll
  for (int e = 0; e < 4; ++e) {
    rstd[e] = __builtin_amdgcn_rsqf(__builtin_fmaf(asf(sa[e]), layer == 0 ? 1.f / 512.f : 1.f / 131072.f, 1e-5f));
    rstd[4 + e] = __builtin_amdgcn_rsqf(__builtin_fmaf(asf(sb[e]), layer == 0 ? 1.f / 512.f : 1.f / 131072.f, 1e-5f));
  }
  load_ws<W0 + 2, W0 + 4>(W, ws);
  STAMP(2);
  col_compute<W0 + 4>(L, rs, k, fr, fi, zr, zi, rstd, c, w, lane, layer == 0 ? OFF_ZA : OFF_ZB, W, ws);
  STAMP(3);
  CSTAMP(23);
  load_ws<W0 + 34, NW_COL>(W, ws);
  __builtin_amdgcn_s_sleep(DELAY_B);
}

#define KA_EARLY
#define KA_LATE
typedef unsigned v16u __attribute__((ext_vector_type(16)));
__global__ void __launch_bounds__(256, 1) k_main(Params) {
  extern __shared__ __attribute__((aligned(16))) unsigned char smem[];
  LAS unsigned char* L = (LAS unsigned char*)smem;
  v16u ka;
  asm volatile("s_load_dwordx16 %0, %1, 0x0" : "=s"(ka) : "s"(__builtin_amdgcn_kernarg_segment_ptr()));
  unsigned touch0;
  {
    unsigned lo, hi;
    asm volatile("s_getpc_b64 s[20:21]\n\ts_add_u32 %0, s20, _Z6k_main6Params@rel32@lo+4\n\ts_addc_u32 %1, s21, _Z6k_main6Params@rel32@hi+12"
                 : "=s"(lo), "=s"(hi) :: "s20", "s21", "scc");
    const int off = threadIdx.x * 128 + (threadIdx.x >= 128 ? (blockIdx.x & 8) * 2048 : 0);
    asm volatile("global_load_dword %0, %1, %2" : "=v"(touch0) : "v"(off < CODE_BYTES ? off : 0), "s"(((unsigned long long)hi << 32) | lo));
    SB();
  }
  KA_EARLY
  asm volatile("s_waitcnt lgkmcnt(0)" : "+s"(ka));
  KA_LATE
  Params p;
  auto ptr = [&](int i) { return ((unsigned long long)ka[2 * i + 1] << 32) | ka[2 * i]; };
  p.x = (const u16*)ptr(0); p.wr = (const u16*)ptr(1); p.wi = (const u16*)ptr(2); p.wg = (const u16*)ptr(3); p.wu = (const u16*)ptr(4);
  p.wd = (const u16*)ptr(5); p.out = (u16*)ptr(6); p.ws = (unsigned char*)ptr(7);
  const rsrc_t rs = __builtin_amdgcn_make_buffer_rsrc((void*)p.ws, 0, WS_BYTES, 0x00020000);
  const v4u srd = make_srd(p.ws, WS_BYTES);
  unsigned long long tag = *(const __attribute__((address_space(4))) unsigned long long*)(p.ws + OFF_CTL);
  const int w0 = __builtin_amdgcn_readfirstlane(threadIdx.x >> 6);
  Late late;
  prep(L, p, rs, srd, tag, touch0, late, w0);
  v4u W[64];
  column<0>(L, rs, srd, W, tag, late, w0);
#pragma unroll 1
  for (int layer = 0; layer < 2; ++layer) {
    const int c = opaque(blockIdx.x), w = opaque(w0), lane = lane_id();
    const Tile t(c);
    WSrc ws;
    ws.srd = srd; ws.srd1 = srd;
    if (w == 3 && layer == 0) ws.srd1.z = 0u;
    ws.lane16 = lane * 16; ws.base = OFF_W1 + (t.fs * 8 + 2 * w) * 32768;
    ffn(L, rs, ws, p, W, layer, c, t.rb, t.fs, t.pi, t.nb, w, lane);
    STAMP(18);
    if (layer == 0) column<1>(L, rs, srd, W, tag, late, w0);
  }
}

extern "C" void kernel_launch(void* const* d_in, const int* in_sizes, int n_in, void* d_out, int out_size, void* d_ws, size_t ws_size,
                              hipStream_t stream) {
  Params p;
  p.x = (const u16*)d_in[0]; p.wr = (const u16*)d_in[1]; p.wi = (const u16*)d_in[2];
  p.wg = (const u16*)d_in[4]; p.wu = (const u16*)d_in[5]; p.wd = (const u16*)d_in[6];
  p.out = (u16*)d_out; p.ws = (unsigned char*)d_ws;
  (void)hipFuncSetAttribute((const void*)k_main, hipFuncAttributeMaxDynamicSharedMemorySize, M_LDS);
  k_main<<<256, 256, M_LDS, stream>>>(p);
}
```
